```python
import torch

def _softmax(x, axis=-1):
    return torch.softmax(x, dim=axis)

def _gelu(x):
    return torch.nn.functional.gelu(x, approximate='tanh')

def _silu(x):
    return torch.nn.functional.silu(x)

def _sigmoid(x):
    return torch.sigmoid(x)

def _layernorm(x, axis=-1, eps=1e-05):
    m = x.mean(dim=axis, keepdim=True)
    v = x.var(dim=axis, keepdim=True, unbiased=False)
    return (x - m) / torch.sqrt(v + eps)

def _scatter_add(tgt, idx, src, axis=0):
    return tgt.clone().index_add_(axis, idx, src)

def _segment_sum(src, seg, n):
    out = torch.zeros((n,) + src.shape[1:], dtype=src.dtype)
    return out.index_add_(0, seg, src)

def _pad(x, pw):
    flat = tuple((v for pair in reversed(pw) for v in pair))
    return torch.nn.functional.pad(x, flat)
kernel_name = 'interleave_layers__memory_aug_recurrent_mem_tr__softmax_full_mha__prompt'

def setup_inputs():

    _w = lambda a: (lambda t: t.to(torch.bfloat16) if t.is_floating_point() else t)(torch.as_tensor(a))

    def _A_setup():
        B, S, Dm = (1, 2048, 640)
        H, Dh = (32, 192)
        M, seg = (8, 32)
        seg = min(seg, S)
        S = S // seg * seg
        return {'x': _w((lambda *s: torch.randn(*s, dtype=torch.bfloat16) * 0.05)(B, S, Dm)), 'mem0': _w((lambda *s: torch.randn(*s, dtype=torch.bfloat16) * 0.8)(B, M, Dm)), 'Wq': _w((lambda *s: torch.randn(*s, dtype=torch.bfloat16) * 0.8)(Dm, H, Dh) * Dm ** (-0.5)), 'Wk': _w((lambda *s: torch.randn(*s, dtype=torch.bfloat16) * 0.8)(Dm, H, Dh) * Dm ** (-0.5)), 'Wv': _w((lambda *s: torch.randn(*s, dtype=torch.bfloat16) * 0.8)(Dm, H, Dh) * Dm ** (-0.5)), 'Wo': _w((lambda *s: torch.randn(*s, dtype=torch.bfloat16) * 0.8)(H, Dh, Dm) * (H * Dh) ** (-0.5)), 'seg_len': seg}
    _A_in = _A_setup()

    def _B_setup():
        B, S, D = (1, 2048, 640)
        return {'x': _w((lambda *s: torch.randn(*s, dtype=torch.bfloat16) * 0.05)(B, S, D)), 'wqkv': _w((lambda *s: torch.randn(*s, dtype=torch.bfloat16) * 0.05)(D, 3 * D)), 'wo': _w((lambda *s: torch.randn(*s, dtype=torch.bfloat16) * 0.05)(D, D))}
    _B_in = _B_setup()
    return {'A_x': _A_in['x'], 'A_mem0': _A_in['mem0'], 'A_Wq': _A_in['Wq'], 'A_Wk': _A_in['Wk'], 'A_Wv': _A_in['Wv'], 'A_Wo': _A_in['Wo'], 'A_seg_len': _A_in['seg_len'], 'B_x': _B_in['x'], 'B_wqkv': _B_in['wqkv'], 'B_wo': _B_in['wo']}

def reference(A_x, A_mem0, A_Wq, A_Wk, A_Wv, A_Wo, A_seg_len, B_x, B_wqkv, B_wo):

    def _A_ref(x, mem0, Wq, Wk, Wv, Wo, seg_len):
        B, S, Dm = x.shape
        H, Dh = (Wq.shape[1], Wq.shape[2])
        M = mem0.shape[1]
        scale = Dh ** (-0.5)
        n_seg = S // seg_len
        L = M + seg_len
        idx = torch.arange(L)
        mask = (idx[:, None] < M) | (idx[:, None] >= idx[None, :])
        mem = mem0
        outs = []
        for s in range(n_seg):
            xs = x[:, s * seg_len:(s + 1) * seg_len, :].clone()
            h = torch.concatenate([mem, xs], axis=1)
            q = torch.einsum('btd,dhe->bhte', h, Wq)
            k = torch.einsum('btd,dhe->bhte', h, Wk)
            v = torch.einsum('btd,dhe->bhte', h, Wv)
            scores = torch.einsum('bhqe,bhke->bhqk', q, k) * scale
            scores = torch.where(mask[None, None], scores, -1000000000.0)
            p = _softmax(scores, axis=-1)
            o = torch.einsum('bhqk,bhke->bhqe', p, v)
            o = torch.einsum('bhte,hed->btd', o, Wo)
            mem = o[:, :M, :].clone()
            outs.append(o[:, M:, :])
        return torch.concatenate(outs, axis=1)

    def _B_ref(x, wqkv, wo):
        B, S, D = x.shape
        H = 32
        Dh = D // H
        qkv = (lambda x, *d: x.permute(*d))((x @ wqkv).reshape(B, S, 3, H, Dh), 2, 0, 3, 1, 4)
        q, k, v = (qkv[0], qkv[1], qkv[2])
        scale = Dh ** (-0.5)
        scores = torch.einsum('bhqd,bhkd->bhqk', q, k) * scale
        p = _softmax(scores, axis=-1)
        o = torch.einsum('bhqk,bhkd->bhqd', p, v)
        return (lambda x, *d: x.permute(*d))(o, 0, 2, 1, 3).reshape(B, S, D) @ wo
    x = A_x
    x = x + _A_ref(_layernorm(x, axis=-1), A_mem0, A_Wq, A_Wk, A_Wv, A_Wo, A_seg_len)
    x = x + _B_ref(_layernorm(x, axis=-1), B_wqkv, B_wo)
    x = x + _A_ref(_layernorm(x, axis=-1), A_mem0, A_Wq, A_Wk, A_Wv, A_Wo, A_seg_len)
    x = x + _B_ref(_layernorm(x, axis=-1), B_wqkv, B_wo)
    return x
```

```cpp
#include <hip/hip_runtime.h>
#include <hip/hip_bf16.h>
#include <stdint.h>
#include <type_traits>
#include <utility>

typedef unsigned short u16;
typedef unsigned long long u64;
typedef __attribute__((ext_vector_type(8))) short bf16x8;
typedef __attribute__((ext_vector_type(4))) short bf16x4;
typedef __attribute__((ext_vector_type(4))) float f32x4;
typedef __attribute__((ext_vector_type(2))) float f32x2;
typedef __attribute__((ext_vector_type(16))) float f32x16;
typedef __attribute__((ext_vector_type(4))) unsigned u32x4;
typedef __attribute__((ext_vector_type(2))) unsigned u32x2;
#define GAS __attribute__((address_space(1)))
#define LAS __attribute__((address_space(3)))

constexpr int S_ = 2048, DM = 640;
constexpr int NH = 32, DH = 192, HD = NH * DH;
constexpr int MEM = 8, SEG = 32, NSEG = S_ / SEG;
constexpr int BH = 32, BD = 20;

constexpr size_t al256(size_t x) { return (x + 255) & ~(size_t)255; }
constexpr unsigned ROW = 32768;
constexpr unsigned M_SLOT = 4 * ROW;
constexpr unsigned OFS_P = 16 * ROW;
constexpr unsigned P_SLOT = 5 * ROW;
constexpr unsigned OFS_CAL = 26 * ROW;
constexpr unsigned OFS_CW = 28 * ROW;
constexpr unsigned CW_JP = 0;
constexpr unsigned CW_SS = CW_JP + 104 * 128;
constexpr unsigned CW_SO = CW_SS + 64 * 128;
constexpr unsigned CW_TQ = CW_SO + 512 * 128;
constexpr unsigned CW_END = CW_TQ + 8 * 128;
static_assert(CW_END <= 4 * ROW, "control words");
constexpr unsigned SYNC_LAYER = 32 * ROW;
constexpr size_t OFF_WF = 2 * (size_t)SYNC_LAYER;
constexpr size_t OFF_UF = OFF_WF + (size_t)768 * 20480;
constexpr size_t OFF_WQKVBT = OFF_UF + (size_t)80 * 16 * 20480;
constexpr size_t OFF_WOBT = OFF_WQKVBT + al256((size_t)3 * DM * DM * 2);
constexpr size_t OFF_XF = OFF_WOBT + al256((size_t)DM * DM * 2);
constexpr size_t OFF_XN = OFF_XF + al256((size_t)S_ * DM * 2);
constexpr size_t OFF_XA = OFF_XN + al256((size_t)S_ * DM * 2);
constexpr size_t OFF_XB = OFF_XA + al256((size_t)S_ * DM * 2);
constexpr unsigned QK_IMG = NSEG * NH * 12 * 1024;
constexpr size_t OFF_QK = OFF_XB + al256((size_t)S_ * DM * 2);
constexpr unsigned Y_IMG = NSEG * NH * 40 * 1024;
constexpr size_t OFF_Y = OFF_QK + 2 * (size_t)QK_IMG;
constexpr unsigned Z_IMG = NSEG * 40 * NH * 256;
constexpr size_t OFF_Z = OFF_Y + Y_IMG;
constexpr unsigned PT_M = NH * 2048, PT_SEG = PT_M + 2 * NH * 256, PT_IMG = NSEG * PT_SEG;
constexpr size_t OFF_PT = OFF_Z + Z_IMG;
constexpr size_t OFF_QB = OFF_PT + PT_IMG;
constexpr size_t OFF_KB = OFF_QB + al256((size_t)BH * S_ * 32 * 2);
constexpr size_t OFF_VBT = OFF_KB + al256((size_t)BH * S_ * 32 * 2);
constexpr size_t WS_TOTAL = OFF_VBT + al256((size_t)BH * S_ * 32 * 2);
constexpr size_t OFF_PART = OFF_Y;
constexpr size_t OFF_OB = OFF_QK;
static_assert(WS_TOTAL + ((size_t)2 << 20) <= ((size_t)256 << 20), "workspace budget");

__device__ __forceinline__ float u2f(unsigned v) { return __builtin_bit_cast(float, v); }
__device__ __forceinline__ unsigned f2u(float v) { return __builtin_bit_cast(unsigned, v); }
__device__ __forceinline__ float bf2f(unsigned v) { return __builtin_bit_cast(float, v << 16); }
__device__ __forceinline__ unsigned f2bf(float f) { __bf16 b = (__bf16)f; return (unsigned)__builtin_bit_cast(u16, b); }
__device__ __forceinline__ float rbf(float f) { return bf2f(f2bf(f)); }
typedef __attribute__((ext_vector_type(2))) __bf16 hbf16x2;
__device__ __forceinline__ unsigned pack2(float lo, float hi) { f32x2 v = {lo, hi}; hbf16x2 b = __builtin_convertvector(v, hbf16x2); return __builtin_bit_cast(unsigned, b); }
__device__ __forceinline__ f32x4 mfma32(bf16x8 a, bf16x8 b, f32x4 c) { return __builtin_amdgcn_mfma_f32_16x16x32_bf16(a, b, c, 0, 0, 0); }
__device__ __forceinline__ f32x4 mfma16(bf16x4 a, bf16x4 b, f32x4 c) { return __builtin_amdgcn_mfma_f32_16x16x16bf16_1k(a, b, c, 0, 0, 0); }
typedef __attribute__((ext_vector_type(8))) unsigned u32x8;
typedef __attribute__((ext_vector_type(8))) __bf16 hbf16x8;
typedef __attribute__((ext_vector_type(16))) __bf16 hbf16x16;
template <typename R>
__device__ __forceinline__ void store16_(u32x4 v, R r, unsigned vo, unsigned so, bool sc1) {
  if (sc1) __builtin_amdgcn_raw_buffer_store_b128(v, r, vo, so, 16); else __builtin_amdgcn_raw_buffer_store_b128(v, r, vo, so, 0);
  asm volatile("s_nop 1" :: "v"(v));
}
#define store16(v, r, vo, so, aux) store16_((v), (r), (vo), (so), (aux) != 0)
__device__ __forceinline__ bf16x4 mk4(unsigned lo, unsigned hi) { u32x2 v = {lo, hi}; return __builtin_bit_cast(bf16x4, v); }
__device__ __forceinline__ bf16x8 mk8(unsigned a, unsigned b, unsigned c, unsigned d) { u32x4 v = {a, b, c, d}; return __builtin_bit_cast(bf16x8, v); }
__device__ __forceinline__ unsigned dpp_ror8(unsigned v) { return (unsigned)__builtin_amdgcn_update_dpp(0, (int)v, 0x128, 0xf, 0xf, false); }

__device__ __forceinline__ void resln_rows(int blk, const u16* __restrict__ xold, const float* __restrict__ part, int nsplit,
                                           u16* __restrict__ xnew, u16* __restrict__ xn, int frag) {
  const int row = blk * 4 + (threadIdx.x >> 6), lane = threadIdx.x & 63;
  const bool two = lane < 16;
  float v[16];
#pragma unroll
  for (int hf = 0; hf < 2; ++hf) {
    if (hf == 1 && !two) {
#pragma unroll
      for (int i = 0; i < 8; ++i) v[8 + i] = 0.f;
      continue;
    }
    const int col = (hf * 64 + lane) * 8;
    u32x4 xo = *(const u32x4*)(xold + (size_t)row * DM + col);
    float a[8];
#pragma unroll
    for (int i = 0; i < 4; ++i) { unsigned u = xo[i]; a[2 * i] = bf2f(u & 0xffff); a[2 * i + 1] = bf2f(u >> 16); }
    if (nsplit > 0) {
      f32x4 s0 = {0.f, 0.f, 0.f, 0.f}, s1 = {0.f, 0.f, 0.f, 0.f};
      for (int s = 0; s < nsplit; ++s) {
        const float* pp = part + ((size_t)s * S_ + row) * DM + col;
        s0 += *(const f32x4*)pp; s1 += *(const f32x4*)(pp + 4);
      }
#pragma unroll
      for (int i = 0; i < 4; ++i) { a[i] = rbf(a[i] + rbf(s0[i])); a[4 + i] = rbf(a[4 + i] + rbf(s1[i])); }
      if (xnew) {
        u32x4 o = {pack2(a[0], a[1]), pack2(a[2], a[3]), pack2(a[4], a[5]), pack2(a[6], a[7])};
        *(u32x4*)(xnew + (size_t)row * DM + col) = o;
      }
    }
#pragma unroll
    for (int i = 0; i < 8; ++i) v[hf * 8 + i] = a[i];
  }
  if (!xn) return;
  float s = 0.f;
#pragma unroll
  for (int i = 0; i < 16; ++i) s += v[i];
#pragma unroll
  for (int o = 32; o >= 1; o >>= 1) s += __shfl_xor(s, o);
  const float mean = s * (1.0f / DM);
  float q = 0.f;
#pragma unroll
  for (int i = 0; i < 8; ++i) { float d = v[i] - mean; q += d * d; }
  if (two) {
#pragma unroll
    for (int i = 8; i < 16; ++i) { float d = v[i] - mean; q += d * d; }
  }
#pragma unroll
  for (int o = 32; o >= 1; o >>= 1) q += __shfl_xor(q, o);
  const float var = rbf(q * (1.0f / DM));
  const float mb = rbf(mean);
  const float den = rbf(sqrtf(rbf(var + 1e-5f)));
#pragma unroll
  for (int hf = 0; hf < 2; ++hf) {
    if (hf == 1 && !two) continue;
    const int ch = hf * 64 + lane;
    float y[8];
#pragma unroll
    for (int i = 0; i < 8; ++i) y[i] = rbf(v[hf * 8 + i] - mb) / den;
    u32x4 o = {pack2(y[0], y[1]), pack2(y[2], y[3]), pack2(y[4], y[5]), pack2(y[6], y[7])};
    if (frag) *(u32x4*)(xn + ((((size_t)(row >> 4)) * 20 + (ch >> 2)) * 64 + (ch & 3) * 16 + (row & 15)) * 8) = o;
    else *(u32x4*)(xn + (size_t)row * DM + ch * 8) = o;
  }
}

__global__ __launch_bounds__(256) void resln_kernel(const u16* __restrict__ xold, const float* __restrict__ part, int nsplit,
                                                    u16* __restrict__ xnew, u16* __restrict__ xn, int frag) {
  resln_rows(blockIdx.x, xold, part, nsplit, xnew, xn, frag);
}
__device__ __forceinline__ void pad_images(u16* __restrict__ qb, int blk) {
  u16* kb = qb + (size_t)BH * S_ * 32; u16* vbt = kb + (size_t)BH * S_ * 32;
  const int i = blk * 256 + threadIdx.x;
  if (i < BH * S_) {
    u32x2 z = {0u, 0u};
#pragma unroll
    for (int k = 0; k < 3; ++k) {
      *(u32x2*)(qb + (size_t)i * 32 + 20 + 4 * k) = z;
      *(u32x2*)(kb + (size_t)i * 32 + 20 + 4 * k) = (u32x2){k == 0 ? 0x3f803f80u : 0u, 0u};
    }
  }
  if (i < BH * 64 * 2 * 24) {
    int blk = i / 24, r = i - blk * 24;
    int e = 20 + (r % 12), hh = r / 12;
    unsigned f = (e == 20) ? 0x3f803f80u : 0u;
    u32x4 z = {f, f, f, f};
    *(u32x4*)(vbt + ((size_t)blk * 64 + e + 32 * hh) * 8) = z;
  }
}

enum { EPI_PART = 1, EPI_QKV_B = 2 };
struct GemmArgs {
  const u16* A; const u16* Bt; void* o0; void* o1; void* o2;
  int lda; int ldb; int Ksplit; int ntn;
  int ntm; int nblk8;
};
template <int EPI, int BN>
__global__ __launch_bounds__(256, 2) void gemm_kernel(GemmArgs g) {
  constexpr int NT = BN / 32;
  __shared__ __attribute__((aligned(16))) u16 smem[2][(128 + BN) * 64];
  const int tid = threadIdx.x, lane = tid & 63, w = tid >> 6;
  const int wr = w >> 1, wc = w & 1, c = lane & 15, gq = lane >> 4;
  int b = blockIdx.x;
  if (g.nblk8) b = (b & 7) * g.nblk8 + (b >> 3);
  const int tiles = g.ntm * g.ntn;
  const int split = b / tiles; b -= split * tiles;
  const int bm = b % g.ntm, bn = b / g.ntm;
  const u16* Ag = g.A + (size_t)bm * 128 * g.lda + (size_t)split * g.Ksplit;
  const u16* Bg = g.Bt + (size_t)bn * BN * g.ldb + (size_t)split * g.Ksplit;
  const int nk = g.Ksplit / 64;
  auto stage = [&](int buf, int kt) {
#pragma unroll
    for (int i = 0; i < 4; ++i) {
      int p = i * 256 + tid, row = p >> 3, ch = (p & 7) ^ (row & 7);
      __builtin_amdgcn_global_load_lds((const unsigned*)(Ag + (size_t)row * g.lda + kt * 64 + ch * 8),
                                       (__attribute__((address_space(3))) unsigned*)&smem[buf][p * 8], 16, 0, 0);
    }
#pragma unroll
    for (int i = 0; i < BN / 32; ++i) {
      int p = i * 256 + tid, row = p >> 3, ch = (p & 7) ^ (row & 7);
      __builtin_amdgcn_global_load_lds((const unsigned*)(Bg + (size_t)row * g.ldb + kt * 64 + ch * 8),
                                       (__attribute__((address_space(3))) unsigned*)&smem[buf][128 * 64 + p * 8], 16, 0, 0);
    }
  };
  f32x4 acc[4][NT];
#pragma unroll
  for (int m = 0; m < 4; ++m)
#pragma unroll
    for (int n = 0; n < NT; ++n) acc[m][n] = (f32x4){0.f, 0.f, 0.f, 0.f};
  bool swapped;
  if (EPI == EPI_QKV_B) swapped = (bn * BN < 2 * DM);
  else swapped = true;
  stage(0, 0);
  for (int kt = 0; kt < nk; ++kt) {
    int cur = kt & 1;
    asm volatile("s_waitcnt vmcnt(0)" ::: "memory");
    __syncthreads();
    if (kt + 1 < nk) stage(cur ^ 1, kt + 1);
    const u16* sa = smem[cur];
    const u16* sb = smem[cur] + 128 * 64;
#pragma unroll
    for (int kk = 0; kk < 2; ++kk) {
      bf16x8 af[4], bfr[NT];
#pragma unroll
      for (int m = 0; m < 4; ++m) {
        int row = wr * 64 + m * 16 + c;
        af[m] = *(const bf16x8*)(sa + row * 64 + (((kk * 4 + gq) ^ (row & 7)) * 8));
      }
#pragma unroll
      for (int n = 0; n < NT; ++n) {
        int row = wc * (BN / 2) + n * 16 + c;
        bfr[n] = *(const bf16x8*)(sb + row * 64 + (((kk * 4 + gq) ^ (row & 7)) * 8));
      }
      if (swapped) {
#pragma unroll
        for (int m = 0; m < 4; ++m)
#pragma unroll
          for (int n = 0; n < NT; ++n) acc[m][n] = mfma32(bfr[n], af[m], acc[m][n]);
      } else {
#pragma unroll
        for (int m = 0; m < 4; ++m)
#pragma unroll
          for (int n = 0; n < NT; ++n) acc[m][n] = mfma32(af[m], bfr[n], acc[m][n]);
      }
    }
  }
  const int row0 = bm * 128 + wr * 64, col0 = bn * BN + wc * (BN / 2);
  if (EPI == EPI_PART) {
    float* dst = (float*)g.o0 + (size_t)split * S_ * DM;
#pragma unroll
    for (int m = 0; m < 4; ++m)
#pragma unroll
      for (int n = 0; n < NT; ++n)
        *(f32x4*)(dst + (size_t)(row0 + m * 16 + c) * DM + col0 + n * 16 + gq * 4) = acc[m][n];
  } else {
    if (swapped) {
#pragma unroll
      for (int m = 0; m < 4; ++m)
#pragma unroll
        for (int n = 0; n < NT; ++n) {
          int nn = col0 + n * 16 + gq * 4;
          u16* dst = (nn < DM) ? (u16*)g.o0 : (u16*)g.o1;
          int cc = (nn < DM) ? nn : nn - DM;
          int hd = cc / BD, e = cc - hd * BD;
          const float qs = (nn < DM) ? 0.22360679774997896f * 1.4426950408889634f : 1.0f;
          u32x2 v = {pack2(acc[m][n][0] * qs, acc[m][n][1] * qs), pack2(acc[m][n][2] * qs, acc[m][n][3] * qs)};
          *(u32x2*)(dst + ((size_t)hd * S_ + row0 + m * 16 + c) * 32 + e) = v;
        }
    } else {
      u16* dst = (u16*)g.o2;
#pragma unroll
      for (int m = 0; m < 4; ++m)
#pragma unroll
        for (int n = 0; n < NT; ++n) {
          int cc = col0 + n * 16 + c - 2 * DM;
          int hd = cc / BD, e = cc - hd * BD;
          int t0 = row0 + m * 16;
          u32x2 v = {pack2(acc[m][n][0], acc[m][n][1]), pack2(acc[m][n][2], acc[m][n][3])};
          *(u32x2*)(dst + ((((size_t)hd * 64 + (t0 >> 5)) * 2 + ((t0 >> 4) & 1)) * 64 + e + 32 * (gq & 1)) * 8 + (gq >> 1) * 4) = v;
        }
    }
  }
}

__device__ __forceinline__ bf16x4 lds_tr(const u16* p) { return __builtin_amdgcn_ds_read_tr16_b64_v4i16((LAS bf16x4*)p); }
__device__ __forceinline__ bf16x8 cat8(bf16x4 a, bf16x4 b) { return __builtin_shufflevector(a, b, 0, 1, 2, 3, 4, 5, 6, 7); }
__device__ __forceinline__ unsigned rtc() { return (unsigned)__builtin_amdgcn_s_memrealtime(); }
__device__ __forceinline__ unsigned mtc() { return (unsigned)__builtin_amdgcn_s_memtime(); }
constexpr unsigned OOB = 0x80000000u;
constexpr unsigned FAR = 0x40000000u;
constexpr unsigned SENT = 0xffffffffu;
struct TrJob { const u16* src; u16* dst; int R; int C; int tile0; int frag; };
struct TrJobs { TrJob j[4]; };
__device__ __forceinline__ void transpose_tile(const TrJobs& jobs, int b, u16 (*tile)[66]) {
  int ji = 0;
#pragma unroll
  for (int i = 1; i < 4; ++i) if (b >= jobs.j[i].tile0) ji = i;
  const u16* src = jobs.j[ji].src; u16* dst = jobs.j[ji].dst;
  const int R = jobs.j[ji].R, C = jobs.j[ji].C, frag = jobs.j[ji].frag;
  const int t = b - jobs.j[ji].tile0, tc = C / 64;
  const int r0 = (t / tc) * 64, c0 = (t % tc) * 64;
  const int tid = threadIdx.x;
#pragma unroll
  for (int i = 0; i < 2; ++i) {
    const int p = i * 256 + tid, r = p >> 3, c = (p & 7) * 8;
    bf16x8 v = *(const bf16x8*)(src + (size_t)(r0 + r) * C + c0 + c);
#pragma unroll
    for (int k = 0; k < 8; ++k) tile[r][c + k] = (u16)v[k];
  }
  __syncthreads();
#pragma unroll
  for (int i = 0; i < 2; ++i) {
    const int p = i * 256 + tid;
    const int c = frag ? (p >> 7) * 16 + (p & 15) : p >> 3, r = frag ? ((p >> 4) & 7) * 8 : (p & 7) * 8;
    bf16x8 v;
#pragma unroll
    for (int k = 0; k < 8; ++k) v[k] = (short)tile[r + k][c];
    if (frag) *(bf16x8*)(dst + ((size_t)((c0 >> 4) + (p >> 7)) * (R >> 5) + (r0 >> 5)) * 512 + (p & 127) * 8) = v;
    else *(bf16x8*)(dst + (size_t)(c0 + c) * R + r0 + r) = v;
  }
}
struct ProArgs { TrJobs jobs; const u16* x; u16* xf; u16* qb; char* sync; const u16* wo; const u16* wv; u16* uf; const u16* mem0;
                 int ntr; int npad; int nsync; int nln; int nu; int pad; };
constexpr int WO_LD = 88;
__global__ __launch_bounds__(256, 2) void prologue_kernel(ProArgs a) {
  __shared__ __attribute__((aligned(16))) u16 smem[192 * WO_LD];
  int b = blockIdx.x;
  const int tid = threadIdx.x, lane = tid & 63, wq = __builtin_amdgcn_readfirstlane(tid >> 6), c = lane & 15, g = lane >> 4;
  if (b < 8) {
    const __amdgpu_buffer_rsrc_t rs = __builtin_amdgcn_make_buffer_rsrc(a.sync, 0, (int)SYNC_LAYER, 0x00020000);
    unsigned best[2][2] = {{~0u, ~0u}, {~0u, ~0u}};
#pragma unroll 1
    for (int r = 0; r < 4; ++r) {
      const unsigned ln = OFS_CAL + (unsigned)(b * 2 + (r & 1)) * 128u;
      const u32x4 z = {0u, 0u, 0u, 0u};
      store16(z, rs, lane < 32 ? ln + (2 * wq + (lane >> 4)) * 4096u + ((lane >> 3) & 1) * 2048u + (lane & 7) * 16u : OOB, 0, 16);
      asm volatile("s_waitcnt vmcnt(0)" ::: "memory");
      __builtin_amdgcn_s_sleep(10);
#pragma unroll
      for (int q = 0; q < 4; ++q) {
        unsigned t0, t1, x;
        const unsigned vo = ln + (2 * wq + (q >> 1)) * 4096u + (q & 1) * 2048u;
        asm volatile("s_memtime vcc\n\ts_waitcnt lgkmcnt(0)\n\ts_mov_b32 %0, vcc_lo\n\tbuffer_load_dword %2, %3, %4, 0 offen sc1\n\ts_waitcnt vmcnt(0)\n\t"
                     "s_memtime vcc\n\ts_waitcnt lgkmcnt(0)\n\ts_mov_b32 %1, vcc_lo" : "=&s"(t0), "=&s"(t1), "=&v"(x) : "v"(vo), "s"(rs) : "vcc", "memory");
        best[q >> 1][q & 1] = min(best[q >> 1][q & 1], min(t1 - t0, 65535u));
      }
    }
    unsigned* tab = (unsigned*)(a.sync + OFS_CAL + ROW);
    if (lane < 2) tab[b * 8 + 2 * wq + lane] = lane ? best[1][0] + best[1][1] : best[0][0] + best[0][1];
    if (tid == 0) tab[64 + b] = (unsigned)__builtin_amdgcn_s_getreg(20 | (3 << 11)) & 7u;
    return;
  }
  b -= 8;
  if (b < a.nu) {
    const int h = b >> 3, cg = b & 7;
    const u16* ap = a.wv + (size_t)(160 * wq + 8 * (c >> 2) + (c & 3)) * HD + h * DH + 8 * g;
    bf16x8 af[2][2][6];
    auto load_a = [&](int pr, int buf) {
#pragma unroll
      for (int kp = 0; kp < 6; ++kp) {
        af[buf][0][kp] = *(const bf16x8*)(ap + (size_t)(32 * pr) * HD + 32 * kp);
        af[buf][1][kp] = *(const bf16x8*)(ap + (size_t)(32 * pr + 4) * HD + 32 * kp);
      }
    };
    bf16x8 st[8];
#pragma unroll
    for (int i = 0; i < 8; ++i) {
      const int p = i * 256 + tid, row = p / 10, ch = p - row * 10;
      if (p < 1920) st[i] = *(const bf16x8*)(a.wo + (size_t)(h * DH + row) * DM + 80 * cg + ch * 8);
    }
    load_a(0, 0);
#pragma unroll
    for (int i = 0; i < 8; ++i) {
      const int p = i * 256 + tid, row = p / 10, ch = p - row * 10;
      if (p < 1920) *(bf16x8*)(smem + row * WO_LD + ch * 8) = st[i];
    }
    __syncthreads();
    load_a(1, 1);
    u16* dst = a.uf + ((size_t)((10 * cg + ((c >> 1) & 1)) * 16 + (h >> 1)) * 20 + 5 * wq) * 512 + (g * 16 + (h & 1) * 8 + 2 * (c >> 2) + (c & 1)) * 8;
    const u16* bp = smem + (8 * g + (c >> 2)) * WO_LD + 4 * (c & 3);
    bf16x8 bw[5][6];
#pragma unroll
    for (int t = 0; t < 5; ++t)
#pragma unroll
      for (int kp = 0; kp < 6; ++kp) bw[t][kp] = cat8(lds_tr(bp + 32 * kp * WO_LD + 16 * t), lds_tr(bp + (32 * kp + 4) * WO_LD + 16 * t));
    __builtin_amdgcn_sched_barrier(0);
#pragma unroll
    for (int pr = 0; pr < 5; ++pr) {
#pragma unroll
      for (int t = 0; t < 5; ++t) {
        f32x4 d0 = {0.f, 0.f, 0.f, 0.f}, d1 = {0.f, 0.f, 0.f, 0.f};
#pragma unroll
        for (int kp = 0; kp < 6; ++kp) {
          d0 = mfma32(af[pr & 1][0][kp], bw[t][kp], d0); d1 = mfma32(af[pr & 1][1][kp], bw[t][kp], d1);
        }
        *(u32x4*)(dst + ((size_t)t * 2 * 16 * 20 + pr) * 512) = (u32x4){pack2(d0[0], d0[1]), pack2(d0[2], d0[3]), pack2(d1[0], d1[1]), pack2(d1[2], d1[3])};
        __builtin_amdgcn_sched_barrier(0);
      }
      __builtin_amdgcn_sched_barrier(0);
      if (pr + 2 < 5) load_a(pr + 2, pr & 1);
      __builtin_amdgcn_sched_barrier(0);
    }
    return;
  }
  b -= a.nu;
  if (b < a.nln) { resln_rows(b, a.x, nullptr, 0, nullptr, a.xf, 1); return; }
  b -= a.nln;
  if (b < a.nsync) {
    const __amdgpu_buffer_rsrc_t rs = __builtin_amdgcn_make_buffer_rsrc(a.sync, 0, (int)(2 * SYNC_LAYER), 0x00020000);
#pragma unroll
    for (int i = 0; i < 4; ++i) {
      const unsigned off = (unsigned)b * 16384u + (unsigned)(i * 256 + tid) * 16u;
      const unsigned o = off & (SYNC_LAYER - 1), rr = o >> 15, in = o & 4095u;
      u32x4 z = {SENT, SENT, SENT, SENT};
      if (o >= OFS_CW) z = (u32x4){0u, 0u, 0u, 0u};
      else if (rr < 4 && (!(rr & 1) || in < 1024u)) {
        const int L = 5 * (rr >> 1) + 4 * (rr & 1) + (in >> 10), ln = (in & 1023u) >> 4;
        const unsigned* row = (const unsigned*)(a.mem0 + (ln & 7) * DM + 64 * L + 16 * (ln >> 4) + 2 * ((ln >> 3) & 1));
        z = (u32x4){min(row[0], SENT - 1), min(row[2], SENT - 1), min(row[4], SENT - 1), min(row[6], SENT - 1)};
      }
      store16(z, rs, (o >= OFS_CAL && o < OFS_CW) ? OOB : off, 0, 16);
    }
    return;
  }
  b -= a.nsync;
  if (b < a.ntr) { transpose_tile(a.jobs, b, (u16 (*)[66])smem); return; }
  pad_images(a.qb, b - a.ntr);
}

struct RecArgs {
  const u16* wf; const u16* uf; const u16* xf; char* qk; char* y; char* z; char* pt; char* sync; const char* cal;
  const u16* xold; u16* xnew; unsigned long long* dbg;
  int layer, tail, d0, g2c, g2t, dur0, lead, slow;
};
template <typename F, int... I> __device__ __forceinline__ void static_for_(F&& f, std::integer_sequence<int, I...>) { (f(std::integral_constant<int, I>()), ...); }
template <int N, typename F> __device__ __forceinline__ void static_for(F&& f) { static_for_(f, std::make_integer_sequence<int, N>()); }
#define RLX_AGENT __ATOMIC_RELAXED, __HIP_MEMORY_SCOPE_AGENT
#define SPIN_LOG 20
constexpr unsigned SPIN_CAP = 1u << SPIN_LOG;
#define PIN_A(x) asm volatile("" : "+a"(x))
#define PIN_V(x) asm volatile("" : "+v"(x))
#define PIN_S(x) do { (x) = __builtin_amdgcn_readfirstlane(x); } while (0)
#define SB() __builtin_amdgcn_sched_barrier(0)
#define NOPS(n) do { SB(); asm volatile("s_nop %0" :: "n"(n)); SB(); } while (0)
#define WG_BARRIER() do { asm volatile("s_waitcnt lgkmcnt(0)" ::: "memory"); __builtin_amdgcn_s_barrier(); asm volatile("" ::: "memory"); } while (0)
#define RFL(x) ((unsigned)__builtin_amdgcn_readfirstlane(x))
template <int CTRL> __device__ __forceinline__ unsigned dppu(unsigned v) { return (unsigned)__builtin_amdgcn_update_dpp(0, (int)v, CTRL, 0xf, 0xf, true); }
template <int CTRL> __device__ __forceinline__ float dppf(float v) { return u2f(dppu<CTRL>(f2u(v))); }
template <int BANKS> __device__ __forceinline__ unsigned ror8_banks(unsigned v) { return (unsigned)__builtin_amdgcn_update_dpp((int)v, (int)v, 0x128, 0xf, BANKS, false); }
constexpr int ROR1 = 0x121, ROR2 = 0x122, ROR4 = 0x124, ROR8 = 0x128, SHR8 = 0x118;
__device__ __forceinline__ float fmx(float a, float b) { return __builtin_elementwise_maximum(a, b); }
__device__ __forceinline__ unsigned mx3(unsigned a, unsigned b, unsigned c) { return max(max(a, b), c); }
__device__ __forceinline__ unsigned mx4(u32x4 v) { return max(mx3(v[0], v[1], v[2]), v[3]); }
__device__ __forceinline__ u32x8 cat2(u32x4 lo, u32x4 hi) { return __builtin_shufflevector(lo, hi, 0, 1, 2, 3, 4, 5, 6, 7); }
__device__ __forceinline__ u32x4 lo4(u32x8 v) { return __builtin_shufflevector(v, v, 0, 1, 2, 3); }
__device__ __forceinline__ u32x4 hi4(u32x8 v) { return __builtin_shufflevector(v, v, 4, 5, 6, 7); }
__device__ __forceinline__ u32x2 lo2(u32x4 v) { return __builtin_shufflevector(v, v, 0, 1); }
__device__ __forceinline__ u32x2 hi2(u32x4 v) { return __builtin_shufflevector(v, v, 2, 3); }
__device__ __forceinline__ u32x4 pack8(f32x4 a, f32x4 b) { return (u32x4){pack2(a[0], a[1]), pack2(a[2], a[3]), pack2(b[0], b[1]), pack2(b[2], b[3])}; }
#define DEV __device__ __forceinline__
DEV void smf_v(f32x4& acc, u32x4 a, u32x8 b, int idx) { asm volatile("v_smfmac_f32_16x16x64_bf16 %0, %1, %2, %3" : "+v"(acc) : "v"(a), "v"(b), "v"(idx)); }
template <int R> DEV void smf_a(f32x4& acc, u32x4 a, int idx) { asm volatile("v_smfmac_f32_16x16x64_bf16 %0, %1, a[%c3:%c4], %2" : "+v"(acc) : "v"(a), "v"(idx), "n"(R), "n"(R + 7)); }
template <int R> DEV void mf32_ba(f32x4& acc, u32x4 a) { asm volatile("v_mfma_f32_16x16x32_bf16 %0, %1, a[%c2:%c3], %0" : "+v"(acc) : "v"(a), "n"(R), "n"(R + 3)); }
template <int R> DEV void mf32z_ba(f32x4& d, u32x4 a) { asm volatile("v_mfma_f32_16x16x32_bf16 %0, %1, a[%c2:%c3], 0" : "=&v"(d) : "v"(a), "n"(R), "n"(R + 3)); }
template <int R> DEV void mf32_aa(f32x4& acc, u32x4 b) { asm volatile("v_mfma_f32_16x16x32_bf16 %0, a[%c2:%c3], %1, %0" : "+v"(acc) : "v"(b), "n"(R), "n"(R + 3)); }
template <int R> DEV void mf32z_aa(f32x4& d, u32x4 b) { asm volatile("v_mfma_f32_16x16x32_bf16 %0, a[%c2:%c3], %1, 0" : "=&v"(d) : "v"(b), "n"(R), "n"(R + 3)); }
template <int R> DEV void ld_a(__amdgpu_buffer_rsrc_t r, unsigned vo, unsigned so) { asm volatile("buffer_load_dwordx4 a[%c3:%c4], %0, %1, %2 offen" :: "v"(vo), "s"(r), "s"(so), "n"(R), "n"(R + 3) : "memory"); }
template <int R> DEV void ld_a1(__amdgpu_buffer_rsrc_t r, unsigned vo, unsigned so) { asm volatile("buffer_load_dwordx4 a[%c3:%c4], %0, %1, %2 offen sc1" :: "v"(vo), "s"(r), "s"(so), "n"(R), "n"(R + 3) : "memory"); }
DEV void mf32(f32x4& acc, u32x4 a, u32x4 b) { asm volatile("v_mfma_f32_16x16x32_bf16 %0, %1, %2, %0" : "+v"(acc) : "v"(a), "v"(b)); }
DEV void mf32z(f32x4& d, u32x4 a, u32x4 b) { asm volatile("v_mfma_f32_16x16x32_bf16 %0, %1, %2, 0" : "=&v"(d) : "v"(a), "v"(b)); }
DEV void mf32_n(f32x4& acc, u32x4 a, u32x4 b) { asm volatile("s_nop 1\n\tv_mfma_f32_16x16x32_bf16 %0, %1, %2, %0" : "+v"(acc) : "v"(a), "v"(b)); }
DEV void mf32z_n(f32x4& d, u32x4 a, u32x4 b) { asm volatile("s_nop 1\n\tv_mfma_f32_16x16x32_bf16 %0, %1, %2, 0" : "=&v"(d) : "v"(a), "v"(b)); }
DEV void mf16(f32x4& acc, u32x2 a, u32x2 b) { asm volatile("v_mfma_f32_16x16x16_bf16 %0, %1, %2, %0" : "+v"(acc) : "v"(a), "v"(b)); }
DEV void mf16z(f32x4& d, u32x2 a, u32x2 b) { asm volatile("v_mfma_f32_16x16x16_bf16 %0, %1, %2, 0" : "=&v"(d) : "v"(a), "v"(b)); }
DEV void zero4(f32x4& d) { f32x2 x, y; asm volatile("v_mov_b64 %0, 0\n\tv_mov_b64 %1, 0" : "=v"(x), "=v"(y)); d = __builtin_shufflevector(x, y, 0, 1, 2, 3); asm volatile("" : "+v"(d)); }
DEV void clock_now(u64& t) { asm volatile("s_memtime %0" : "=s"(t)); }
template <typename T> DEV void pin_v(T& x) { asm volatile("" : "+v"(x)); }
#define AGPR_FENCE() asm volatile("" ::: \
    "a0", "a1", "a2", "a3", "a4", "a5", "a6", "a7", "a8", "a9", "a10", "a11", "a12", "a13", "a14", "a15", "a16", "a17", "a18", "a19", "a20", "a21", "a22", \
    "a23", "a24", "a25", "a26", "a27", "a28", "a29", "a30", "a31", "a32", "a33", "a34", "a35", "a36", "a37", "a38", "a39", "a40", "a41", "a42", "a43", \
    "a44", "a45", "a46", "a47", "a48", "a49", "a50", "a51", "a52", "a53", "a54", "a55", "a56", "a57", "a58", "a59", "a60", "a61", "a62", "a63", "a64", \
    "a65", "a66", "a67", "a68", "a69", "a70", "a71", "a72", "a73", "a74", "a75", "a76", "a77", "a78", "a79", "a80", "a81", "a82", "a83", "a84", "a85", \
    "a86", "a87", "a88", "a89", "a90", "a91", "a92", "a93", "a94", "a95", "a96", "a97", "a98", "a99", "a100", "a101", "a102", "a103", "a104", "a105", \
    "a106", "a107", "a108", "a109", "a110", "a111", "a112", "a113", "a114", "a115", "a116", "a117", "a118", "a119", "a120", "a121", "a122", "a123", \
    "a124", "a125", "a126", "a127", "a128", "a129", "a130", "a131", "a132", "a133", "a134", "a135", "a136", "a137", "a138", "a139", "a140", "a141", \
    "a142", "a143", "a144", "a145", "a146", "a147", "a148", "a149", "a150", "a151", "a152", "a153", "a154", "a155", "a156", "a157", "a158", "a159", \
    "a160", "a161", "a162", "a163", "a164", "a165", "a166", "a167", "a168", "a169", "a170", "a171", "a172", "a173", "a174", "a175", "a176", "a177", \
    "a178", "a179", "a180", "a181", "a182", "a183", "a184", "a185", "a186", "a187", "a188", "a189", "a190", "a191", "a192", "a193", "a194", "a195", \
    "a196", "a197", "a198", "a199", "a200", "a201", "a202", "a203", "a204", "a205", "a206", "a207", "a208", "a209", "a210", "a211", "a212", "a213", \
    "a214", "a215", "a216", "a217", "a218", "a219", "a220", "a221", "a222", "a223", "a224", "a225", "a226", "a227", "a228", "a229", "a230", "a231", \
    "a232", "a233", "a234", "a235", "a236", "a237", "a238", "a239", "a240", "a241", "a242", "a243", "a244", "a245", "a246", "a247", "a248", "a249", \
    "a250", "a251", "a252", "a253", "a254", "a255")
#define LDP16(r, off, so) __builtin_amdgcn_raw_buffer_load_b128((r), (off), (so), 0)
#define VM_WAIT(n) asm volatile("s_waitcnt vmcnt(%0)" :: "n"(n) : "memory")
#define LD16(r, off, so) __builtin_amdgcn_raw_buffer_load_b128((r), (off), (so), 16)
#define LD8(r, off, so) __builtin_amdgcn_raw_buffer_load_b64((r), (off), (so), 16)
#define LD4(r, off, so) __builtin_amdgcn_raw_buffer_load_b32((r), (off), (so), 16)
#define ST16(v, r, off, so) store16((v), (r), (off), (so), 16)
#define RSRC(p, n) __builtin_amdgcn_make_buffer_rsrc((void*)(p), 0, (int)(n), 0x00020000)
#define LDS16(off) (*(LAS u32x4*)(lds + (off)))
template <int IMM> DEV void dma1k(__amdgpu_buffer_rsrc_t r, unsigned vo, unsigned so, unsigned ldsoff) {
  asm volatile("s_mov_b32 m0, %0\n\ts_nop 0\n\tbuffer_load_dwordx4 %1, %2, %3 offen offset:%c4 lds" :: "s"(ldsoff), "v"(vo), "s"(r), "s"(so), "n"(IMM) : "memory");
}

DEV int s_clamp(int v, int lim) { int r; asm("s_min_i32 %0, %1, %2\n\ts_max_i32 %0, %0, %3" : "=&s"(r) : "s"(v), "n"(lim), "n"(-lim) : "scc"); return r; }
DEV unsigned s_mulshr16(unsigned x, unsigned y) { unsigned r; asm("s_mul_i32 %0, %1, %2\n\ts_lshr_b32 %0, %0, 16" : "=s"(r) : "s"(x), "s"(y) : "scc"); return r; }
struct Clock {
  unsigned me, re, dm, dt, rho, sig;
  float fm, ft, fx, fy;
  DEV void edge() {
    u64 m, r0, r1, r2, r3, r4, r5, r6, r7; unsigned n;
#define CNT_(k) "s_cmp_eq_u64 %" #k ", %1\n\ts_addc_u32 %9, %9, 0\n\t"
    asm volatile("s_memtime %0\n\ts_memrealtime %1\n\ts_nop 2\n\ts_memrealtime %2\n\ts_nop 2\n\ts_memrealtime %3\n\ts_nop 2\n\ts_memrealtime %4\n\ts_nop 2\n\t"
                 "s_memrealtime %5\n\ts_nop 2\n\ts_memrealtime %6\n\ts_nop 2\n\ts_memrealtime %7\n\ts_nop 2\n\ts_memrealtime %8\n\t"
                 "s_mov_b32 %9, 0\n\ts_waitcnt lgkmcnt(0)\n\t" CNT_(2) CNT_(3) CNT_(4) CNT_(5) CNT_(6) CNT_(7) CNT_(8)
                 : "=&s"(m), "=&s"(r0), "=&s"(r1), "=&s"(r2), "=&s"(r3), "=&s"(r4), "=&s"(r5), "=&s"(r6), "=&s"(r7), "=&s"(n) :: "scc", "memory");
    take(m, r0, n);
  }
  DEV void take(u64 m, u64 r0, unsigned n) {
    const unsigned m1 = (unsigned)m + 16u * n + 12u, t1 = ((unsigned)r0 + 4u) * 8u;
    dm = m1 - me; dt = t1 - re; me = m1; re = t1;
  }
  template <int K> DEV void rate_step() {
    if (K == 0) { fm = (float)dm; pin_v(fm); }
    if (K == 1) { ft = (float)dt; pin_v(ft); }
    if (K == 2) { fy = __builtin_amdgcn_rcpf(fm); pin_v(fy); }
    if (K == 3) { fx = __builtin_amdgcn_rcpf(ft); pin_v(fx); }
    if (K == 4) { fm *= 2048.0f; pin_v(fm); }
    if (K == 5) { ft *= 65536.0f; pin_v(ft); }
    if (K == 6) { fm *= fx; pin_v(fm); }
    if (K == 7) { ft *= fy; pin_v(ft); }
    if (K == 8) { fm = u2f((unsigned)fm); pin_v(fm); }
    if (K == 9) { ft = u2f((unsigned)ft); pin_v(ft); }
    if (K == 10) { dm = RFL(f2u(fm)); asm volatile("" : "+s"(dm)); }
    if (K == 11) { dt = RFL(f2u(ft)); asm volatile("" : "+s"(dt)); }
    if (K == 12) { const bool ok = dm - 12u * 256u < 14u * 256u; rho = ok ? dm : rho; sig = ok ? dt : sig; asm volatile("" : "+s"(rho), "+s"(sig)); }
  }
  template <int K> DEV void rho_step() {
    if (K < 2) rate_step<K>(); else if (K < 4) rate_step<K + 1>(); else if (K < 7) rate_step<2 * K - 2>();
    else { rho = dm - 12u * 256u < 14u * 256u ? dm : rho; asm volatile("" : "+s"(rho)); }
  }
  DEV void rate() { static_for<13>([&](auto K) { rate_step<decltype(K)::value>(); }); }
  DEV void init() { rho = 22u * 256u; sig = 65536u * 8u / 22u; me = 0u; re = 0u; edge(); }
  DEV unsigned local(unsigned t) const { unsigned r = me + (unsigned)((s_clamp((int)(t - re), 100000) * (int)rho) >> 11); PIN_S(r); return r; }
  DEV unsigned global(unsigned m) const { int p = s_clamp((int)(m - me), 40000) * (int)sig; PIN_S(p); p >>= 16; PIN_S(p); return re + (unsigned)p; }
};
__device__ __forceinline__ void wait_until(unsigned tgt) {
  unsigned t;
  asm volatile("1:\n\ts_memtime vcc\n\ts_waitcnt lgkmcnt(0)\n\ts_sub_i32 %0, %1, vcc_lo\n\ts_sub_u32 vcc_hi, %0, 209\n\ts_cmp_lt_u32 vcc_hi, 60000\n\ts_cbranch_scc1 1b\n\t"
               "s_sub_i32 %0, %0, 112\n\ts_max_i32 %0, %0, 0\n\ts_min_i32 %0, %0, 96\n\ts_and_b32 %0, %0, 0x7c\n\t"
               "s_getpc_b64 vcc\n2:\n\ts_sub_u32 %0, 3f-2b, %0\n\ts_add_u32 vcc_lo, vcc_lo, %0\n\ts_addc_u32 vcc_hi, vcc_hi, 0\n\ts_setpc_b64 vcc\n\t"
               ".rept 24\n\ts_nop 0\n\t.endr\n3:" : "=&s"(t) : "s"(tgt) : "vcc", "scc", "memory");
}
__device__ __forceinline__ void coarse_until(unsigned tgt) {
  for (;;) { const unsigned rem = tgt - mtc(); if (rem - 1u >= 60000u) break; }
}
struct Sched {
  unsigned T, tA, tP, dur;
  __device__ __forceinline__ void next(u32x2 an, const RecArgs& a) {
    int d = (int)(an[0] - T); unsigned gp = an[1];
    d = max(d, (int)dppu<ROR8>(d)); gp = max(gp, dppu<ROR8>(gp));
    d = max(d, (int)dppu<ROR4>(d)); gp = max(gp, dppu<ROR4>(gp));
    d = max(d, (int)dppu<ROR2>(d)); gp = max(gp, dppu<ROR2>(gp));
    d = max(d, (int)dppu<ROR1>(d)); gp = max(gp, dppu<ROR1>(gp));
    const int d0 = __builtin_amdgcn_readlane(d, 0), d1 = __builtin_amdgcn_readlane(d, 16);
    const unsigned g0 = __builtin_amdgcn_readlane(gp, 0), g1 = __builtin_amdgcn_readlane(gp, 16);
    const int dm = max(d0, d1); const unsigned gm = max(g0, g1);
    tP = T + (unsigned)dm + a.d0;
    T = tP + (gm & 0xffffu);
    if (gm < 0x10000u) dur += (dm - (int)dur) >> 2;
    tA = T + dur - a.lead;
    PIN_S(T); PIN_S(tA); PIN_S(tP); PIN_S(dur);
  }
};
template <typename R>
__device__ __forceinline__ u32x2 ann_valid(u32x2 an, R rs, unsigned vo, unsigned so, bool& dead) {
  if (__builtin_expect(__any(max(an[0], an[1]) == SENT), 0)) {
    for (unsigned spins = 0; !dead;) {
      __builtin_amdgcn_s_sleep(3);
      const bool need = max(an[0], an[1]) == SENT;
      const u32x2 n = LD8(rs, need ? vo : OOB, so);
      if (need) an = n;
      if (!__any(max(an[0], an[1]) == SENT)) break;
      if (++spins > SPIN_CAP) dead = true;
      asm volatile("" ::: "memory");
    }
  }
  return an;
}
template <typename R>
__device__ __forceinline__ void need_progress(R rs, unsigned vo, unsigned want, unsigned& have) {
  for (unsigned spins = 0; have < want && spins < SPIN_CAP; ++spins) {
    unsigned v = LD4(rs, vo, 0);
    v = min(v, dppu<0x4E>(v)); v = min(v, dppu<0xB1>(v));
    have = RFL(v);
    if (have < want) __builtin_amdgcn_s_sleep(8);
    asm volatile("" ::: "memory");
  }
}
struct Softmax {
  f32x4 s; float m, x;
  DEV void a() {
    float t;
    x = fmx(fmx(s[0], s[1]), s[2]); pin_v(x); x = fmx(fmx(x, s[3]), m);
    asm volatile("s_nop 1\n\tv_max_f32_dpp %0, %0, %0 row_ror:8 row_mask:0xf bank_mask:0xf\n\ts_nop 1\n\tv_max_f32_dpp %0, %0, %0 row_ror:4 row_mask:0xf bank_mask:0xf\n\t"
                 "v_mov_b32 %1, %0\n\ts_nop 1\n\tv_permlane32_swap_b32 %0, %1\n\tv_max_f32 %0, %0, %1\n\tv_mul_f32 %0, 0xbdd53b94, %0" : "+v"(x), "=&v"(t));
  }
  DEV void b() {
    const float C = 0.07216878364870322f * 1.4426950408889634f;
    s = __builtin_elementwise_fma(s, (f32x4){C, C, C, C}, (f32x4){x, x, x, x}); m = __builtin_fmaf(m, C, x);
#pragma unroll
    for (int i = 0; i < 4; ++i) s[i] = __builtin_amdgcn_exp2f(s[i]);
    m = __builtin_amdgcn_exp2f(m);
    pin_v(s); pin_v(m);
  }
  DEV void c() {
    float t;
    const f32x2 u = (f32x2){s[0], s[1]} + (f32x2){s[2], s[3]};
    x = (u[0] + m) + u[1];
    asm volatile("s_nop 1\n\tv_add_f32_dpp %0, %0, %0 row_ror:8 row_mask:0xf bank_mask:0xf\n\ts_nop 1\n\tv_add_f32_dpp %0, %0, %0 row_ror:4 row_mask:0xf bank_mask:0xf\n\t"
                 "v_mov_b32 %1, %0\n\ts_nop 1\n\tv_permlane32_swap_b32 %0, %1\n\tv_add_f32 %0, %0, %1\n\tv_rcp_f32 %0, %0" : "+v"(x), "=&v"(t));
  }
  DEV u32x4 d() {
    unsigned o0, o1, o2, o3; float t, u;
    asm volatile("s_nop 0\n\tv_mul_f32 %4, %10, %11\n\tv_mul_f32 %5, %10, %11\n\tv_mul_f32 %0, %6, %11\n\tv_mul_f32 %2, %7, %11\n\tv_permlane32_swap_b32 %4, %5\n\t"
                 "v_mul_f32 %1, %8, %11\n\tv_mul_f32 %3, %9, %11\n\tv_cvt_pk_bf16_f32 %0, %0, |%2|\n\tv_cvt_pk_bf16_f32 %1, %1, |%3|\n\tv_cvt_pk_bf16_f32 %4, %4, |%5|\n\t"
                 "v_mov_b32_dpp %2, %0 row_ror:8 row_mask:0xf bank_mask:0xf\n\tv_mov_b32_dpp %3, %1 row_ror:8 row_mask:0xf bank_mask:0xf\n\t"
                 "v_mov_b32_dpp %0, %4 row_shr:8 row_mask:0xf bank_mask:0x4\n\tv_mov_b32_dpp %1, %4 row_shr:4 row_mask:0xf bank_mask:0x4\n\t"
                 "v_mov_b32_dpp %2, %4 quad_perm:[0,1,2,3] row_mask:0xf bank_mask:0x4\n\tv_mov_b32_dpp %3, %4 row_shl:4 row_mask:0xf bank_mask:0x4"
                 : "=&v"(o0), "=&v"(o1), "=&v"(o2), "=&v"(o3), "=&v"(t), "=&v"(u) : "v"(s[0]), "v"(s[1]), "v"(s[2]), "v"(s[3]), "v"(m), "v"(x));
    return (u32x4){o0, o1, o2, o3};
  }
};
#define STAMP(k)
#define STAMP_DECL(sh)
#define STAMP_FLUSH(s, ref, x0, x1)

#define LD_GAP 4
#define LD_UP 5
#define LD_NOP 11
#define SM_W 2
constexpr int S_CT = 0, S_EX = 16, S_ET = S_EX + 4 * 2048, S_LW = S_ET + 4 * 5120, S_NL = 13, S_END = S_LW + 4 * S_NL * 2048;
__host__ __device__ constexpr int s_q(int n) { return n < 30 ? n / 6 : n < 50 ? 5 + (n - 30) / 4 : 5 + (n - 50) / 2; }
__host__ __device__ constexpr int s_t(int n) { return n < 30 ? n % 6 : n < 50 ? (n - 30) % 4 : 4 + (n & 1); }
__host__ __device__ constexpr bool s_lds(int n) { return n % 4 == 3 && n / 4 < S_NL; }
__host__ __device__ constexpr int s_ri(int n) { return n - ((n + 1) / 4 < S_NL ? (n + 1) / 4 : S_NL); }
__device__ __forceinline__ void score_role(const RecArgs& a, LAS char* lds, int rep, int il, unsigned posM, unsigned posP) {
  const int lane = threadIdx.x & 63, w = __builtin_amdgcn_readfirstlane(threadIdx.x >> 6), c = lane & 15, g = lane >> 4, hf = c >> 3, i8 = c & 7;
  const int h = rep * 4 + il, iod = rep >> 2;
  const __amdgpu_buffer_rsrc_t rs = RSRC(a.sync, SYNC_LAYER), rq = RSRC(a.qk, 2 * QK_IMG), rp = RSRC(a.pt, PT_IMG);
  Clock ck; ck.init();
  u32x8 WV[28 - S_NL];
  {
    const __amdgpu_buffer_rsrc_t rw = RSRC(a.wf, 768 * 20480);
    const unsigned vo = lane * 16, wb = (unsigned)(h * 12 + 3 * w) * 20480u;
    static_for<60>([&](auto N) {
      constexpr int n = decltype(N)::value, q = s_q(n), t = s_t(n), ri = s_ri(n);
      const unsigned so = wb + (unsigned)((t & 1) * 384 + (t >> 1)) * 20480u + (unsigned)(q < 5 ? q + 5 * iod : q - 5 * iod) * 2048u;
      if (s_lds(n)) { LDS16(S_LW + (w * S_NL + n / 4) * 2048 + lane * 16) = LDP16(rw, vo, so); LDS16(S_LW + (w * S_NL + n / 4) * 2048 + 1024 + lane * 16) = LDP16(rw, vo + 1024, so); }
      else if (ri >= 32) WV[ri < 32 ? 0 : ri - 32] = cat2(LDP16(rw, vo, so), LDP16(rw, vo + 1024, so));
    });
    SB();
    static_for<60>([&](auto N) {
      constexpr int n = decltype(N)::value, q = s_q(n), t = s_t(n), ri = s_ri(n);
      const unsigned so = wb + (unsigned)((t & 1) * 384 + (t >> 1)) * 20480u + (unsigned)(q < 5 ? q + 5 * iod : q - 5 * iod) * 2048u;
      if (!s_lds(n) && ri < 32) { ld_a<8 * (ri & 31)>(rw, vo, so); ld_a<8 * (ri & 31) + 4>(rw, vo, so + 1024u); VM_WAIT(40); }
    });
    VM_WAIT(0);
#pragma unroll
    for (int i = 0; i < 28 - S_NL; ++i) PIN_V(WV[i]);
  }
  const int sidx = hf ? 0xEEEE : 0x4444;
  u32x4 idn;
#pragma unroll
  for (int jj = 0; jj < 4; ++jj) idn[jj] = (((c >> 2) & 1) == (g & 1) && jj == ((c & 3) >> 1) + 2 * hf) ? ((c & 1) ? 0x3f800000u : 0x3f80u) : 0u;
  const unsigned lomask = hf ? 0u : ~0u;
  unsigned l16 = lane * 16u;
  const unsigned an_ld = lane < 32 ? ((lane >> 4) * 2 + 1) * ROW + posM * 4096u + 1024u + c * 128u : OOB;
  const unsigned an_st = w == 3 ? ((h >> 4) * 2 + 1) * ROW + (lane >> 3) * 4096u + 1024u + (h & 15) * 128u + i8 * 16u : OOB;
  const int sr = (lane & 3) | ((lane >> 2) & 4), sg = ((lane >> 2) & 1) | ((lane >> 4) & 2), sk = ((lane >> 1) & 6) | (lane >> 5), cq = 8 * (w & 1) + sr;
  const bool smem_lane = (lane & 44) == 8;
  const unsigned p_st = !hf ? OFS_P + (h >> 3) * ROW + (8 / SM_W) * w * 4096u + ((h >> 1) & 3) * 1024u + sg * 256u + (h & 1) * 128u + sr * 16u
                            : smem_lane ? OFS_P + 4 * ROW + (8 / SM_W) * w * 4096u + h * 128u + sr * 16u : OOB;
  const unsigned pt_st = !hf ? (unsigned)(h * 2048 + (w >> 1) * 1024 + (sg * 16 + cq) * 16) : smem_lane ? PT_M + ((w >> 1) * NH + h) * 256u + cq * 16u : OOB;
  const unsigned lw = S_LW + w * S_NL * 2048 + lane * 16;
  unsigned ex_w = S_EX + w * 2048 + lane * 16; PIN_V(ex_w);
  const unsigned ex_r = S_EX + (sg * 16 + hf * 8 + sr) * 16, ex_m = S_EX + 1024 + ((2 + (sk >> 2)) * 16 + sr) * 16 + (sk & 3) * 4;
  const unsigned jp_vo = lane < 4 ? OFS_CW + CW_JP + (unsigned)((((lane & 1) * 384 + h * 12 + ((lane >> 1) & 1) * 11) / 20) * 128) : OOB;
  unsigned jhave = 0;
  u32x4 kf[3], qf[3];
  auto load_qk = [&](int s) {
    need_progress(rs, jp_vo, (unsigned)s + 1u, jhave);
    const unsigned so = (unsigned)((s * NH + h) * 12 + 3 * w) * 1024u;
#pragma unroll
    for (int p = 0; p < 3; ++p) { kf[p] = LD16(rq, l16 + p * 1024, so + QK_IMG); qf[p] = LD16(rq, l16 + p * 1024, so); }
  };
  load_qk(0);
  bool dead = false;
  unsigned extra = 0;
  Softmax tk; tk.s = (f32x4){0.f, 0.f, 0.f, 0.f}; tk.m = 0.f;
  ck.edge(); ck.rate();
  Sched sd; sd.T = ck.global(mtc()); sd.tA = sd.T - 8000u; sd.tP = sd.T; sd.dur = a.dur0;
  unsigned mT = mtc(), a_tl = 0u, a_sl = ck.re + a.slow, a_gp = a.g2t + s_mulshr16(a.g2c, ck.sig) + 400u, a_th = 0x80000000u;
  STAMP_DECL(0);
#pragma unroll 1
  for (int s = 0; s < NSEG; ++s) {
    const unsigned sl = (unsigned)(s & 3) * M_SLOT, soA = sl + posM * 4096u + iod * (2 * ROW), soB = sl + posM * 4096u + (1 - iod) * (2 * ROW);
    PIN_V(l16);
    f32x4 acc[6], dx, dm, tr, ts;
    u32x4 cm[10], ta, tc, bq, tl[2], th[2]; u32x2 X[3];
    bool first = true;
#define LDM(q) LD16(rs, l16 + ((q) % 5 < 4 ? ((q) % 5) * 1024 : 0), ((q) < 5 ? soA : soB) + ((q) % 5 < 4 ? 0 : ROW))
#define POS(n) do { constexpr int q_ = s_q(n), t_ = s_t(n), ri_ = s_ri(n); \
      if (s_lds(n)) smf_v(acc[t_], cm[q_], cat2(tl[((n) / 4) & 1], th[((n) / 4) & 1]), sidx); \
      else if (ri_ < 32) smf_a<8 * (ri_ & 31)>(acc[t_], cm[q_], sidx); else smf_v(acc[t_], cm[q_], WV[ri_ < 32 ? 0 : ri_ - 32], sidx); \
      SB(); \
      if ((n) % 4 == 3 && (n) / 4 + 2 < S_NL) tl[((n) / 4) & 1] = LDS16(lw + ((n) / 4 + 2) * 2048); \
      if ((n) % 4 == 0 && (n) > 0 && (n) / 4 + 1 < S_NL) th[((n) / 4 + 1) & 1] = LDS16(lw + ((n) / 4 + 1) * 2048 + 1024); \
      SB(); } while (0)
#define CVT(dst, src, k) do { unsigned v_ = pack2((src)[2 * (k)], (src)[2 * (k) + 1]); PIN_V(v_); dst = v_; SB(); } while (0)
#define B_LO(p, k) do { unsigned v_ = X[p][k] & lomask; PIN_V(v_); bq[k] = v_; SB(); } while (0)
#define B_HI(p, k) do { unsigned v_ = dppu<SHR8>(X[p][k]); PIN_V(v_); bq[2 + (k)] = v_; SB(); } while (0)
    u64 tann;
    auto prime = [&]() {
      tl[0] = LDS16(lw); th[0] = LDS16(lw + 1024); tl[1] = LDS16(lw + 2048); th[1] = LDS16(lw + 3072);
#pragma unroll
      for (int t = 0; t < 6; ++t) zero4(acc[t]);
      SB();
    };
    prime();
    const unsigned sp = (unsigned)(s & 1) * P_SLOT;
    asm volatile("" :: "s"(soA), "s"(soB), "s"(soA + ROW), "s"(soB + ROW), "s"(a_tl), "s"(a_sl), "s"(a_gp), "s"(a_gp | 0x10000u), "s"(a_th), "s"(sp), "s"(sp + 4096u));
    wait_until(mT);
    SB();
    STAMP(0);
    cm[0] = LDM(0); SB(); tk.a(); SB();
    cm[1] = LDM(1); SB(); tk.b(); SB();
    cm[2] = LDM(2); SB(); tk.c(); SB();
    cm[3] = LDM(3); SB();
    u32x4 ptk = tk.d(); PIN_V(ptk); SB();
    cm[4] = LDM(4); SB();
#pragma unroll
    for (int q = 5; q < LD_UP; ++q) { NOPS(LD_NOP); cm[q] = LDM(q); SB(); }
    unsigned chk = 0;
    static_for<48>([&](auto N) {
      constexpr int n = decltype(N)::value, q = s_q(n);
      POS(n);
      if (n == 0) STAMP(1);
      if (n == 47) clock_now(tann);
      if constexpr (n % 4 == 2 && LD_UP + n / 4 < 10) cm[LD_UP + n / 4] = LDM(LD_UP + n / 4);
      SB();
      if (n == 1 || n == 9 || n == 13 || n == 21 || n == 25 || n == 32 || n == 36 || n == 40 || n == 44 || n == 47) { chk = mx3(chk, cm[q][0], cm[q][2]); pin_v(chk); }
      SB();
    });
    POS(48);
    u64 bad = __builtin_amdgcn_ballot_w64(chk == SENT); asm volatile("" : "+s"(bad)); SB();
    POS(49);
    if (__builtin_expect(bad != 0, 0)) {
      first = false;
      for (unsigned spins = 0;;) {
        __builtin_amdgcn_s_sleep(1);
        unsigned m = 0;
#pragma unroll
        for (int q = 5; q < 10; ++q) cm[q] = LDM(q);
#pragma unroll
        for (int q = 5; q < 10; ++q) m = max(m, mx4(cm[q]));
        SB();
#pragma unroll
        for (int q = 0; q < 5; ++q) cm[q] = LDM(q);
#pragma unroll
        for (int q = 0; q < 5; ++q) m = max(m, mx4(cm[q]));
        if (!__any(m == SENT) || dead) break;
        if (++spins > SPIN_CAP) dead = true;
        asm volatile("" ::: "memory");
      }
      prime();
      NOPS(1);
      static_for<50>([&](auto N) {
        constexpr int n = decltype(N)::value;
        POS(n);
        if constexpr (n % 6 == 5 && n < 30) cm[5 + n / 6] = LDM(5 + n / 6);
        SB();
      });
      asm volatile("s_memtime %0" : "=s"(tann));
    }
    STAMP(2);
    CVT(ta[0], acc[0], 0);
    POS(50); asm volatile("s_waitcnt lgkmcnt(0)" : "+s"(tann), "+v"(tl[0]), "+v"(th[0])); SB(); CVT(ta[1], acc[0], 1);
    POS(51); int a_d = (int)((unsigned)tann - ck.me); asm volatile("s_min_u32 %0, %0, %1" : "+s"(a_d) : "n"(40000) : "scc"); SB(); CVT(ta[2], acc[1], 0);
    POS(52); a_d = (a_d * (int)ck.sig) >> 16; asm volatile("" : "+s"(a_d)); SB(); CVT(ta[3], acc[1], 1);
    POS(53); unsigned a_x, a_g;
    asm volatile("s_cmp_gt_i32 %2, %3\n\ts_cselect_b32 %0, %4, %5\n\ts_cselect_b32 %1, %6, %7" : "=&s"(a_x), "=&s"(a_g) : "s"(a_d), "s"(a_th), "s"(a_sl), "s"(a_tl), "s"(a_gp | 0x10000u), "s"(a_gp) : "scc");
    SB(); CVT(tc[0], acc[2], 0);
    mf32z(tr, ta, idn); SB();
    a_x = (a_x + (unsigned)a_d) & ~1u; asm volatile("" : "+s"(a_x)); SB();
    bq[0] = a_x; bq[1] = a_g; bq[2] = 0u; bq[3] = 0u; asm volatile("" : "=v"(bq[2]), "=v"(bq[3])); PIN_V(bq); SB();
    POS(54); __builtin_amdgcn_raw_buffer_store_b128(bq, rs, an_st, sl, 16); SB(); CVT(tc[1], acc[2], 1);
    POS(55); CVT(tc[2], acc[3], 0);
    asm volatile("" :: "v"(bq));
    POS(56); CVT(tc[3], acc[3], 1); CVT(X[0][0], tr, 0);
    POS(57); CVT(X[0][1], tr, 1);
    mf32z(ts, tc, idn); SB(); B_LO(0, 0); B_LO(0, 1);
    POS(58); B_HI(0, 0);
    POS(59); B_HI(0, 1);
    mf16z(dm, X[0], X[0]); SB(); CVT(X[1][0], ts, 0); CVT(X[1][1], ts, 1);
    mf32z(dx, kf[0], bq); SB(); CVT(ta[0], acc[4], 0); CVT(ta[1], acc[4], 1); CVT(ta[2], acc[5], 0); CVT(ta[3], acc[5], 1);
    B_LO(1, 0); B_LO(1, 1);
    mf32z(tr, ta, idn); SB(); B_HI(1, 0); B_HI(1, 1);
    mf16(dm, X[1], X[1]); SB();
    mf32(dx, kf[1], bq); SB();
    CVT(X[2][0], tr, 0); CVT(X[2][1], tr, 1); B_LO(2, 0); B_LO(2, 1);
    mf16(dm, X[2], X[2]); SB(); B_HI(2, 0); B_HI(2, 1);
    mf32_n(dx, kf[2], bq); SB();
    STAMP(3);
    NOPS(2);
    LDS16(ex_w + 1024) = __builtin_bit_cast(u32x4, dm);
    NOPS(3);
    LDS16(ex_w) = __builtin_bit_cast(u32x4, dx);
    WG_BARRIER();
    STAMP(4);
    if (SM_W == 4 || w < SM_W) {
      f32x4 e[4]; float em[4];
#pragma unroll
      for (int k = 0; k < 4; ++k) e[k] = __builtin_bit_cast(f32x4, LDS16(ex_r + k * 2048));
#pragma unroll
      for (int k = 0; k < 4; ++k) em[k] = *(LAS float*)(lds + ex_m + k * 2048);
      SB();
      Softmax sm;
      sm.s = (e[0] + e[1]) + (e[2] + e[3]);
      const f32x2 u = (f32x2){em[0], em[1]} + (f32x2){em[2], em[3]};
      sm.m = u[0] + u[1];
      sm.a(); sm.b(); sm.c();
      const u32x4 v = sm.d();
#pragma unroll
      for (int k = 0; k < 8 / SM_W - 1; ++k) __builtin_amdgcn_raw_buffer_store_b128(v, rs, p_st, sp + k * 4096u, 16);
      ST16(v, rs, p_st, sp + (8 / SM_W - 1) * 4096u);
    }
    SB();
    STAMP(5);
    u32x2 an; unsigned jv;
    asm volatile("s_cmp_lg_u32 %7, 3\n\ts_cbranch_scc1 1f\n\tbuffer_load_dwordx2 %0, %2, %4, %5 offen sc1\n\tbuffer_load_dword %1, %3, %4, %6 offen sc1\n1:"
                 : "=&v"(an), "=&v"(jv) : "v"(an_ld), "v"(jp_vo), "s"(rs), "s"(sl), "s"(jhave < NSEG ? 0u : FAR), "s"(w) : "scc", "memory");
    SB();
    {
      f32x4 d[5];
      mf16z(d[0], lo2(kf[0]), lo2(qf[0])); mf16z(d[1], lo2(kf[0]), hi2(qf[0])); SB();
      ST16(ptk, rp, pt_st, s > 0 ? (unsigned)(s - 1) * PT_SEG : FAR); SB();
      mf16z(d[2], hi2(kf[0]), hi2(qf[0])); mf16z(d[3], X[0], lo2(qf[0])); mf16z(d[4], X[0], hi2(qf[0])); SB();
      __builtin_amdgcn_raw_ptr_buffer_atomic_add_i32(1, rs, l16 << 20, (w == 2 && s > 1) ? OFS_CW + CW_SS + (unsigned)(s - 2) * 128u : FAR, 0);
      SB();
#pragma unroll
      for (int p = 1; p < 3; ++p) {
        mf16(d[0], lo2(kf[p]), lo2(qf[p])); mf16(d[1], lo2(kf[p]), hi2(qf[p])); mf16(d[2], hi2(kf[p]), hi2(qf[p])); mf16(d[3], X[p], lo2(qf[p])); mf16(d[4], X[p], hi2(qf[p]));
      }
      NOPS(3);
#pragma unroll
      for (int t = 0; t < 5; ++t) { LDS16(S_ET + w * 5120 + t * 1024 + lane * 16) = __builtin_bit_cast(u32x4, d[t]); SB(); }
    }
    const unsigned mT0 = mT;
    if (w == 3) {
      asm volatile("s_waitcnt vmcnt(2)" : "+v"(an), "+v"(jv));
      an = ann_valid(an, rs, an_ld, sl, dead);
      sd.next(an, a);
      jv = max(min(jv, dppu<0x4E>(jv)), jhave); jv = min(jv, dppu<0xB1>(jv));
      extra = !first && s > 1 ? min(extra + 24u, 240u) : extra - (extra != 0u);
      PIN_S(extra);
      ck.edge(); ck.rate();
      const unsigned cold = s < 1 ? 400u : 0u;
      a_tl = ck.re + s_mulshr16(a.tail, ck.sig) + cold; a_sl = ck.re + a.slow;
      a_gp = a.g2t + s_mulshr16(a.g2c, ck.sig) + extra + cold; a_th = sd.tA - 112u - ck.re;
      if (l16 == 0u) *(LAS u32x2*)(lds + S_CT) = (u32x2){ck.local(sd.T), jv};
    }
    STAMP(6);
    load_qk(s + 1 < NSEG ? s + 1 : s);
    WG_BARRIER();
    {
      const int b = w >> 1;
      f32x4 e[4]; float em[4];
      const u32x2 ct = *(LAS u32x2*)(lds + S_CT);
#pragma unroll
      for (int k = 0; k < 4; ++k) {
        e[k] = __builtin_bit_cast(f32x4, LDS16(S_ET + k * 5120 + (b ? 1 + hf : 0) * 1024 + (sg * 16 + cq) * 16));
        em[k] = *(LAS float*)(lds + S_ET + k * 5120 + (3 + b) * 1024 + ((2 + (sk >> 2)) * 16 + cq) * 16 + (sk & 3) * 4);
      }
      mT = RFL(ct[0]); jhave = max(jhave, RFL(ct[1]));
      f32x4 es = (e[0] + e[1]) + (e[2] + e[3]);
      PIN_V(es);
#pragma unroll
      for (int r = 0; r < 4; ++r) tk.s[r] = 16 * hf + 4 * sg + r <= 8 * w + sr ? es[r] : -INFINITY;
      tk.m = (em[0] + em[1]) + (em[2] + em[3]);
      PIN_V(tk.s); PIN_V(tk.m);
    }
    STAMP(7);
    STAMP_FLUSH(s, mT0, sd.T, first ? 0u : 1u);
  }
  tk.a(); tk.b(); tk.c();
  ST16(tk.d(), rp, pt_st, (unsigned)(NSEG - 1) * PT_SEG);
  asm volatile("s_waitcnt vmcnt(0)" ::: "memory");
  WG_BARRIER();
  __builtin_amdgcn_raw_ptr_buffer_atomic_add_i32(1, rs, l16 << 20, w < 2 ? OFS_CW + CW_SS + (unsigned)(NSEG - 2 + w) * 128u : FAR, 0);
}

constexpr int O_M = 0, O_RED = O_M + 10240;
#define P_GAP 11
#define OM_LAG 64u
__device__ __forceinline__ void out_role(const RecArgs& a, LAS char* lds, int rep, int k, unsigned posM, unsigned posP) {
  const int lane = threadIdx.x & 63, w = __builtin_amdgcn_readfirstlane(threadIdx.x >> 6), c = lane & 15, g = lane >> 4, hf = c >> 3, i8 = c & 7;
  const int i = rep * 10 + k, iod = rep >> 2, ct = i >> 1, o = i & 1;
  const __amdgpu_buffer_rsrc_t rs = RSRC(a.sync, SYNC_LAYER), ry = RSRC(a.y, Y_IMG), rz = RSRC(a.z, Z_IMG);
  const unsigned l16 = lane * 16;
  Clock ck; ck.init();
  const int t0 = 8 * iod + 2 * w, t2 = 8 * (1 - iod) + 2 * w;
  u32x8 UV[8];
  {
    const __amdgpu_buffer_rsrc_t rw = RSRC(a.uf, 80 * 16 * 20480);
    static_for<40>([&](auto F) {
      constexpr int f = decltype(F)::value, sl = f / 10, L = f % 10;
      const unsigned so = (unsigned)(i * 16 + (sl < 2 ? t0 : t2) + (sl & 1)) * 20480u + L * 2048u;
      if (f < 32) { ld_a<8 * (f & 31)>(rw, l16, so); ld_a<8 * (f & 31) + 4>(rw, l16, so + 1024u); VM_WAIT(40); }
      else UV[f & 7] = cat2(LDP16(rw, l16, so), LDP16(rw, l16 + 1024, so));
    });
    VM_WAIT(0);
#pragma unroll
    for (int f = 0; f < 8; ++f) PIN_V(UV[f]);
  }
  const int sidx = hf ? 0xEEEE : 0x4444;
  const int hq = 2 * ((g < 2 ? t0 : t2) + (g & 1)) + hf;
  const unsigned y_vo = hf * 40960u + g * 256u + o * 128u + i8 * 16u;
  const unsigned z_vo = hq * 256u + o * 128u + i8 * 16u;
  const unsigned p_vo = OFS_P + posP * 4096u + l16, pm_vo = OFS_P + 4 * ROW + posP * 4096u + hq * 128u + i8 * 16u;
  const unsigned pA = (unsigned)(t0 >> 2) * ROW + (t0 & 3) * 1024u, pB = (unsigned)(t2 >> 2) * ROW + (t2 & 3) * 1024u;
  const unsigned m_vo = posM * 4096u + l16;
  const unsigned an_ld = lane < 32 ? ((lane >> 4) * 2 + 1) * ROW + posM * 4096u + 1024u + c * 128u : OOB;
  const int Lm = i >> 3;
  const unsigned m_st = ((Lm / 5) * 2 + (Lm % 5 == 4)) * ROW + (lane >> 3) * 4096u + (Lm % 5 & 3) * 1024u + (i & 7) * 128u + i8 * 16u;
  const unsigned mc1 = w == 0 ? ROW : 2 * ROW + (w - 1) * 1024u, mc2 = w == 0 ? 2 * ROW + 3072u : 3 * ROW;
  unsigned mo[3], ml[3];
#pragma unroll
  for (int q = 0; q < 3; ++q) {
    const bool skip = w + 4 * q >= 10 || (w + 4 * q == Lm && (lane >> 3) == (i & 7));
    mo[q] = skip ? OOB : m_vo; ml[q] = skip ? O_RED + 2304 + (lane & 31) * 16 : O_M + (w + 4 * q) * 1024 + lane * 16;
  }
  const unsigned own_l = lane < 8 ? O_M + i * 128 + lane * 16 : O_RED + 2304 + (lane & 31) * 16;
  if (w == 0) LDS16(own_l) = LD16(rs, m_st, 0);
  const unsigned rz_vo = w < 2 ? OFS_P + ((2 * k + w) >> 2) * ROW + posP * 4096u + ((2 * k + w) & 3) * 1024u + l16
                       : w == 2 ? ((k / 5) * 2 + (k % 5 == 4)) * ROW + (k % 5 & 3) * 1024u + m_vo
                       : k < 4 ? ((k >> 1) * 2 + 1) * ROW + 1024u + (k & 1) * 1024u + m_vo : OOB;
  int rz_b = w == 3 ? 2 : 1; unsigned rz_m = w < 2 ? 1u : 3u, rz_k = w < 2 ? P_SLOT : M_SLOT;
  asm volatile("" : "+s"(rz_b), "+s"(rz_m), "+s"(rz_k));
  const unsigned red_w = (g >> 1) == hf ? O_RED + i8 * 272 + (g & 1) * 128 + (w * 2 + hf) * 8 : O_RED + 2304 + lane * 8;
  const unsigned red_r = O_RED + i8 * 272 + g * 64 + hf * 32;
  const unsigned jp_vo = lane < 4 ? OFS_CW + CW_JP + (unsigned)(40 + rep * 8 + (16 * k + (lane & 1) * 15) / 20) * 128u : OOB;
  unsigned jhave = 0;
  bool dead = false;
  ck.edge(); ck.rate();
  Sched sd; sd.T = ck.global(mtc()); sd.tA = sd.T; sd.tP = sd.T; sd.dur = a.dur0;
  STAMP_DECL(8);
  u32x4 zf = {0u, 0u, 0u, 0u};
  auto step = [&](int s, auto LAST) {
    constexpr bool last = decltype(LAST)::value;
    const unsigned sl = (unsigned)(s & 3) * M_SLOT;
    const unsigned mT = ck.local(sd.T);
    need_progress(rs, jp_vo, (unsigned)s + 1u, jhave);
    static_for<8>([&](auto K) { ck.rho_step<decltype(K)::value>(); });
    const unsigned mA = ck.local(sd.tA);
    STAMP(0);
    coarse_until(ck.local(sd.T + OM_LAG));
    SB();
    u32x4 c0 = LD16(rs, mo[0], sl + w * 1024u), c1 = LD16(rs, mo[1], sl + mc1), c2 = LD16(rs, mo[2], sl + mc2);
    SB();
    u32x4 yf[4];
    if constexpr (!last) {
      const unsigned ys = (unsigned)(s * NH * 40 + ct) * 1024u;
#pragma unroll
      for (int q = 0; q < 4; ++q) { NOPS(7); yf[q] = LD16(ry, y_vo, ys + ((q < 2 ? t0 : t2) + (q & 1)) * 81920u); }
    }
    SB();
    ST16(zf, rz, z_vo, s < 1 ? FAR : (unsigned)((s - 1) * 40 + ct) * 8192u);
    unsigned jv = LD4(rs, jp_vo, jhave < NSEG ? 0u : FAR);
    f32x4 za[4];
#pragma unroll
    for (int t = 0; t < 4; ++t) zero4(za[t]);
    unsigned sv = SENT; PIN_V(sv);
    u32x4 pz = {sv, sv, sv, sv}; PIN_V(pz);
    SB();
    unsigned fails = 0;
    if (__builtin_expect(__any(mx3(mx4(c0), mx4(c1), mx4(c2)) == SENT), 0)) {
      fails = 1;
      for (unsigned spins = 0; !dead;) {
        __builtin_amdgcn_s_sleep(4);
        const bool r0 = mx4(c0) == SENT, r1 = mx4(c1) == SENT, r2 = mx4(c2) == SENT;
        const u32x4 n0 = LD16(rs, r0 ? m_vo : OOB, sl + w * 1024u), n1 = LD16(rs, r1 ? m_vo : OOB, sl + mc1), n2 = LD16(rs, r2 ? m_vo : OOB, sl + mc2);
        if (r0) c0 = n0;
        if (r1) c1 = n1;
        if (r2) c2 = n2;
        if (!__any(mx3(mx4(c0), mx4(c1), mx4(c2)) == SENT)) break;
        if (++spins > SPIN_CAP) dead = true;
        asm volatile("" ::: "memory");
      }
    }
    LDS16(ml[0]) = c0; LDS16(ml[1]) = c1; LDS16(ml[2]) = c2;
    WG_BARRIER();
    STAMP(1);
    {
      u32x4 mf[3];
      mf[0] = LDS16(O_M + l16); mf[1] = LDS16(O_M + 1024 + l16);
      ST16(pz, rs, rz_vo, s < rz_b ? FAR : ((unsigned)(s - rz_b) & rz_m) * rz_k);
      SB();
      static_for<10>([&](auto LL) {
        constexpr int L = decltype(LL)::value;
        smf_a<8 * L>(za[0], mf[L % 3], sidx); SB();
        if (L + 2 < 10) mf[(L + 2) % 3] = LDS16(O_M + (L + 2) * 1024 + l16);
        SB();
        smf_a<80 + 8 * L>(za[1], mf[L % 3], sidx);
        smf_a<160 + 8 * L>(za[2], mf[L % 3], sidx);
        if (L < 2) smf_a<240 + 8 * (L & 1)>(za[3], mf[L % 3], sidx); else smf_v(za[3], mf[L % 3], UV[L < 2 ? 0 : L - 2], sidx);
        SB();
      });
    }
    STAMP(2);
    u32x2 an;
    if constexpr (!last) { coarse_until(mA); SB(); an = LD8(rs, an_ld, sl); } else NOPS(7);
    SB();
    {
      float u[8];
#pragma unroll
      for (int q = 0; q < 8; ++q) { auto x = __builtin_amdgcn_permlane32_swap(f2u(za[q >> 2][q & 3]), f2u(za[2 + (q >> 2)][q & 3]), false, false); u[q] = u2f(x[0]) + u2f(x[1]); }
      auto x = __builtin_amdgcn_permlane16_swap(pack2(u[0], u[1]), pack2(u[4], u[5]), false, false);
      auto y = __builtin_amdgcn_permlane16_swap(pack2(u[2], u[3]), pack2(u[6], u[7]), false, false);
      zf = (u32x4){x[0], y[0], x[1], y[1]};
      PIN_V(zf);
    }
    if constexpr (last) { ST16(zf, rz, z_vo, (unsigned)(s * 40 + ct) * 8192u); return; }
    SB();
    ck.edge();
    const unsigned sp = (unsigned)(s & 1) * P_SLOT, sn = (unsigned)((s + 1) & 3) * M_SLOT;
    asm volatile("" :: "s"(sp + pA), "s"(sp + pB), "s"(sp), "s"(sn), "s"(ck.me), "s"(ck.re));
    SB();
    an = ann_valid(an, rs, an_ld, sl, dead);
    sd.next(an, a);
    const unsigned mP = ck.local(sd.tP);
    STAMP(3);
    SB();
    wait_until(mP);
    SB();
    u32x4 pf[5];
    pf[0] = LD16(rs, p_vo, sp + pA); NOPS(P_GAP); pf[1] = LD16(rs, p_vo, sp + pA + 1024u); NOPS(P_GAP);
    pf[2] = LD16(rs, p_vo, sp + pB); NOPS(P_GAP); pf[3] = LD16(rs, p_vo, sp + pB + 1024u); NOPS(P_GAP);
    pf[4] = LD16(rs, pm_vo, sp); SB();
    asm volatile("" :: "s"(sd.T), "s"(sd.tA), "s"(sd.dur));
    jv = min(jv, dppu<0xB1>(jv)); jhave = max(jhave, RFL(jv));
    STAMP(4);
    SB();
    f32x4 d;
    unsigned chk;
    mf32z(d, yf[0], pf[0]); SB(); chk = max(pf[0][0], pf[0][2]); PIN_V(chk); SB();
    mf32(d, yf[1], pf[1]); SB(); chk = mx3(chk, pf[1][0], pf[1][2]); PIN_V(chk); SB();
    mf32(d, yf[2], pf[2]); SB(); chk = mx3(chk, pf[2][0], pf[2][2]); PIN_V(chk); SB();
    mf32(d, yf[3], pf[3]); SB(); chk = mx3(chk, pf[3][0], pf[3][2]); PIN_V(chk); SB();
    mf32(d, zf, pf[4]); SB(); chk = mx3(chk, pf[4][0], pf[4][2]);
    if (__builtin_expect(__any(chk == SENT), 0)) {
      fails |= 2;
      for (unsigned spins = 0; !dead;) {
        __builtin_amdgcn_s_sleep(2);
        bool nd[5]; u32x4 n[5]; unsigned m = 0;
#pragma unroll
        for (int q = 0; q < 5; ++q) { nd[q] = mx4(pf[q]) == SENT; n[q] = LD16(rs, nd[q] ? (q < 4 ? p_vo : pm_vo) : OOB, sp + (q < 2 ? pA : q < 4 ? pB : 0u) + (q & 1) * 1024u); }
#pragma unroll
        for (int q = 0; q < 5; ++q) { if (nd[q]) pf[q] = n[q]; m = max(m, mx4(pf[q])); }
        if (!__any(m == SENT)) break;
        if (++spins > SPIN_CAP) dead = true;
        asm volatile("" ::: "memory");
      }
#pragma unroll
      for (int q = 0; q < 5; ++q) PIN_V(pf[q]);
      NOPS(1);
      mf32z(d, yf[0], pf[0]); mf32(d, yf[1], pf[1]); mf32(d, yf[2], pf[2]); mf32(d, yf[3], pf[3]); mf32(d, zf, pf[4]);
      NOPS(7);
    }
    STAMP(5);
    NOPS(1);
    *(LAS f32x2*)(lds + red_w) = (f32x2){d[0], d[1]}; *(LAS f32x2*)(lds + red_w + 64) = (f32x2){d[2], d[3]};
    WG_BARRIER();
    if (w == 0) {
      const f32x4 e = __builtin_bit_cast(f32x4, LDS16(red_r)) + __builtin_bit_cast(f32x4, LDS16(red_r + 16));
      const f32x2 u = (f32x2){e[0], e[1]} + (f32x2){e[2], e[3]};
      unsigned o0, o1, o2, o3;
      asm volatile("s_nop 1\n\tv_add_f32_dpp %0, %4, %4 row_ror:8 row_mask:0xf bank_mask:0xf\n\tv_add_f32_dpp %1, %5, %5 row_ror:8 row_mask:0xf bank_mask:0xf\n\t"
                   "v_cvt_pk_bf16_f32 %2, %0, %1\n\tv_min_u32 %0, -2, %2\n\tv_min_u32 %1, -2, %2\n\ts_nop 1\n\tv_permlane16_swap_b32 %0, %1\n\t"
                   "v_mov_b32 %2, %0\n\tv_mov_b32 %3, %1\n\ts_nop 0\n\tv_permlane32_swap_b32 %0, %2\n\tv_permlane32_swap_b32 %1, %3"
                   : "=&v"(o0), "=&v"(o1), "=&v"(o2), "=&v"(o3) : "v"(u[0]), "v"(u[1]));
      ST16(((u32x4){o0, o1, o2, o3}), rs, m_st, sn);
      LDS16(own_l) = (u32x4){o0, o1, o2, o3};
    } else {
      __builtin_amdgcn_s_sleep(3);
      __builtin_amdgcn_raw_ptr_buffer_atomic_add_i32(1, rs, l16 << 20, (w == 3 && s > 0) ? OFS_CW + CW_SO + rep * 128u + (unsigned)(s - 1) * 1024u : FAR, 0);
    }
    SB();
    STAMP(6);
    STAMP_FLUSH(s, mT, mP, fails);
  };
#pragma unroll 1
  for (int s = 0; s < NSEG - 1; ++s) step(s, std::false_type());
  step(NSEG - 1, std::true_type());
  VM_WAIT(0);
  WG_BARRIER();
  __builtin_amdgcn_raw_ptr_buffer_atomic_add_i32(1, rs, l16 << 20, w < 2 ? OFS_CW + CW_SO + rep * 128u + (unsigned)(NSEG - 2 + w) * 1024u : FAR, 0);
}

#define J_PACE 0u
template <bool YO>
__device__ __forceinline__ void proj_role(const RecArgs& a, LAS char* lds, int jn) {
  const int lane = threadIdx.x & 63, w = __builtin_amdgcn_readfirstlane(threadIdx.x >> 6), c = lane & 15, g = lane >> 4;
  const __amdgpu_buffer_rsrc_t rs = RSRC(a.sync, SYNC_LAYER), rx = RSRC(a.xf, S_ * DM * 2), ro = YO ? RSRC(a.y, Y_IMG) : RSRC(a.qk, 2 * QK_IMG);
  const unsigned l16 = lane * 16;
#define DMA_P(seg, p) dma1k<((p) & 3) * 1024>(rx, l16, (unsigned)(seg) * 40960u + w * 10240u + ((p) >> 2) * 4096u, (unsigned)(uintptr_t)lds + ((seg) & 1) * 40960u + w * 10240u + ((p) >> 2) * 4096u)
  static_for<10>([&](auto P_) { DMA_P(0, decltype(P_)::value); });
  const int T0 = 20 * (YO ? jn - 40 : jn) + 5 * w;
  u32x4 WV[36];
  {
    const __amdgpu_buffer_rsrc_t rw = YO ? RSRC(a.uf, 80 * 16 * 20480) : RSRC(a.wf, 768 * 20480);
    static_for<100>([&](auto F) {
      constexpr int f = decltype(F)::value;
      const unsigned so = (unsigned)T0 * 20480u + f * 1024u;
      if (f < 64) { ld_a<4 * (f & 63)>(rw, l16, so); VM_WAIT(24); } else WV[f < 64 ? 0 : f - 64] = LDP16(rw, l16, so);
    });
#pragma unroll
    for (int f = 0; f < 36; ++f) PIN_V(WV[f]);
  }
  const unsigned o_vo = YO ? (c >> 3) * 40960u + g * 256u + (c & 7) * 16u : l16;
  unsigned o_so[5];
#pragma unroll
  for (int t = 0; t < 5; ++t) {
    const unsigned T = T0 + t;
    o_so[t] = YO ? (T & 15u) * 81920u + (T >> 5) * 1024u + ((T >> 4) & 1u) * 128u : T < 384u ? T * 1024u : T < 768u ? QK_IMG + (T - 384u) * 1024u : FAR;
  }
  unsigned tnext = mtc();
  u32x4 pend[5];
  auto put = [&](int s, int t) { ST16(pend[t], ro, o_vo, o_so[t] + (unsigned)s * (YO ? NH * 40960u : 384u * 1024u)); };
#pragma unroll 1
  for (int s = 0; s < NSEG; ++s) {
    VM_WAIT(0);
    WG_BARRIER();
    const bool lag = s > 3;
    __builtin_amdgcn_raw_buffer_store_b32(lag ? (unsigned)s - 1u : (unsigned)s, rs, l16 << 20, (w == 0 && s > 0) ? OFS_CW + CW_JP + jn * 128u : FAR, 16);
    if (J_PACE && s >= 6) coarse_until(tnext);
    if (J_PACE) tnext = mtc() + J_PACE;
    const int sx = s + 1 < NSEG ? s + 1 : s - 1, sp = lag ? s - 1 : (int)(FAR / (YO ? NH * 40960u : 384u * 1024u));
    const unsigned xb = (s & 1) * 40960 + l16;
    f32x4 acc[10]; u32x4 xr[3][2];
#pragma unroll
    for (int kb = 0; kb < 2; ++kb) { xr[kb][0] = LDS16(xb + kb * 1024); xr[kb][1] = LDS16(xb + 20480 + kb * 1024); }
    SB();
    static_for<20>([&](auto KB) {
      constexpr int kb = decltype(KB)::value;
      static_for<10>([&](auto Q) {
        constexpr int q = decltype(Q)::value, f = (q >> 1) * 20 + kb;
        if (f < 64) {
          if (kb == 0) { if (YO) mf32z_ba<4 * (f & 63)>(acc[q], xr[0][q & 1]); else mf32z_aa<4 * (f & 63)>(acc[q], xr[0][q & 1]); }
          else { if (YO) mf32_ba<4 * (f & 63)>(acc[q], xr[kb % 3][q & 1]); else mf32_aa<4 * (f & 63)>(acc[q], xr[kb % 3][q & 1]); }
        } else {
          const u32x4 wv = WV[f < 64 ? 0 : f - 64], xv = xr[kb % 3][q & 1];
          if (kb == 0) { if (YO) mf32z(acc[q], xv, wv); else mf32z(acc[q], wv, xv); }
          else { if (YO) mf32(acc[q], xv, wv); else mf32(acc[q], wv, xv); }
        }
        SB();
        if (q >= 2 && q < 4 && kb + 2 < 20) xr[(kb + 2) % 3][q & 1] = LDS16(xb + (q & 1) * 20480 + (kb + 2) * 1024);
        if constexpr (q == 5 && kb < 10) DMA_P(sx, kb);
        if constexpr (q == 7 && kb % 3 == 1 && kb < 15) put(sp, kb / 3);
        SB();
      });
    });
    NOPS(3);
#pragma unroll
    for (int t = 0; t < 5; ++t) { pend[t] = pack8(acc[2 * t], acc[2 * t + 1]); PIN_V(pend[t]); SB(); }
    if (s < 3 || s == NSEG - 1) {
#pragma unroll
      for (int t = 0; t < 5; ++t) put(s, t);
    }
  }
  VM_WAIT(0);
  WG_BARRIER();
  __builtin_amdgcn_raw_buffer_store_b32((unsigned)NSEG, rs, l16 << 20, w == 0 ? OFS_CW + CW_JP + jn * 128u : FAR, 16);
}

constexpr int T_LD = 84;
#define T_FLY 0
__device__ __forceinline__ void token_role(const RecArgs& a, LAS char* lds, int rep) {
  const int tid = threadIdx.x, w = __builtin_amdgcn_readfirstlane(tid >> 6);
  const __amdgpu_buffer_rsrc_t rs = RSRC(a.sync, SYNC_LAYER), ry = RSRC(a.y, Y_IMG), rz = RSRC(a.z, Z_IMG), rp = RSRC(a.pt, PT_IMG);
  LAS float* part = (LAS float*)lds;
  LAS unsigned* tick = (LAS unsigned*)(lds + 4 * 32 * T_LD * 4);
  auto wait_count = [&](unsigned off, unsigned want) {
    if (w == 0) {
      for (unsigned spins = 0; spins < SPIN_CAP; ++spins) {
        if (RFL(LD4(rs, off, 0)) >= want) break;
        __builtin_amdgcn_s_sleep(12);
        asm volatile("" ::: "memory");
      }
    }
    WG_BARRIER();
  };
#pragma unroll 1
  for (;;) {
    WG_BARRIER();
    if (tid == 0) *tick = __hip_atomic_fetch_add((GAS unsigned*)(a.sync + OFS_CW + CW_TQ + rep * 128), 1u, RLX_AGENT);
    WG_BARRIER();
    const int n = __builtin_amdgcn_readfirstlane(*(volatile LAS unsigned*)tick);
    if (n >= 64) return;
    const int s = rep + 8 * (n >> 3), cg = n & 7;
    const bool calm = s < NSEG - 3;
    int lane = tid & 63; PIN_V(lane);
    const int c = lane & 15, g = lane >> 4;
    u32x4 xo[2];
#pragma unroll
    for (int r = 0; r < 2; ++r) { const int p = r * 256 + tid; if (p < 320) xo[r] = *(const u32x4*)(a.xold + (size_t)(s * 32 + p / 10) * DM + 80 * cg + (p % 10) * 8); }
    wait_count(OFS_CW + CW_SO + (s * 8 + cg) * 128, 10u);
    const unsigned l16 = lane * 16;
    static_for<50>([&](auto F) {
      constexpr int f = decltype(F)::value, kk = f / 5, t = f % 5;
      if (kk < 8) ld_a1<4 * f>(ry, l16, (unsigned)((s * NH + 8 * w + kk) * 40 + 5 * cg + t) * 1024u);
      else ld_a1<4 * f>(rz, l16, (unsigned)((s * 40 + 5 * cg + t) * NH + 8 * w + 4 * (kk - 8)) * 256u);
      if (t == 4 && calm) VM_WAIT(T_FLY);
    });
    VM_WAIT(40);
    wait_count(OFS_CW + CW_SS + s * 128, 32u);
    u32x4 bf[10][2];
#pragma unroll
    for (int kk = 0; kk < 10; ++kk)
#pragma unroll
      for (int b = 0; b < 2; ++b) {
        bf[kk][b] = LD16(rp, l16, (unsigned)s * PT_SEG + (kk < 8 ? (unsigned)((8 * w + kk) * 2 + b) * 1024u : PT_M + (unsigned)(b * NH + 8 * w + 4 * (kk - 8)) * 256u));
        if (b == 1 && (kk & 1) && calm) VM_WAIT(T_FLY);
      }
    f32x4 acc[5][2];
    static_for<100>([&](auto N) {
      constexpr int n = decltype(N)::value, kk = n / 10, t = (n % 10) >> 1, b = n & 1;
      if (kk == 0) mf32z_aa<4 * t>(acc[t][b], bf[0][b]); else mf32_aa<4 * (5 * kk + t)>(acc[t][b], bf[kk][b]);
    });
    NOPS(7);
#pragma unroll
    for (int t = 0; t < 5; ++t)
#pragma unroll
      for (int b = 0; b < 2; ++b) {
        LAS float* p = part + (w * 32 + 16 * b + c) * T_LD + 16 * t + 8 * (g & 1) + 2 * (g >> 1);
        *(LAS f32x2*)p = (f32x2){acc[t][b][0], acc[t][b][1]}; *(LAS f32x2*)(p + 4) = (f32x2){acc[t][b][2], acc[t][b][3]};
      }
    WG_BARRIER();
#pragma unroll
    for (int r = 0; r < 2; ++r) {
      const int p = r * 256 + tid;
      if (p < 320) {
        const LAS float* q = part + (p / 10) * T_LD + (p % 10) * 8;
        f32x4 s0 = *(const LAS f32x4*)q, s1 = *(const LAS f32x4*)(q + 4);
#pragma unroll
        for (int k = 1; k < 4; ++k) { s0 += *(const LAS f32x4*)(q + k * 32 * T_LD); s1 += *(const LAS f32x4*)(q + k * 32 * T_LD + 4); }
        u32x4 o;
#pragma unroll
        for (int k = 0; k < 2; ++k) {
          o[k] = pack2(bf2f(xo[r][k] & 0xffff) + rbf(s0[2 * k]), bf2f(xo[r][k] >> 16) + rbf(s0[2 * k + 1]));
          o[2 + k] = pack2(bf2f(xo[r][2 + k] & 0xffff) + rbf(s1[2 * k]), bf2f(xo[r][2 + k] >> 16) + rbf(s1[2 * k + 1]));
        }
        *(u32x4*)(a.xnew + (size_t)(s * 32 + p / 10) * DM + 80 * cg + (p % 10) * 8) = o;
      }
    }
  }
}

__global__ __launch_bounds__(256, 1) void rec_kernel(RecArgs a) {
  __shared__ __attribute__((aligned(16))) char lds_[S_END];
  LAS char* lds = (LAS char*)lds_;
  const int b = blockIdx.x, rep = b & 7, il = b >> 3;
  AGPR_FENCE();
  if (il < 14) {
    const int lane = threadIdx.x & 63;
    const unsigned* tab = (const unsigned*)a.cal;
    const unsigned x = tab[64 + (lane >> 3)] & 7u, xr = tab[64 + rep] & 7u;
    unsigned v = tab[(lane & 56) + (((((x & 1u) << 2) | (x >> 1)) ^ lane) & 7u)] & 0x1ffffu;
    v += __shfl_xor(v, 8); v += __shfl_xor(v, 16); v += __shfl_xor(v, 32);
    v = v * 8u + (lane & 7);
    v = min(v, (unsigned)__shfl_xor(v, 1)); v = min(v, (unsigned)__shfl_xor(v, 2)); v = min(v, (unsigned)__shfl_xor(v, 4));
    const unsigned posP = RFL(((((xr & 1u) << 2) | (xr >> 1)) ^ v ^ (unsigned)a.layer) & 7u), posM = posP ^ 4u;
    if (il < 4) score_role(a, lds, rep, il, posM, posP); else out_role(a, lds, rep, il - 4, posM, posP);
    return;
  }
  if (il < 19) { if (b != 19 * 8 - 1) proj_role<false>(a, lds, b - 14 * 8); }
  else if (il < 27) proj_role<true>(a, lds, 40 + rep * 8 + il - 19);
  AGPR_FENCE();
  token_role(a, lds, rep);
}

constexpr int KQ = S_ / 4;
__global__ __launch_bounds__(1024, 4) void attb_kernel(const u16* __restrict__ Qp, const u16* __restrict__ Kp, const u16* __restrict__ VbT,
                                                       u16* __restrict__ O) {
  __shared__ float mrg_[4][3][2][13][64];
  const int lane = threadIdx.x & 63, wv = __builtin_amdgcn_readfirstlane(threadIdx.x >> 6), kh = wv >> 2, r = lane & 31, hh = lane >> 5;
  float (*mrg)[2][13][64] = mrg_[wv & 3];
  const int hd = (blockIdx.x & 7) * 4 + ((blockIdx.x >> 3) & 3), qb = blockIdx.x >> 5;
  const int q0 = qb * 256 + (wv & 3) * 64;
  u32x4 qf[2][2];
#pragma unroll
  for (int qt = 0; qt < 2; ++qt)
#pragma unroll
    for (int ks = 0; ks < 2; ++ks) qf[qt][ks] = *(const u32x4*)(Qp + ((size_t)hd * S_ + q0 + qt * 32 + r) * 32 + 16 * ks + 8 * hh);
  const u16* kp = Kp + ((size_t)hd * S_ + kh * KQ + r) * 32 + 8 * hh;
  const u16* vp = VbT + (((size_t)hd * 64 + kh * (KQ / 32)) * 2 * 64 + lane) * 8;
  f32x16 o[2];
#pragma unroll
  for (int qt = 0; qt < 2; ++qt)
#pragma unroll
    for (int i = 0; i < 16; ++i) o[qt][i] = 0.f;
  float m[2] = {0.f, 0.f};
  bf16x8 kv[2][4];
  kv[0][0] = *(const bf16x8*)(kp); kv[0][1] = *(const bf16x8*)(kp + 16); kv[0][2] = *(const bf16x8*)(vp); kv[0][3] = *(const bf16x8*)(vp + 512);
#pragma unroll 1
  for (int k2 = 0; k2 < KQ; k2 += 64) {
#pragma unroll
    for (int u = 0; u < 2; ++u) {
      const int k0 = k2 + 32 * u;
      {
        const int kn = (k0 + 32 < KQ) ? k0 + 32 : k0;
        kv[u ^ 1][0] = *(const bf16x8*)(kp + (size_t)kn * 32); kv[u ^ 1][1] = *(const bf16x8*)(kp + (size_t)kn * 32 + 16);
        kv[u ^ 1][2] = *(const bf16x8*)(vp + (size_t)kn * 32); kv[u ^ 1][3] = *(const bf16x8*)(vp + (size_t)kn * 32 + 512);
      }
      f32x16 sa[2];
#pragma unroll
      for (int qt = 0; qt < 2; ++qt) {
#pragma unroll
        for (int i = 0; i < 16; ++i) sa[qt][i] = 0.f;
        sa[qt] = __builtin_amdgcn_mfma_f32_32x32x16_bf16(kv[u][0], __builtin_bit_cast(bf16x8, qf[qt][0]), sa[qt], 0, 0, 0);
        sa[qt] = __builtin_amdgcn_mfma_f32_32x32x16_bf16(kv[u][1], __builtin_bit_cast(bf16x8, qf[qt][1]), sa[qt], 0, 0, 0);
      }
#pragma unroll
      for (int qt = 0; qt < 2; ++qt) {
        float mx = fmx(fmx(sa[qt][0], sa[qt][1]), sa[qt][2]);
#pragma unroll
        for (int i = 3; i < 15; i += 2) mx = fmx(fmx(mx, sa[qt][i]), sa[qt][i + 1]);
        mx = fmx(mx, sa[qt][15]);
        if (__builtin_expect(__any(mx > 6.f) || k0 == 0, 0)) {
          { auto rr = __builtin_amdgcn_permlane32_swap(f2u(mx), f2u(mx), false, false); mx = fmx(u2f(rr[0]), u2f(rr[1])); }
          if (k0 != 0) mx = fmx(mx, 0.f);
          const float mn = m[qt] + mx, hi = rbf(mn), lo = rbf(mn - hi), me = hi + lo;
          const float delta = me - m[qt], alpha = k0 == 0 ? 1.0f : __builtin_amdgcn_exp2f(-delta);
          m[qt] = me;
          if (hh == 0) qf[qt][1][2] = pack2(-hi, -lo);
#pragma unroll
          for (int i = 0; i < 16; ++i) { sa[qt][i] -= delta; o[qt][i] *= alpha; }
        }
        float p[16];
#pragma unroll
        for (int i = 0; i < 16; ++i) p[i] = __builtin_amdgcn_exp2f(sa[qt][i]);
        bf16x8 p0 = mk8(pack2(p[0], p[1]), pack2(p[2], p[3]), pack2(p[4], p[5]), pack2(p[6], p[7]));
        bf16x8 p1 = mk8(pack2(p[8], p[9]), pack2(p[10], p[11]), pack2(p[12], p[13]), pack2(p[14], p[15]));
        o[qt] = __builtin_amdgcn_mfma_f32_32x32x16_bf16(kv[u][2], p0, o[qt], 0, 0, 0);
        o[qt] = __builtin_amdgcn_mfma_f32_32x32x16_bf16(kv[u][3], p1, o[qt], 0, 0, 0);
      }
    }
  }
  if (kh > 0) {
#pragma unroll
    for (int qt = 0; qt < 2; ++qt) {
#pragma unroll
      for (int i = 0; i < 12; ++i) mrg[kh - 1][qt][i][lane] = o[qt][i];
      mrg[kh - 1][qt][12][lane] = m[qt];
    }
  }
  __syncthreads();
  if (kh == 0) {
#pragma unroll
    for (int qt = 0; qt < 2; ++qt) {
      float m1 = mrg[0][qt][12][lane], m2 = mrg[1][qt][12][lane], m3 = mrg[2][qt][12][lane];
      float mn = fmaxf(fmaxf(m[qt], m1), fmaxf(m2, m3));
      float a0 = __builtin_amdgcn_exp2f(m[qt] - mn), a1 = __builtin_amdgcn_exp2f(m1 - mn);
      float a2 = __builtin_amdgcn_exp2f(m2 - mn), a3 = __builtin_amdgcn_exp2f(m3 - mn);
      float v[12];
#pragma unroll
      for (int i = 0; i < 12; ++i) v[i] = (o[qt][i] * a0 + mrg[0][qt][i][lane] * a1) + (mrg[1][qt][i][lane] * a2 + mrg[2][qt][i][lane] * a3);
      auto lr = __builtin_amdgcn_permlane32_swap(f2u(v[8]), f2u(v[8]), false, false);
      float inv = 1.0f / u2f(lr[1]);
      u16* op = O + (size_t)(q0 + qt * 32 + r) * DM + hd * BD + 4 * hh;
      u32x2 x0 = {pack2(v[0] * inv, v[1] * inv), pack2(v[2] * inv, v[3] * inv)};
      *(u32x2*)(op) = x0;
      u32x2 x1 = {pack2(v[4] * inv, v[5] * inv), pack2(v[6] * inv, v[7] * inv)};
      *(u32x2*)(op + 8) = x1;
      if (hh == 0) {
        u32x2 x2 = {pack2(v[8] * inv, v[9] * inv), pack2(v[10] * inv, v[11] * inv)};
        *(u32x2*)(op + 16) = x2;
      }
    }
  }
}

#define P_TAIL 1300
#define P_D0 140
#define P_G2C 900
#define P_G2T 300
#define P_DUR0 1000
#define P_LEAD 260
#define P_SLOW 900
extern "C" void kernel_launch(void* const* d_in, const int* in_sizes, int n_in, void* d_out, int out_size, void* d_ws,
                              size_t ws_size, hipStream_t stream) {
  const u16* A_x = (const u16*)d_in[0];
  const u16* A_mem0 = (const u16*)d_in[1];
  const u16* A_Wq = (const u16*)d_in[2];
  const u16* A_Wk = (const u16*)d_in[3];
  const u16* A_Wv = (const u16*)d_in[4];
  const u16* A_Wo = (const u16*)d_in[5];
  const u16* B_wqkv = (const u16*)d_in[8];
  const u16* B_wo = (const u16*)d_in[9];
  char* ws = (char*)(((uintptr_t)d_ws + ((size_t)2 << 20) - 1) & ~(uintptr_t)(((size_t)2 << 20) - 1));
  u16* WF = (u16*)(ws + OFF_WF); u16* UF = (u16*)(ws + OFF_UF);
  u16* WqkvBT = (u16*)(ws + OFF_WQKVBT); u16* WoBT = (u16*)(ws + OFF_WOBT);
  u16* xf = (u16*)(ws + OFF_XF); u16* xn = (u16*)(ws + OFF_XN); u16* xa = (u16*)(ws + OFF_XA); u16* xb = (u16*)(ws + OFF_XB);
  float* part = (float*)(ws + OFF_PART);
  u16* Qb = (u16*)(ws + OFF_QB); u16* Kb = (u16*)(ws + OFF_KB); u16* VbT = (u16*)(ws + OFF_VBT); u16* Ob = (u16*)(ws + OFF_OB);
  {
    ProArgs pa;
    TrJobs& jb = pa.jobs;
    int t0 = 0;
    auto add = [&](int i, const u16* s, u16* d, int R, int C, int frag) { jb.j[i].src = s; jb.j[i].dst = d; jb.j[i].R = R; jb.j[i].C = C; jb.j[i].tile0 = t0; jb.j[i].frag = frag; t0 += (R / 64) * (C / 64); };
    add(0, A_Wq, WF, DM, HD, 1);
    add(1, A_Wk, WF + (size_t)HD * DM, DM, HD, 1);
    add(2, B_wqkv, WqkvBT, DM, 3 * DM, 0);
    add(3, B_wo, WoBT, DM, DM, 0);
    pa.x = A_x; pa.xf = xf; pa.qb = Qb; pa.sync = ws; pa.wo = A_Wo; pa.wv = A_Wv; pa.uf = UF; pa.mem0 = A_mem0;
    pa.pad = 0; pa.nu = NH * 8;
    pa.ntr = t0; pa.npad = (BH * 64 * 2 * 24 + 255) / 256; pa.nsync = (int)(2 * SYNC_LAYER / 16384); pa.nln = S_ / 4;
    prologue_kernel<<<8 + pa.nu + pa.ntr + pa.npad + pa.nsync + pa.nln, 256, 0, stream>>>(pa);
  }
  const u16* xcur = A_x;
  for (int layer = 0; layer < 2; ++layer) {
    {
      RecArgs r{WF, UF, xf, ws + OFF_QK, ws + OFF_Y, ws + OFF_Z, ws + OFF_PT, ws + (size_t)layer * SYNC_LAYER, ws + OFS_CAL + ROW, xcur, xa, nullptr,
                layer, P_TAIL, P_D0, P_G2C, P_G2T, P_DUR0, P_LEAD, P_SLOW};
      rec_kernel<<<256, 256, 0, stream>>>(r);
    }
    resln_kernel<<<S_ / 4, 256, 0, stream>>>(xa, part, 0, nullptr, xn, 0);
    {
      GemmArgs g{xn, WqkvBT, Qb, Kb, VbT, DM, DM, DM, 3 * DM / 64, S_ / 128, (S_ / 128) * (3 * DM / 64) / 8};
      gemm_kernel<EPI_QKV_B, 64><<<(S_ / 128) * (3 * DM / 64), 256, 0, stream>>>(g);
    }
    attb_kernel<<<BH * 8, 1024, 0, stream>>>(Qb, Kb, VbT, Ob);
    {
      GemmArgs g{Ob, WoBT, part, nullptr, nullptr, DM, DM, DM / 2, DM / 64, S_ / 128, (S_ / 128) * (DM / 64) * 2 / 8};
      gemm_kernel<EPI_PART, 64><<<(S_ / 128) * (DM / 64) * 2, 256, 0, stream>>>(g);
    }
    if (layer == 0) {
      resln_kernel<<<S_ / 4, 256, 0, stream>>>(xa, part, 2, xb, xf, 1);
      xcur = xb;
    } else {
      resln_kernel<<<S_ / 4, 256, 0, stream>>>(xa, part, 2, (u16*)d_out, nullptr, 0);
    }
  }
}
```

```cpp
#include <hip/hip_runtime.h>
#include <hip/hip_bf16.h>
#include <stdint.h>
#include <type_traits>
#include <utility>

typedef unsigned short u16;
typedef unsigned long long u64;
typedef __attribute__((ext_vector_type(8))) short bf16x8;
typedef __attribute__((ext_vector_type(4))) short bf16x4;
typedef __attribute__((ext_vector_type(4))) float f32x4;
typedef __attribute__((ext_vector_type(2))) float f32x2;
typedef __attribute__((ext_vector_type(16))) float f32x16;
typedef __attribute__((ext_vector_type(4))) unsigned u32x4;
typedef __attribute__((ext_vector_type(2))) unsigned u32x2;
#define GAS __attribute__((address_space(1)))
#define LAS __attribute__((address_space(3)))

constexpr int S_ = 2048, DM = 640;
constexpr int NH = 32, DH = 192, HD = NH * DH;
constexpr int MEM = 8, SEG = 32, NSEG = S_ / SEG;
constexpr int BH = 32, BD = 20;

constexpr size_t al256(size_t x) { return (x + 255) & ~(size_t)255; }
constexpr unsigned ROW = 32768;
constexpr unsigned M_SLOT = 4 * ROW;
constexpr unsigned OFS_P = 16 * ROW;
constexpr unsigned P_SLOT = 5 * ROW;
constexpr unsigned OFS_CAL = 26 * ROW;
constexpr unsigned OFS_CW = 28 * ROW;
constexpr unsigned CW_JP = 0;
constexpr unsigned CW_SS = CW_JP + 104 * 128;
constexpr unsigned CW_SO = CW_SS + 64 * 128;
constexpr unsigned CW_TQ = CW_SO + 512 * 128;
constexpr unsigned CW_END = CW_TQ + 8 * 128;
static_assert(CW_END <= 4 * ROW, "control words");
constexpr unsigned SYNC_LAYER = 32 * ROW;
constexpr size_t OFF_WF = 2 * (size_t)SYNC_LAYER;
constexpr size_t OFF_UF = OFF_WF + (size_t)768 * 20480;
constexpr size_t OFF_WQKVBT = OFF_UF + (size_t)80 * 16 * 20480;
constexpr size_t OFF_WOBT = OFF_WQKVBT + al256((size_t)3 * DM * DM * 2);
constexpr size_t OFF_XF = OFF_WOBT + al256((size_t)DM * DM * 2);
constexpr size_t OFF_XN = OFF_XF + al256((size_t)S_ * DM * 2);
constexpr size_t OFF_XA = OFF_XN + al256((size_t)S_ * DM * 2);
constexpr size_t OFF_XB = OFF_XA + al256((size_t)S_ * DM * 2);
constexpr unsigned QK_IMG = NSEG * NH * 12 * 1024;
constexpr size_t OFF_QK = OFF_XB + al256((size_t)S_ * DM * 2);
constexpr unsigned Y_IMG = NSEG * NH * 40 * 1024;
constexpr size_t OFF_Y = OFF_QK + 2 * (size_t)QK_IMG;
constexpr unsigned Z_IMG = NSEG * 40 * NH * 256;
constexpr size_t OFF_Z = OFF_Y + Y_IMG;
constexpr unsigned PT_M = NH * 2048, PT_SEG = PT_M + 2 * NH * 256, PT_IMG = NSEG * PT_SEG;
constexpr size_t OFF_PT = OFF_Z + Z_IMG;
constexpr size_t OFF_QB = OFF_PT + PT_IMG;
constexpr size_t OFF_KB = OFF_QB + al256((size_t)BH * S_ * 32 * 2);
constexpr size_t OFF_VBT = OFF_KB + al256((size_t)BH * S_ * 32 * 2);
constexpr size_t WS_TOTAL = OFF_VBT + al256((size_t)BH * S_ * 32 * 2);
constexpr size_t OFF_PART = OFF_Y;
constexpr size_t OFF_OB = OFF_QK;
static_assert(WS_TOTAL + ((size_t)2 << 20) <= ((size_t)256 << 20), "workspace budget");

__device__ __forceinline__ float u2f(unsigned v) { return __builtin_bit_cast(float, v); }
__device__ __forceinline__ unsigned f2u(float v) { return __builtin_bit_cast(unsigned, v); }
__device__ __forceinline__ float bf2f(unsigned v) { return __builtin_bit_cast(float, v << 16); }
__device__ __forceinline__ unsigned f2bf(float f) { __bf16 b = (__bf16)f; return (unsigned)__builtin_bit_cast(u16, b); }
__device__ __forceinline__ float rbf(float f) { return bf2f(f2bf(f)); }
typedef __attribute__((ext_vector_type(2))) __bf16 hbf16x2;
__device__ __forceinline__ unsigned pack2(float lo, float hi) { f32x2 v = {lo, hi}; hbf16x2 b = __builtin_convertvector(v, hbf16x2); return __builtin_bit_cast(unsigned, b); }
__device__ __forceinline__ f32x4 mfma32(bf16x8 a, bf16x8 b, f32x4 c) { return __builtin_amdgcn_mfma_f32_16x16x32_bf16(a, b, c, 0, 0, 0); }
__device__ __forceinline__ f32x4 mfma16(bf16x4 a, bf16x4 b, f32x4 c) { return __builtin_amdgcn_mfma_f32_16x16x16bf16_1k(a, b, c, 0, 0, 0); }
typedef __attribute__((ext_vector_type(8))) unsigned u32x8;
typedef __attribute__((ext_vector_type(8))) __bf16 hbf16x8;
typedef __attribute__((ext_vector_type(16))) __bf16 hbf16x16;
template <typename R>
__device__ __forceinline__ void store16_(u32x4 v, R r, unsigned vo, unsigned so, bool sc1) {
  if (sc1) __builtin_amdgcn_raw_buffer_store_b128(v, r, vo, so, 16); else __builtin_amdgcn_raw_buffer_store_b128(v, r, vo, so, 0);
  asm volatile("s_nop 1" :: "v"(v));
}
#define store16(v, r, vo, so, aux) store16_((v), (r), (vo), (so), (aux) != 0)
__device__ __forceinline__ bf16x4 mk4(unsigned lo, unsigned hi) { u32x2 v = {lo, hi}; return __builtin_bit_cast(bf16x4, v); }
__device__ __forceinline__ bf16x8 mk8(unsigned a, unsigned b, unsigned c, unsigned d) { u32x4 v = {a, b, c, d}; return __builtin_bit_cast(bf16x8, v); }
__device__ __forceinline__ unsigned dpp_ror8(unsigned v) { return (unsigned)__builtin_amdgcn_update_dpp(0, (int)v, 0x128, 0xf, 0xf, false); }

__device__ __forceinline__ void resln_rows(int blk, const u16* __restrict__ xold, const float* __restrict__ part, int nsplit,
                                           u16* __restrict__ xnew, u16* __restrict__ xn, int frag) {
  const int row = blk * 4 + (threadIdx.x >> 6), lane = threadIdx.x & 63;
  const bool two = lane < 16;
  float v[16];
#pragma unroll
  for (int hf = 0; hf < 2; ++hf) {
    if (hf == 1 && !two) {
#pragma unroll
      for (int i = 0; i < 8; ++i) v[8 + i] = 0.f;
      continue;
    }
    const int col = (hf * 64 + lane) * 8;
    u32x4 xo = *(const u32x4*)(xold + (size_t)row * DM + col);
    float a[8];
#pragma unroll
    for (int i = 0; i < 4; ++i) { unsigned u = xo[i]; a[2 * i] = bf2f(u & 0xffff); a[2 * i + 1] = bf2f(u >> 16); }
    if (nsplit > 0) {
      f32x4 s0 = {0.f, 0.f, 0.f, 0.f}, s1 = {0.f, 0.f, 0.f, 0.f};
      for (int s = 0; s < nsplit; ++s) {
        const float* pp = part + ((size_t)s * S_ + row) * DM + col;
        s0 += *(const f32x4*)pp; s1 += *(const f32x4*)(pp + 4);
      }
#pragma unroll
      for (int i = 0; i < 4; ++i) { a[i] = rbf(a[i] + rbf(s0[i])); a[4 + i] = rbf(a[4 + i] + rbf(s1[i])); }
      if (xnew) {
        u32x4 o = {pack2(a[0], a[1]), pack2(a[2], a[3]), pack2(a[4], a[5]), pack2(a[6], a[7])};
        *(u32x4*)(xnew + (size_t)row * DM + col) = o;
      }
    }
#pragma unroll
    for (int i = 0; i < 8; ++i) v[hf * 8 + i] = a[i];
  }
  if (!xn) return;
  float s = 0.f;
#pragma unroll
  for (int i = 0; i < 16; ++i) s += v[i];
#pragma unroll
  for (int o = 32; o >= 1; o >>= 1) s += __shfl_xor(s, o);
  const float mean = s * (1.0f / DM);
  float q = 0.f;
#pragma unroll
  for (int i = 0; i < 8; ++i) { float d = v[i] - mean; q += d * d; }
  if (two) {
#pragma unroll
    for (int i = 8; i < 16; ++i) { float d = v[i] - mean; q += d * d; }
  }
#pragma unroll
  for (int o = 32; o >= 1; o >>= 1) q += __shfl_xor(q, o);
  const float var = rbf(q * (1.0f / DM));
  const float mb = rbf(mean);
  const float den = rbf(sqrtf(rbf(var + 1e-5f)));
#pragma unroll
  for (int hf = 0; hf < 2; ++hf) {
    if (hf == 1 && !two) continue;
    const int ch = hf * 64 + lane;
    float y[8];
#pragma unroll
    for (int i = 0; i < 8; ++i) y[i] = rbf(v[hf * 8 + i] - mb) / den;
    u32x4 o = {pack2(y[0], y[1]), pack2(y[2], y[3]), pack2(y[4], y[5]), pack2(y[6], y[7])};
    if (frag) *(u32x4*)(xn + ((((size_t)(row >> 4)) * 20 + (ch >> 2)) * 64 + (ch & 3) * 16 + (row & 15)) * 8) = o;
    else *(u32x4*)(xn + (size_t)row * DM + ch * 8) = o;
  }
}

__global__ __launch_bounds__(256) void resln_kernel(const u16* __restrict__ xold, const float* __restrict__ part, int nsplit,
                                                    u16* __restrict__ xnew, u16* __restrict__ xn, int frag) {
  resln_rows(blockIdx.x, xold, part, nsplit, xnew, xn, frag);
}
__device__ __forceinline__ void pad_images(u16* __restrict__ qb, int blk) {
  u16* kb = qb + (size_t)BH * S_ * 32; u16* vbt = kb + (size_t)BH * S_ * 32;
  const int i = blk * 256 + threadIdx.x;
  if (i < BH * S_) {
    u32x2 z = {0u, 0u};
#pragma unroll
    for (int k = 0; k < 3; ++k) {
      *(u32x2*)(qb + (size_t)i * 32 + 20 + 4 * k) = z;
      *(u32x2*)(kb + (size_t)i * 32 + 20 + 4 * k) = (u32x2){k == 0 ? 0x3f803f80u : 0u, 0u};
    }
  }
  if (i < BH * 64 * 2 * 24) {
    int blk = i / 24, r = i - blk * 24;
    int e = 20 + (r % 12), hh = r / 12;
    unsigned f = (e == 20) ? 0x3f803f80u : 0u;
    u32x4 z = {f, f, f, f};
    *(u32x4*)(vbt + ((size_t)blk * 64 + e + 32 * hh) * 8) = z;
  }
}

enum { EPI_PART = 1, EPI_QKV_B = 2 };
struct GemmArgs {
  const u16* A; const u16* Bt; void* o0; void* o1; void* o2;
  int lda; int ldb; int Ksplit; int ntn;
  int ntm; int nblk8;
};
template <int EPI, int BN>
__global__ __launch_bounds__(256, 2) void gemm_kernel(GemmArgs g) {
  constexpr int NT = BN / 32;
  __shared__ __attribute__((aligned(16))) u16 smem[2][(128 + BN) * 64];
  const int tid = threadIdx.x, lane = tid & 63, w = tid >> 6;
  const int wr = w >> 1, wc = w & 1, c = lane & 15, gq = lane >> 4;
  int b = blockIdx.x;
  if (g.nblk8) b = (b & 7) * g.nblk8 + (b >> 3);
  const int tiles = g.ntm * g.ntn;
  const int split = b / tiles; b -= split * tiles;
  const int bm = b % g.ntm, bn = b / g.ntm;
  const u16* Ag = g.A + (size_t)bm * 128 * g.lda + (size_t)split * g.Ksplit;
  const u16* Bg = g.Bt + (size_t)bn * BN * g.ldb + (size_t)split * g.Ksplit;
  const int nk = g.Ksplit / 64;
  auto stage = [&](int buf, int kt) {
#pragma unroll
    for (int i = 0; i < 4; ++i) {
      int p = i * 256 + tid, row = p >> 3, ch = (p & 7) ^ (row & 7);
      __builtin_amdgcn_global_load_lds((const unsigned*)(Ag + (size_t)row * g.lda + kt * 64 + ch * 8),
                                       (__attribute__((address_space(3))) unsigned*)&smem[buf][p * 8], 16, 0, 0);
    }
#pragma unroll
    for (int i = 0; i < BN / 32; ++i) {
      int p = i * 256 + tid, row = p >> 3, ch = (p & 7) ^ (row & 7);
      __builtin_amdgcn_global_load_lds((const unsigned*)(Bg + (size_t)row * g.ldb + kt * 64 + ch * 8),
                                       (__attribute__((address_space(3))) unsigned*)&smem[buf][128 * 64 + p * 8], 16, 0, 0);
    }
  };
  f32x4 acc[4][NT];
#pragma unroll
  for (int m = 0; m < 4; ++m)
#pragma unroll
    for (int n = 0; n < NT; ++n) acc[m][n] = (f32x4){0.f, 0.f, 0.f, 0.f};
  bool swapped;
  if (EPI == EPI_QKV_B) swapped = (bn * BN < 2 * DM);
  else swapped = true;
  stage(0, 0);
  for (int kt = 0; kt < nk; ++kt) {
    int cur = kt & 1;
    asm volatile("s_waitcnt vmcnt(0)" ::: "memory");
    __syncthreads();
    if (kt + 1 < nk) stage(cur ^ 1, kt + 1);
    const u16* sa = smem[cur];
    const u16* sb = smem[cur] + 128 * 64;
#pragma unroll
    for (int kk = 0; kk < 2; ++kk) {
      bf16x8 af[4], bfr[NT];
#pragma unroll
      for (int m = 0; m < 4; ++m) {
        int row = wr * 64 + m * 16 + c;
        af[m] = *(const bf16x8*)(sa + row * 64 + (((kk * 4 + gq) ^ (row & 7)) * 8));
      }
#pragma unroll
      for (int n = 0; n < NT; ++n) {
        int row = wc * (BN / 2) + n * 16 + c;
        bfr[n] = *(const bf16x8*)(sb + row * 64 + (((kk * 4 + gq) ^ (row & 7)) * 8));
      }
      if (swapped) {
#pragma unroll
        for (int m = 0; m < 4; ++m)
#pragma unroll
          for (int n = 0; n < NT; ++n) acc[m][n] = mfma32(bfr[n], af[m], acc[m][n]);
      } else {
#pragma unroll
        for (int m = 0; m < 4; ++m)
#pragma unroll
          for (int n = 0; n < NT; ++n) acc[m][n] = mfma32(af[m], bfr[n], acc[m][n]);
      }
    }
  }
  const int row0 = bm * 128 + wr * 64, col0 = bn * BN + wc * (BN / 2);
  if (EPI == EPI_PART) {
    float* dst = (float*)g.o0 + (size_t)split * S_ * DM;
#pragma unroll
    for (int m = 0; m < 4; ++m)
#pragma unroll
      for (int n = 0; n < NT; ++n)
        *(f32x4*)(dst + (size_t)(row0 + m * 16 + c) * DM + col0 + n * 16 + gq * 4) = acc[m][n];
  } else {
    if (swapped) {
#pragma unroll
      for (int m = 0; m < 4; ++m)
#pragma unroll
        for (int n = 0; n < NT; ++n) {
          int nn = col0 + n * 16 + gq * 4;
          u16* dst = (nn < DM) ? (u16*)g.o0 : (u16*)g.o1;
          int cc = (nn < DM) ? nn : nn - DM;
          int hd = cc / BD, e = cc - hd * BD;
          const float qs = (nn < DM) ? 0.22360679774997896f * 1.4426950408889634f : 1.0f;
          u32x2 v = {pack2(acc[m][n][0] * qs, acc[m][n][1] * qs), pack2(acc[m][n][2] * qs, acc[m][n][3] * qs)};
          *(u32x2*)(dst + ((size_t)hd * S_ + row0 + m * 16 + c) * 32 + e) = v;
        }
    } else {
      u16* dst = (u16*)g.o2;
#pragma unroll
      for (int m = 0; m < 4; ++m)
#pragma unroll
        for (int n = 0; n < NT; ++n) {
          int cc = col0 + n * 16 + c - 2 * DM;
          int hd = cc / BD, e = cc - hd * BD;
          int t0 = row0 + m * 16;
          u32x2 v = {pack2(acc[m][n][0], acc[m][n][1]), pack2(acc[m][n][2], acc[m][n][3])};
          *(u32x2*)(dst + ((((size_t)hd * 64 + (t0 >> 5)) * 2 + ((t0 >> 4) & 1)) * 64 + e + 32 * (gq & 1)) * 8 + (gq >> 1) * 4) = v;
        }
    }
  }
}

__device__ __forceinline__ bf16x4 lds_tr(const u16* p) { return __builtin_amdgcn_ds_read_tr16_b64_v4i16((LAS bf16x4*)p); }
__device__ __forceinline__ bf16x8 cat8(bf16x4 a, bf16x4 b) { return __builtin_shufflevector(a, b, 0, 1, 2, 3, 4, 5, 6, 7); }
__device__ __forceinline__ unsigned rtc() { return (unsigned)__builtin_amdgcn_s_memrealtime(); }
__device__ __forceinline__ unsigned mtc() { return (unsigned)__builtin_amdgcn_s_memtime(); }
constexpr unsigned OOB = 0x80000000u;
constexpr unsigned FAR = 0x40000000u;
constexpr unsigned SENT = 0xffffffffu;
struct TrJob { const u16* src; u16* dst; int R; int C; int tile0; int frag; };
struct TrJobs { TrJob j[4]; };
__device__ __forceinline__ void transpose_tile(const TrJobs& jobs, int b, u16 (*tile)[66]) {
  int ji = 0;
#pragma unroll
  for (int i = 1; i < 4; ++i) if (b >= jobs.j[i].tile0) ji = i;
  const u16* src = jobs.j[ji].src; u16* dst = jobs.j[ji].dst;
  const int R = jobs.j[ji].R, C = jobs.j[ji].C, frag = jobs.j[ji].frag;
  const int t = b - jobs.j[ji].tile0, tc = C / 64;
  const int r0 = (t / tc) * 64, c0 = (t % tc) * 64;
  const int tid = threadIdx.x;
#pragma unroll
  for (int i = 0; i < 2; ++i) {
    const int p = i * 256 + tid, r = p >> 3, c = (p & 7) * 8;
    bf16x8 v = *(const bf16x8*)(src + (size_t)(r0 + r) * C + c0 + c);
#pragma unroll
    for (int k = 0; k < 8; ++k) tile[r][c + k] = (u16)v[k];
  }
  __syncthreads();
#pragma unroll
  for (int i = 0; i < 2; ++i) {
    const int p = i * 256 + tid;
    const int c = frag ? (p >> 7) * 16 + (p & 15) : p >> 3, r = frag ? ((p >> 4) & 7) * 8 : (p & 7) * 8;
    bf16x8 v;
#pragma unroll
    for (int k = 0; k < 8; ++k) v[k] = (short)tile[r + k][c];
    if (frag) *(bf16x8*)(dst + ((size_t)((c0 >> 4) + (p >> 7)) * (R >> 5) + (r0 >> 5)) * 512 + (p & 127) * 8) = v;
    else *(bf16x8*)(dst + (size_t)(c0 + c) * R + r0 + r) = v;
  }
}
struct ProArgs { TrJobs jobs; const u16* x; u16* xf; u16* qb; char* sync; const u16* wo; const u16* wv; u16* uf; const u16* mem0;
                 int ntr; int npad; int nsync; int nln; int nu; int pad; };
#define CAL_N 4
constexpr int WO_LD = 88;
__global__ __launch_bounds__(256, 2) void prologue_kernel(ProArgs a) {
  __shared__ __attribute__((aligned(16))) u16 smem[192 * WO_LD];
  int b = blockIdx.x;
  const int tid = threadIdx.x, lane = tid & 63, wq = __builtin_amdgcn_readfirstlane(tid >> 6), c = lane & 15, g = lane >> 4;
  if (b < 8) {
    const __amdgpu_buffer_rsrc_t rs = __builtin_amdgcn_make_buffer_rsrc(a.sync, 0, (int)SYNC_LAYER, 0x00020000);
    if (wq) return;
    unsigned best = 65535u;
    const u32x4 z = {0u, 0u, 0u, 0u};
    const unsigned st = OFS_CAL + b * 256u + (lane >> 3) * 2048u + (lane & 7) * 16u;
    store16(z, rs, st, 0, 16); store16(z, rs, st, 16384, 16);
#pragma unroll 1
    for (int r = 0; r < CAL_N; ++r) {
      store16(z, rs, st, ((r + 1) & 1) * 128u, 16); store16(z, rs, st, 16384u + ((r + 1) & 1) * 128u, 16);
      asm volatile("s_waitcnt vmcnt(0)" ::: "memory");
#pragma unroll 1
      for (int q = 0; q < 16; ++q) {
        unsigned t0, t1, x;
        const unsigned vo = OFS_CAL + (unsigned)(b * 2 + (r & 1)) * 128u + q * 2048u;
        asm volatile("s_memtime vcc\n\ts_waitcnt lgkmcnt(0)\n\ts_mov_b32 %0, vcc_lo\n\tbuffer_load_dword %2, %3, %4, 0 offen sc1\n\ts_waitcnt vmcnt(0)\n\t"
                     "s_memtime vcc\n\ts_waitcnt lgkmcnt(0)\n\ts_mov_b32 %1, vcc_lo" : "=&s"(t0), "=&s"(t1), "=&v"(x) : "v"(vo), "s"(rs) : "vcc", "memory");
        if (lane == q) best = min(best, t1 - t0);
      }
    }
    unsigned* tab = (unsigned*)(a.sync + OFS_CAL + ROW);
    best += __shfl_xor(best, 1);
    if (lane < 16 && !(lane & 1)) tab[b * 8 + (lane >> 1)] = best;
    if (tid == 0) tab[64 + b] = (unsigned)__builtin_amdgcn_s_getreg(20 | (3 << 11)) & 7u;
    return;
  }
  b -= 8;
  if (b < a.nu) {
    const int h = b >> 3, cg = b & 7;
    const u16* ap = a.wv + (size_t)(160 * wq + 8 * (c >> 2) + (c & 3)) * HD + h * DH + 8 * g;
    bf16x8 af[2][2][6];
    auto load_a = [&](int pr, int buf) {
#pragma unroll
      for (int kp = 0; kp < 6; ++kp) {
        af[buf][0][kp] = *(const bf16x8*)(ap + (size_t)(32 * pr) * HD + 32 * kp);
        af[buf][1][kp] = *(const bf16x8*)(ap + (size_t)(32 * pr + 4) * HD + 32 * kp);
      }
    };
    bf16x8 st[8];
#pragma unroll
    for (int i = 0; i < 8; ++i) {
      const int p = i * 256 + tid, row = p / 10, ch = p - row * 10;
      if (p < 1920) st[i] = *(const bf16x8*)(a.wo + (size_t)(h * DH + row) * DM + 80 * cg + ch * 8);
    }
    load_a(0, 0);
#pragma unroll
    for (int i = 0; i < 8; ++i) {
      const int p = i * 256 + tid, row = p / 10, ch = p - row * 10;
      if (p < 1920) *(bf16x8*)(smem + row * WO_LD + ch * 8) = st[i];
    }
    __syncthreads();
    load_a(1, 1);
    u16* dst = a.uf + ((size_t)((10 * cg + ((c >> 1) & 1)) * 16 + (h >> 1)) * 20 + 5 * wq) * 512 + (g * 16 + (h & 1) * 8 + 2 * (c >> 2) + (c & 1)) * 8;
    const u16* bp = smem + (8 * g + (c >> 2)) * WO_LD + 4 * (c & 3);
    bf16x8 bw[5][6];
#pragma unroll
    for (int t = 0; t < 5; ++t)
#pragma unroll
      for (int kp = 0; kp < 6; ++kp) bw[t][kp] = cat8(lds_tr(bp + 32 * kp * WO_LD + 16 * t), lds_tr(bp + (32 * kp + 4) * WO_LD + 16 * t));
    __builtin_amdgcn_sched_barrier(0);
#pragma unroll
    for (int pr = 0; pr < 5; ++pr) {
#pragma unroll
      for (int t = 0; t < 5; ++t) {
        f32x4 d0 = {0.f, 0.f, 0.f, 0.f}, d1 = {0.f, 0.f, 0.f, 0.f};
#pragma unroll
        for (int kp = 0; kp < 6; ++kp) {
          d0 = mfma32(af[pr & 1][0][kp], bw[t][kp], d0); d1 = mfma32(af[pr & 1][1][kp], bw[t][kp], d1);
        }
        *(u32x4*)(dst + ((size_t)t * 2 * 16 * 20 + pr) * 512) = (u32x4){pack2(d0[0], d0[1]), pack2(d0[2], d0[3]), pack2(d1[0], d1[1]), pack2(d1[2], d1[3])};
        __builtin_amdgcn_sched_barrier(0);
      }
      __builtin_amdgcn_sched_barrier(0);
      if (pr + 2 < 5) load_a(pr + 2, pr & 1);
      __builtin_amdgcn_sched_barrier(0);
    }
    return;
  }
  b -= a.nu;
  if (b < a.nln) { resln_rows(b, a.x, nullptr, 0, nullptr, a.xf, 1); return; }
  b -= a.nln;
  if (b < a.nsync) {
    const __amdgpu_buffer_rsrc_t rs = __builtin_amdgcn_make_buffer_rsrc(a.sync, 0, (int)(2 * SYNC_LAYER), 0x00020000);
#pragma unroll
    for (int i = 0; i < 4; ++i) {
      const unsigned off = (unsigned)b * 16384u + (unsigned)(i * 256 + tid) * 16u;
      const unsigned o = off & (SYNC_LAYER - 1), rr = o >> 15, in = o & 4095u;
      u32x4 z = {SENT, SENT, SENT, SENT};
      if (o >= OFS_CW) z = (u32x4){0u, 0u, 0u, 0u};
      else if (rr < 4 && (!(rr & 1) || in < 1024u)) {
        const int L = 5 * (rr >> 1) + 4 * (rr & 1) + (in >> 10), ln = (in & 1023u) >> 4;
        const unsigned* row = (const unsigned*)(a.mem0 + (ln & 7) * DM + 64 * L + 16 * (ln >> 4) + 2 * ((ln >> 3) & 1));
        z = (u32x4){min(row[0], SENT - 1), min(row[2], SENT - 1), min(row[4], SENT - 1), min(row[6], SENT - 1)};
      }
      store16(z, rs, (o >= OFS_CAL && o < OFS_CW) ? OOB : off, 0, 16);
    }
    return;
  }
  b -= a.nsync;
  if (b < a.ntr) { transpose_tile(a.jobs, b, (u16 (*)[66])smem); return; }
  pad_images(a.qb, b - a.ntr);
}

struct RecArgs {
  const u16* wf; const u16* uf; const u16* xf; char* qk; char* y; char* z; char* pt; char* sync; const char* cal;
  const u16* xold; u16* xnew; unsigned long long* dbg;
  int layer, tail, d0, g2c, g2t, dur0, tol, slow;
};
template <typename F, int... I> __device__ __forceinline__ void static_for_(F&& f, std::integer_sequence<int, I...>) { (f(std::integral_constant<int, I>()), ...); }
template <int N, typename F> __device__ __forceinline__ void static_for(F&& f) { static_for_(f, std::make_integer_sequence<int, N>()); }
#define RLX_AGENT __ATOMIC_RELAXED, __HIP_MEMORY_SCOPE_AGENT
#define SPIN_LOG 20
constexpr unsigned SPIN_CAP = 1u << SPIN_LOG;
#define PIN_A(x) asm volatile("" : "+a"(x))
#define PIN_V(x) asm volatile("" : "+v"(x))
#define PIN_S(x) do { (x) = __builtin_amdgcn_readfirstlane(x); } while (0)
#define SB() __builtin_amdgcn_sched_barrier(0)
#define NOPS(n) do { SB(); asm volatile("s_nop %0" :: "n"(n)); SB(); } while (0)
#define WG_BARRIER() do { asm volatile("s_waitcnt lgkmcnt(0)" ::: "memory"); __builtin_amdgcn_s_barrier(); asm volatile("" ::: "memory"); } while (0)
#define RFL(x) ((unsigned)__builtin_amdgcn_readfirstlane(x))
template <int CTRL> __device__ __forceinline__ unsigned dppu(unsigned v) { return (unsigned)__builtin_amdgcn_update_dpp(0, (int)v, CTRL, 0xf, 0xf, true); }
template <int CTRL> __device__ __forceinline__ float dppf(float v) { return u2f(dppu<CTRL>(f2u(v))); }
template <int BANKS> __device__ __forceinline__ unsigned ror8_banks(unsigned v) { return (unsigned)__builtin_amdgcn_update_dpp((int)v, (int)v, 0x128, 0xf, BANKS, false); }
constexpr int ROR1 = 0x121, ROR2 = 0x122, ROR4 = 0x124, ROR8 = 0x128, SHR8 = 0x118;
__device__ __forceinline__ float fmx(float a, float b) { return __builtin_elementwise_maximum(a, b); }
__device__ __forceinline__ unsigned mx3(unsigned a, unsigned b, unsigned c) { return max(max(a, b), c); }
__device__ __forceinline__ unsigned mx4(u32x4 v) { return max(mx3(v[0], v[1], v[2]), v[3]); }
__device__ __forceinline__ u32x8 cat2(u32x4 lo, u32x4 hi) { return __builtin_shufflevector(lo, hi, 0, 1, 2, 3, 4, 5, 6, 7); }
__device__ __forceinline__ u32x4 lo4(u32x8 v) { return __builtin_shufflevector(v, v, 0, 1, 2, 3); }
__device__ __forceinline__ u32x4 hi4(u32x8 v) { return __builtin_shufflevector(v, v, 4, 5, 6, 7); }
__device__ __forceinline__ u32x2 lo2(u32x4 v) { return __builtin_shufflevector(v, v, 0, 1); }
__device__ __forceinline__ u32x2 hi2(u32x4 v) { return __builtin_shufflevector(v, v, 2, 3); }
__device__ __forceinline__ u32x4 pack8(f32x4 a, f32x4 b) { return (u32x4){pack2(a[0], a[1]), pack2(a[2], a[3]), pack2(b[0], b[1]), pack2(b[2], b[3])}; }
#define DEV __device__ __forceinline__
DEV void smf_v(f32x4& acc, u32x4 a, u32x8 b, int idx) { asm volatile("v_smfmac_f32_16x16x64_bf16 %0, %1, %2, %3" : "+v"(acc) : "v"(a), "v"(b), "v"(idx)); }
template <int R> DEV void smf_a(f32x4& acc, u32x4 a, int idx) { asm volatile("v_smfmac_f32_16x16x64_bf16 %0, %1, a[%c3:%c4], %2" : "+v"(acc) : "v"(a), "v"(idx), "n"(R), "n"(R + 7)); }
template <int R> DEV void mf32_ba(f32x4& acc, u32x4 a) { asm volatile("v_mfma_f32_16x16x32_bf16 %0, %1, a[%c2:%c3], %0" : "+v"(acc) : "v"(a), "n"(R), "n"(R + 3)); }
template <int R> DEV void mf32z_ba(f32x4& d, u32x4 a) { asm volatile("v_mfma_f32_16x16x32_bf16 %0, %1, a[%c2:%c3], 0" : "=&v"(d) : "v"(a), "n"(R), "n"(R + 3)); }
template <int R> DEV void mf32_aa(f32x4& acc, u32x4 b) { asm volatile("v_mfma_f32_16x16x32_bf16 %0, a[%c2:%c3], %1, %0" : "+v"(acc) : "v"(b), "n"(R), "n"(R + 3)); }
template <int R> DEV void mf32z_aa(f32x4& d, u32x4 b) { asm volatile("v_mfma_f32_16x16x32_bf16 %0, a[%c2:%c3], %1, 0" : "=&v"(d) : "v"(b), "n"(R), "n"(R + 3)); }
template <int R> DEV void ld_a(__amdgpu_buffer_rsrc_t r, unsigned vo, unsigned so) { asm volatile("buffer_load_dwordx4 a[%c3:%c4], %0, %1, %2 offen" :: "v"(vo), "s"(r), "s"(so), "n"(R), "n"(R + 3) : "memory"); }
template <int R> DEV void ld_a1(__amdgpu_buffer_rsrc_t r, unsigned vo, unsigned so) { asm volatile("buffer_load_dwordx4 a[%c3:%c4], %0, %1, %2 offen sc1" :: "v"(vo), "s"(r), "s"(so), "n"(R), "n"(R + 3) : "memory"); }
DEV void mf32(f32x4& acc, u32x4 a, u32x4 b) { asm volatile("v_mfma_f32_16x16x32_bf16 %0, %1, %2, %0" : "+v"(acc) : "v"(a), "v"(b)); }
DEV void mf32z(f32x4& d, u32x4 a, u32x4 b) { asm volatile("v_mfma_f32_16x16x32_bf16 %0, %1, %2, 0" : "=&v"(d) : "v"(a), "v"(b)); }
DEV void mf32_n(f32x4& acc, u32x4 a, u32x4 b) { asm volatile("s_nop 1\n\tv_mfma_f32_16x16x32_bf16 %0, %1, %2, %0" : "+v"(acc) : "v"(a), "v"(b)); }
DEV void mf32z_n(f32x4& d, u32x4 a, u32x4 b) { asm volatile("s_nop 1\n\tv_mfma_f32_16x16x32_bf16 %0, %1, %2, 0" : "=&v"(d) : "v"(a), "v"(b)); }
DEV void mf16(f32x4& acc, u32x2 a, u32x2 b) { asm volatile("v_mfma_f32_16x16x16_bf16 %0, %1, %2, %0" : "+v"(acc) : "v"(a), "v"(b)); }
DEV void mf16z(f32x4& d, u32x2 a, u32x2 b) { asm volatile("v_mfma_f32_16x16x16_bf16 %0, %1, %2, 0" : "=&v"(d) : "v"(a), "v"(b)); }
DEV void zero4(f32x4& d) { f32x2 x, y; asm volatile("v_mov_b64 %0, 0\n\tv_mov_b64 %1, 0" : "=v"(x), "=v"(y)); d = __builtin_shufflevector(x, y, 0, 1, 2, 3); asm volatile("" : "+v"(d)); }
DEV void clock_now(u64& t) { asm volatile("s_memtime %0" : "=s"(t)); }
DEV void zero2(f32x2& x) { asm volatile("v_mov_b64 %0, 0" : "=v"(x)); }
DEV int fresh_lane() { int l; asm volatile("v_mbcnt_lo_u32_b32 %0, -1, 0\n\tv_mbcnt_hi_u32_b32 %0, -1, %0" : "=v"(l)); __builtin_assume(l >= 0 && l < 64); return l; }
DEV void pin_s(unsigned& x) { asm volatile("" : "+s"(x)); }
template <typename T> DEV void pin_v(T& x) { asm volatile("" : "+v"(x)); }
#define AGPR_FENCE() asm volatile("" ::: \
    "a0", "a1", "a2", "a3", "a4", "a5", "a6", "a7", "a8", "a9", "a10", "a11", "a12", "a13", "a14", "a15", "a16", "a17", "a18", "a19", "a20", "a21", "a22", \
    "a23", "a24", "a25", "a26", "a27", "a28", "a29", "a30", "a31", "a32", "a33", "a34", "a35", "a36", "a37", "a38", "a39", "a40", "a41", "a42", "a43", \
    "a44", "a45", "a46", "a47", "a48", "a49", "a50", "a51", "a52", "a53", "a54", "a55", "a56", "a57", "a58", "a59", "a60", "a61", "a62", "a63", "a64", \
    "a65", "a66", "a67", "a68", "a69", "a70", "a71", "a72", "a73", "a74", "a75", "a76", "a77", "a78", "a79", "a80", "a81", "a82", "a83", "a84", "a85", \
    "a86", "a87", "a88", "a89", "a90", "a91", "a92", "a93", "a94", "a95", "a96", "a97", "a98", "a99", "a100", "a101", "a102", "a103", "a104", "a105", \
    "a106", "a107", "a108", "a109", "a110", "a111", "a112", "a113", "a114", "a115", "a116", "a117", "a118", "a119", "a120", "a121", "a122", "a123", \
    "a124", "a125", "a126", "a127", "a128", "a129", "a130", "a131", "a132", "a133", "a134", "a135", "a136", "a137", "a138", "a139", "a140", "a141", \
    "a142", "a143", "a144", "a145", "a146", "a147", "a148", "a149", "a150", "a151", "a152", "a153", "a154", "a155", "a156", "a157", "a158", "a159", \
    "a160", "a161", "a162", "a163", "a164", "a165", "a166", "a167", "a168", "a169", "a170", "a171", "a172", "a173", "a174", "a175", "a176", "a177", \
    "a178", "a179", "a180", "a181", "a182", "a183", "a184", "a185", "a186", "a187", "a188", "a189", "a190", "a191", "a192", "a193", "a194", "a195", \
    "a196", "a197", "a198", "a199", "a200", "a201", "a202", "a203", "a204", "a205", "a206", "a207", "a208", "a209", "a210", "a211", "a212", "a213", \
    "a214", "a215", "a216", "a217", "a218", "a219", "a220", "a221", "a222", "a223", "a224", "a225", "a226", "a227", "a228", "a229", "a230", "a231", \
    "a232", "a233", "a234", "a235", "a236", "a237", "a238", "a239", "a240", "a241", "a242", "a243", "a244", "a245", "a246", "a247", "a248", "a249", \
    "a250", "a251", "a252", "a253", "a254", "a255")
#define LDP16(r, off, so) __builtin_amdgcn_raw_buffer_load_b128((r), (off), (so), 0)
#define VM_WAIT(n) asm volatile("s_waitcnt vmcnt(%0)" :: "n"(n) : "memory")
#define LD16(r, off, so) __builtin_amdgcn_raw_buffer_load_b128((r), (off), (so), 16)
#define LD8(r, off, so) __builtin_amdgcn_raw_buffer_load_b64((r), (off), (so), 16)
#define LD4(r, off, so) __builtin_amdgcn_raw_buffer_load_b32((r), (off), (so), 16)
#define ST16(v, r, off, so) store16((v), (r), (off), (so), 16)
#define RSRC(p, n) __builtin_amdgcn_make_buffer_rsrc((void*)(p), 0, (int)(n), 0x00020000)
#define LDS16(off) (*(LAS u32x4*)(lds + (off)))
template <int IMM> DEV void dma1k(__amdgpu_buffer_rsrc_t r, unsigned vo, unsigned so, unsigned ldsoff) {
  asm volatile("s_mov_b32 m0, %0\n\ts_nop 0\n\tbuffer_load_dwordx4 %1, %2, %3 offen offset:%c4 lds" :: "s"(ldsoff), "v"(vo), "s"(r), "s"(so), "n"(IMM) : "memory");
}

DEV int s_clamp(int v, int lim) { int r; asm("s_min_i32 %0, %1, %2\n\ts_max_i32 %0, %0, %3" : "=&s"(r) : "s"(v), "n"(lim), "n"(-lim) : "scc"); return r; }
DEV unsigned s_mulshr16(unsigned x, unsigned y) { unsigned r; asm("s_mul_i32 %0, %1, %2\n\ts_lshr_b32 %0, %0, 16" : "=s"(r) : "s"(x), "s"(y) : "scc"); return r; }
struct Clock {
  unsigned me, re, dm, dt, rho, sig;
  float fm, ft, fx, fy;
  DEV unsigned edge() {
    u64 m, r0, r1, r2, r3, r4, r5, r6, r7; unsigned n;
#define CNT_(k) "s_cmp_eq_u64 %" #k ", %1\n\ts_addc_u32 %9, %9, 0\n\t"
    asm volatile("s_memtime %0\n\ts_memrealtime %1\n\ts_nop 2\n\ts_memrealtime %2\n\ts_nop 2\n\ts_memrealtime %3\n\ts_nop 2\n\ts_memrealtime %4\n\ts_nop 2\n\t"
                 "s_memrealtime %5\n\ts_nop 2\n\ts_memrealtime %6\n\ts_nop 2\n\ts_memrealtime %7\n\ts_nop 2\n\ts_memrealtime %8\n\t"
                 "s_mov_b32 %9, 0\n\ts_waitcnt lgkmcnt(0)\n\t" CNT_(2) CNT_(3) CNT_(4) CNT_(5) CNT_(6) CNT_(7) CNT_(8)
                 : "=&s"(m), "=&s"(r0), "=&s"(r1), "=&s"(r2), "=&s"(r3), "=&s"(r4), "=&s"(r5), "=&s"(r6), "=&s"(r7), "=&s"(n) :: "scc", "memory");
    take(m, r0, n);
    return (unsigned)m;
  }
  DEV void take(u64 m, u64 r0, unsigned n) {
    const unsigned m1 = (unsigned)m + 16u * n + 12u, t1 = ((unsigned)r0 + 4u) * 8u;
    dm = m1 - me; dt = t1 - re; me = m1; re = t1;
  }
  DEV void edge_z(f32x4 (&z)[4], u32x4 x0, u32x4 x1, u32x8 u0, u32x8 u1, int idx) {
    u64 m, r0, r1, r2, r3, r4, r5, r6, r7; unsigned n;
#define EZ_(acc, x, b) "v_smfmac_f32_16x16x64_bf16 %" #acc ", %" #x ", " b ", %18\n\t"
    asm volatile(EZ_(10, 14, "a[64:71]") "s_memtime %0\n\ts_memrealtime %1\n\t" EZ_(11, 14, "a[144:151]") "s_nop 0\n\ts_memrealtime %2\n\t" EZ_(12, 14, "a[224:231]") "s_nop 0\n\ts_memrealtime %3\n\t"
                 EZ_(13, 14, "%16") "s_nop 0\n\ts_memrealtime %4\n\t" EZ_(10, 15, "a[72:79]") "s_nop 0\n\ts_memrealtime %5\n\t" EZ_(11, 15, "a[152:159]") "s_nop 0\n\ts_memrealtime %6\n\t"
                 EZ_(12, 15, "a[232:239]") "s_nop 0\n\ts_memrealtime %7\n\t" EZ_(13, 15, "%17") "s_nop 0\n\ts_memrealtime %8\n\t"
                 "s_mov_b32 %9, 0\n\ts_waitcnt lgkmcnt(0)\n\t" CNT_(2) CNT_(3) CNT_(4) CNT_(5) CNT_(6) CNT_(7) CNT_(8)
                 : "=&s"(m), "=&s"(r0), "=&s"(r1), "=&s"(r2), "=&s"(r3), "=&s"(r4), "=&s"(r5), "=&s"(r6), "=&s"(r7), "=&s"(n), "+v"(z[0]), "+v"(z[1]), "+v"(z[2]), "+v"(z[3])
                 : "v"(x0), "v"(x1), "v"(u0), "v"(u1), "v"(idx) : "scc", "memory");
    take(m, r0, n);
  }
  template <int K> DEV void rate_step() {
    if (K == 0) { fm = (float)dm; pin_v(fm); }
    if (K == 1) { ft = (float)dt; pin_v(ft); }
    if (K == 2) { fy = __builtin_amdgcn_rcpf(fm); pin_v(fy); }
    if (K == 3) { fx = __builtin_amdgcn_rcpf(ft); pin_v(fx); }
    if (K == 4) { fm *= 2048.0f; pin_v(fm); }
    if (K == 5) { ft *= 65536.0f; pin_v(ft); }
    if (K == 6) { fm *= fx; pin_v(fm); }
    if (K == 7) { ft *= fy; pin_v(ft); }
    if (K == 8) { fm = u2f((unsigned)fm); pin_v(fm); }
    if (K == 9) { ft = u2f((unsigned)ft); pin_v(ft); }
    if (K == 10) { dm = RFL(f2u(fm)); asm volatile("" : "+s"(dm)); }
    if (K == 11) { dt = RFL(f2u(ft)); asm volatile("" : "+s"(dt)); }
    if (K == 12) { const bool ok = dm - 12u * 256u < 14u * 256u; rho = ok ? dm : rho; sig = ok ? dt : sig; asm volatile("" : "+s"(rho), "+s"(sig)); }
  }
  template <int K> DEV void rho_step() {
    if (K < 2) rate_step<K>(); else if (K < 4) rate_step<K + 1>(); else if (K < 7) rate_step<2 * K - 2>();
    else { rho = dm - 12u * 256u < 14u * 256u ? dm : rho; asm volatile("" : "+s"(rho)); }
  }
  DEV void rate() { static_for<13>([&](auto K) { rate_step<decltype(K)::value>(); }); }
  DEV void init() { rho = 22u * 256u; sig = 65536u * 8u / 22u; me = 0u; re = 0u; edge(); }
  DEV unsigned local(unsigned t) const { unsigned r = me + (unsigned)((s_clamp((int)(t - re), 100000) * (int)rho) >> 11); PIN_S(r); return r; }
  DEV unsigned global(unsigned m) const { int p = s_clamp((int)(m - me), 40000) * (int)sig; PIN_S(p); p >>= 16; PIN_S(p); return re + (unsigned)p; }
};
__device__ __forceinline__ void wait_until(unsigned tgt) {
  unsigned t;
  asm volatile("1:\n\ts_memtime vcc\n\ts_waitcnt lgkmcnt(0)\n\ts_sub_i32 %0, %1, vcc_lo\n\ts_sub_u32 vcc_hi, %0, 209\n\ts_cmp_lt_u32 vcc_hi, 60000\n\ts_cbranch_scc1 1b\n\t"
               "s_sub_i32 %0, %0, 112\n\ts_max_i32 %0, %0, 0\n\ts_min_i32 %0, %0, 96\n\ts_and_b32 %0, %0, 0x7c\n\t"
               "s_getpc_b64 vcc\n2:\n\ts_sub_u32 %0, 3f-2b, %0\n\ts_add_u32 vcc_lo, vcc_lo, %0\n\ts_addc_u32 vcc_hi, vcc_hi, 0\n\ts_setpc_b64 vcc\n\t"
               ".rept 24\n\ts_nop 0\n\t.endr\n3:" : "=&s"(t) : "s"(tgt) : "vcc", "scc", "memory");
}
__device__ __forceinline__ void coarse_until(unsigned tgt) {
  for (;;) { const unsigned rem = tgt - mtc(); if (rem - 1u >= 60000u) break; }
}
#define DUR_UP 6
#define DUR_DN 2
DEV int max32(int d) {
  d = max(d, (int)dppu<ROR8>(d)); d = max(d, (int)dppu<ROR4>(d)); d = max(d, (int)dppu<ROR2>(d)); d = max(d, (int)dppu<ROR1>(d));
  return max(__builtin_amdgcn_readlane(d, 0), __builtin_amdgcn_readlane(d, 16));
}
struct Sched {
  unsigned T, tP, dur, gap, more;
  DEV void init(unsigned t, const RecArgs& a) { T = t; tP = t; dur = a.dur0; gap = 0u; more = 0u; }
  DEV unsigned on_time() const { return tP + gap; }
  DEV unsigned limit(const RecArgs& a) const { return T + dur + a.tol; }
  DEV void next(u32x2 an, const RecArgs& a, bool start) {
    const int dm = max32((int)(an[0] - T)), gm = max32((int)an[1]), over = dm - (int)dur;
    const bool late = start || over > a.tol;
    more = late ? more + 150u : 0u;
    T = late ? T + (unsigned)dm + a.slow + more : tP + gap;
    int dd = late ? (start ? 0 : DUR_UP) : max(over, over >> DUR_DN);
    PIN_S(dd);
    dur += (unsigned)dd; gap = (unsigned)gm;
    tP = T + dur + a.d0;
    PIN_S(T); PIN_S(tP); PIN_S(dur); PIN_S(more); PIN_S(gap);
  }
};
template <typename R>
__device__ __forceinline__ u32x2 ann_valid(u32x2 an, R rs, unsigned vo, unsigned so, bool& dead) {
  if (__builtin_expect(__any(max(an[0], an[1]) == SENT), 0)) {
    for (unsigned spins = 0; !dead;) {
      __builtin_amdgcn_s_sleep(3);
      const bool need = max(an[0], an[1]) == SENT;
      const u32x2 n = LD8(rs, need ? vo : OOB, so);
      if (need) an = n;
      if (!__any(max(an[0], an[1]) == SENT)) break;
      if (++spins > SPIN_CAP) dead = true;
      asm volatile("" ::: "memory");
    }
  }
  return an;
}
template <typename R>
__device__ __forceinline__ void need_progress(R rs, unsigned vo, unsigned want, unsigned& have) {
  for (unsigned spins = 0; have < want && spins < SPIN_CAP; ++spins) {
    unsigned v = LD4(rs, vo, 0);
    v = min(v, dppu<0x4E>(v)); v = min(v, dppu<0xB1>(v));
    have = RFL(v);
    if (have < want) __builtin_amdgcn_s_sleep(8);
    asm volatile("" ::: "memory");
  }
}
struct Softmax {
  f32x4 s; float m, x;
  DEV void a() {
    float t;
    x = fmx(fmx(s[0], s[1]), s[2]); pin_v(x); x = fmx(fmx(x, s[3]), m);
    asm volatile("s_nop 1\n\tv_max_f32_dpp %0, %0, %0 row_ror:8 row_mask:0xf bank_mask:0xf\n\ts_nop 1\n\tv_max_f32_dpp %0, %0, %0 row_ror:4 row_mask:0xf bank_mask:0xf\n\t"
                 "v_mov_b32 %1, %0\n\ts_nop 1\n\tv_permlane32_swap_b32 %0, %1\n\tv_max_f32 %0, %0, %1\n\tv_mul_f32 %0, 0xbdd53b94, %0" : "+v"(x), "=&v"(t));
  }
  DEV void b() {
    const float C = 0.07216878364870322f * 1.4426950408889634f;
    s = __builtin_elementwise_fma(s, (f32x4){C, C, C, C}, (f32x4){x, x, x, x}); m = __builtin_fmaf(m, C, x);
#pragma unroll
    for (int i = 0; i < 4; ++i) s[i] = __builtin_amdgcn_exp2f(s[i]);
    m = __builtin_amdgcn_exp2f(m);
    pin_v(s); pin_v(m);
  }
  DEV void c() {
    float t;
    const f32x2 u = (f32x2){s[0], s[1]} + (f32x2){s[2], s[3]};
    x = (u[0] + m) + u[1];
    asm volatile("s_nop 1\n\tv_add_f32_dpp %0, %0, %0 row_ror:8 row_mask:0xf bank_mask:0xf\n\ts_nop 1\n\tv_add_f32_dpp %0, %0, %0 row_ror:4 row_mask:0xf bank_mask:0xf\n\t"
                 "v_mov_b32 %1, %0\n\ts_nop 1\n\tv_permlane32_swap_b32 %0, %1\n\tv_add_f32 %0, %0, %1\n\tv_rcp_f32 %0, %0" : "+v"(x), "=&v"(t));
  }
  DEV u32x4 d() {
    unsigned o0, o1, o2, o3; float t, u;
    asm volatile("s_nop 0\n\tv_mul_f32 %4, %10, %11\n\tv_mul_f32 %5, %10, %11\n\tv_mul_f32 %0, %6, %11\n\tv_mul_f32 %2, %7, %11\n\tv_permlane32_swap_b32 %4, %5\n\t"
                 "v_mul_f32 %1, %8, %11\n\tv_mul_f32 %3, %9, %11\n\tv_cvt_pk_bf16_f32 %0, %0, |%2|\n\tv_cvt_pk_bf16_f32 %1, %1, |%3|\n\tv_cvt_pk_bf16_f32 %4, %4, |%5|\n\t"
                 "v_mov_b32_dpp %2, %0 row_ror:8 row_mask:0xf bank_mask:0xf\n\tv_mov_b32_dpp %3, %1 row_ror:8 row_mask:0xf bank_mask:0xf\n\t"
                 "v_mov_b32_dpp %0, %4 row_shr:8 row_mask:0xf bank_mask:0x4\n\tv_mov_b32_dpp %1, %4 row_shr:4 row_mask:0xf bank_mask:0x4\n\t"
                 "v_mov_b32_dpp %2, %4 quad_perm:[0,1,2,3] row_mask:0xf bank_mask:0x4\n\tv_mov_b32_dpp %3, %4 row_shl:4 row_mask:0xf bank_mask:0x4"
                 : "=&v"(o0), "=&v"(o1), "=&v"(o2), "=&v"(o3), "=&v"(t), "=&v"(u) : "v"(s[0]), "v"(s[1]), "v"(s[2]), "v"(s[3]), "v"(m), "v"(x));
    return (u32x4){o0, o1, o2, o3};
  }
};
#define TMARK(k)
#define STAMP(k)
#define STAMP_DECL(sh)
#define STAMP_FLUSH(s, ref, x0, x1)

#define AN_AT 780u
#define LATE_PAD 480u
#define HEAD 1u
#define S_FINE 0
#define EX_UP 16u
#define SM_W 2
constexpr int S_EX = 0, S_ET = S_EX + 4 * 2048, S_CT = S_ET + 4 * 5120, S_LW = S_CT + 48, S_NL = 13, S_END = S_LW + 4 * S_NL * 2048;
__host__ __device__ constexpr int s_q(int n) { return n < 30 ? n / 6 : n < 50 ? 5 + (n - 30) / 4 : 5 + (n - 50) / 2; }
__host__ __device__ constexpr int s_t(int n) { return n < 30 ? n % 6 : n < 50 ? (n - 30) % 4 : 4 + (n & 1); }
__host__ __device__ constexpr bool s_lds(int n) { return n % 4 == 3 && n / 4 < S_NL; }
__host__ __device__ constexpr int s_ri(int n) { return n - ((n + 1) / 4 < S_NL ? (n + 1) / 4 : S_NL); }
template <int kind>
__device__ __forceinline__ void score_role(const RecArgs& a, LAS char* lds, int rep, int il, unsigned posM, unsigned posP) {
  const int lane = fresh_lane(), w = __builtin_amdgcn_readfirstlane(threadIdx.x >> 6), c = lane & 15, g = lane >> 4, hf = c >> 3, i8 = c & 7;
  const int h = rep * 4 + il, iod = rep >> 2;
  const __amdgpu_buffer_rsrc_t rs = RSRC(a.sync, SYNC_LAYER), rq = RSRC(a.qk, 2 * QK_IMG), rp = RSRC(a.pt, PT_IMG);
  Clock ck; ck.init();
  u32x8 WV[28 - S_NL];
  {
    const __amdgpu_buffer_rsrc_t rw = RSRC(a.wf, 768 * 20480);
    const unsigned vo = lane * 16, wb = (unsigned)(h * 12 + 3 * w) * 20480u;
    static_for<60>([&](auto N) {
      constexpr int n = decltype(N)::value, q = s_q(n), t = s_t(n), ri = s_ri(n);
      const unsigned so = wb + (unsigned)((t & 1) * 384 + (t >> 1)) * 20480u + (unsigned)(q < 5 ? q + 5 * iod : q - 5 * iod) * 2048u;
      if (s_lds(n)) { LDS16(S_LW + (w * S_NL + n / 4) * 2048 + lane * 16) = LDP16(rw, vo, so); LDS16(S_LW + (w * S_NL + n / 4) * 2048 + 1024 + lane * 16) = LDP16(rw, vo + 1024, so); }
      else if (ri >= 32) WV[ri < 32 ? 0 : ri - 32] = cat2(LDP16(rw, vo, so), LDP16(rw, vo + 1024, so));
    });
    SB();
    static_for<60>([&](auto N) {
      constexpr int n = decltype(N)::value, q = s_q(n), t = s_t(n), ri = s_ri(n);
      const unsigned so = wb + (unsigned)((t & 1) * 384 + (t >> 1)) * 20480u + (unsigned)(q < 5 ? q + 5 * iod : q - 5 * iod) * 2048u;
      if (!s_lds(n) && ri < 32) { ld_a<8 * (ri & 31)>(rw, vo, so); ld_a<8 * (ri & 31) + 4>(rw, vo, so + 1024u); VM_WAIT(40); }
    });
    VM_WAIT(0);
#pragma unroll
    for (int i = 0; i < 28 - S_NL; ++i) PIN_V(WV[i]);
  }
  const int sidx = hf ? 0xEEEE : 0x4444;
  u32x4 idn;
#pragma unroll
  for (int jj = 0; jj < 4; ++jj) idn[jj] = (((c >> 2) & 1) == (g & 1) && jj == ((c & 3) >> 1) + 2 * hf) ? ((c & 1) ? 0x3f800000u : 0x3f80u) : 0u;
  const unsigned lomask = hf ? 0u : ~0u;
  unsigned l16 = lane * 16u;
  const unsigned an_ld = lane < 32 && lane != h ? ((lane >> 4) * 2 + 1) * ROW + posM * 4096u + 1024u + c * 128u : OOB;
  const unsigned an_st = ((h >> 4) * 2 + 1) * ROW + (lane >> 3) * 4096u + 1024u + (h & 15) * 128u + i8 * 16u;
  const int sr = (lane & 3) | ((lane >> 2) & 4), sg = ((lane >> 2) & 1) | ((lane >> 4) & 2), sk = ((lane >> 1) & 6) | (lane >> 5), cq = 8 * (w & 1) + sr;
  const bool smem_lane = (lane & 44) == 8;
  const unsigned p_st = !hf ? OFS_P + (h >> 3) * ROW + (8 / SM_W) * w * 4096u + ((h >> 1) & 3) * 1024u + sg * 256u + (h & 1) * 128u + sr * 16u
                            : smem_lane ? OFS_P + 4 * ROW + (8 / SM_W) * w * 4096u + h * 128u + sr * 16u : OOB;
  const unsigned pt_st = !hf ? (unsigned)(h * 2048 + (w >> 1) * 1024 + (sg * 16 + cq) * 16) : smem_lane ? PT_M + ((w >> 1) * NH + h) * 256u + cq * 16u : OOB;
  const unsigned lw = S_LW + w * S_NL * 2048 + lane * 16;
  unsigned ex_w = S_EX + w * 2048 + lane * 16; PIN_V(ex_w);
  const unsigned et_w = S_ET + w * 5120 + lane * 16, et_r = S_ET + ((w >> 1) ? 1 + hf : 0) * 1024 + (sg * 16 + cq) * 16,
                 et_m = S_ET + (3 + (w >> 1)) * 1024 + ((2 + (sk >> 2)) * 16 + cq) * 16 + (sk & 3) * 4;
  const unsigned ex_r = S_EX + (sg * 16 + hf * 8 + sr) * 16, ex_m = S_EX + 1024 + ((2 + (sk >> 2)) * 16 + sr) * 16 + (sk & 3) * 4;
  const unsigned jp_vo = lane < 4 ? OFS_CW + CW_JP + (unsigned)((((lane & 1) * 384 + h * 12 + ((lane >> 1) & 1) * 11) / 20) * 128) : OOB;
  unsigned jhave = 0;
  u32x4 kf[3], qf[3];
  auto load_qk = [&](unsigned so) {
#pragma unroll
    for (int p = 0; p < 3; ++p) { kf[p] = LD16(rq, l16 + p * 1024, so + QK_IMG); qf[p] = LD16(rq, l16 + p * 1024, so); }
  };
  TMARK(1);
  need_progress(rs, jp_vo, HEAD, jhave);
  TMARK(2);
  unsigned qk_so = (unsigned)(h * 12 + 3 * w) * 1024u;
  load_qk(qk_so);
  bool dead = false;
  unsigned extra = 0;
  Softmax tk; tk.s = (f32x4){0.f, 0.f, 0.f, 0.f}; tk.m = 0.f;
  ck.edge(); ck.rate();
  Sched sd; sd.init(ck.global(mtc()), a);
  if (kind == 2) LDS16(S_CT) = (u32x4){0u, 0u, 1u, 0u};
  unsigned mT = mtc();
  u32x2 an = {0u, 0u}; bool owed = false;
  const unsigned kA = posM * 4096u + iod * (2 * ROW), kB = posM * 4096u + (1 - iod) * (2 * ROW);
  unsigned sl = 0u, soA = kA, soB = kB, soA1 = kA + ROW, soB1 = kB + ROW, sp = 0u, jp_so = 0u, pt_so = FAR;
  f32x4 acc[6];
  u32x4 tl[2], th[2];
  auto prime = [&]() {
    tl[0] = LDS16(lw); th[0] = LDS16(lw + 1024); tl[1] = LDS16(lw + 2048); th[1] = LDS16(lw + 3072);
#pragma unroll
    for (int t = 0; t < 6; ++t) zero4(acc[t]);
    SB();
  };
  prime();
  auto token_parts = [&](f32x4 (&e)[4], float (&em)[4]) {
#pragma unroll
    for (int k = 0; k < 4; ++k) e[k] = __builtin_bit_cast(f32x4, LDS16(et_r + k * 5120));
#pragma unroll
    for (int k = 0; k < 4; ++k) em[k] = *(LAS float*)(lds + et_m + k * 5120);
  };
  auto token_sums = [&](f32x4 (&e)[4], float (&em)[4]) {
    f32x4 es = (e[0] + e[1]) + (e[2] + e[3]);
    PIN_V(es);
#pragma unroll
    for (int r = 0; r < 4; ++r) tk.s[r] = 16 * hf + 4 * sg + r <= 8 * w + sr ? es[r] : -INFINITY;
    const f32x2 u = (f32x2){em[0], em[1]} + (f32x2){em[2], em[3]};
    tk.m = u[0] + u[1];
    pin_v(tk.s); pin_v(tk.m);
  };
  unsigned budget = SPIN_CAP;
  STAMP_DECL(0);
#pragma unroll 1
  for (int s = 0; s < NSEG; ++s) {
    PIN_V(l16);
    f32x4 dx, dm, tr, ts;
    u32x4 cm[10], ta, tc, bq; u32x2 X[3];
#define LDM(q) LD16(rs, l16 + ((q) % 5 < 4 ? ((q) % 5) * 1024 : 0), (q) < 4 ? soA : (q) == 4 ? soA1 : (q) < 9 ? soB : soB1)
#define POS(n) do { constexpr int q_ = s_q(n), t_ = s_t(n), ri_ = s_ri(n); \
      if (s_lds(n)) smf_v(acc[t_], cm[q_], cat2(tl[((n) / 4) & 1], th[((n) / 4) & 1]), sidx); \
      else if (ri_ < 32) smf_a<8 * (ri_ & 31)>(acc[t_], cm[q_], sidx); else smf_v(acc[t_], cm[q_], WV[ri_ < 32 ? 0 : ri_ - 32], sidx); \
      SB(); \
      if ((n) % 4 == 3 && (n) / 4 + 2 < S_NL) tl[((n) / 4) & 1] = LDS16(lw + ((n) / 4 + 2) * 2048); \
      if ((n) % 4 == 0 && (n) > 0 && (n) / 4 + 1 < S_NL) th[((n) / 4 + 1) & 1] = LDS16(lw + ((n) / 4 + 1) * 2048 + 1024); \
      SB(); } while (0)
#define CVT(dst, src, k) do { unsigned v_ = pack2((src)[2 * (k)], (src)[2 * (k) + 1]); PIN_V(v_); dst = v_; SB(); } while (0)
#define B_LO(p, k) do { unsigned v_ = X[p][k] & lomask; PIN_V(v_); bq[k] = v_; SB(); } while (0)
#define B_HI(p, k) do { unsigned v_ = dppu<SHR8>(X[p][k]); PIN_V(v_); bq[2 + (k)] = v_; SB(); } while (0)
    unsigned jpend; u64 tb1 = 0;
    SB();
    wait_until(mT);
    SB();
    STAMP(0);
    cm[0] = LDM(0); SB(); tk.a(); SB();
    cm[1] = LDM(1); SB(); tk.b(); SB();
    cm[2] = LDM(2); SB(); tk.c(); SB();
    cm[3] = LDM(3); SB();
    u32x4 ptk = tk.d(); PIN_V(ptk); SB();
    cm[4] = LDM(4); SB();
    unsigned chk = 0;
    static_for<48>([&](auto N) {
      constexpr int n = decltype(N)::value, q = s_q(n);
      POS(n);
      if (n == 0 && !S_FINE) STAMP(1);
      if constexpr (n % 4 == 2 && 5 + n / 4 < 10) cm[5 + n / 4] = LDM(5 + n / 4);
      SB();
      if (n == 1 || n == 9 || n == 13 || n == 21 || n == 25 || n == 32 || n == 36 || n == 40 || n == 44 || n == 47) { chk = mx3(chk, cm[q][0], cm[q][2]); pin_v(chk); }
      SB();
    });
    POS(48);
    u64 bad = __builtin_amdgcn_ballot_w64(chk == SENT); asm volatile("" : "+s"(bad)); SB();
    POS(49);
    if (__builtin_expect(bad != 0, 0)) {
      *(LAS unsigned*)(lds + S_CT + 8) = (unsigned)s + 1u;
      u64 miss;
      do {
        __builtin_amdgcn_s_sleep(1);
        unsigned m = 0;
#pragma unroll
        for (int q = 5; q < 10; ++q) cm[q] = LDM(q);
#pragma unroll
        for (int q = 5; q < 10; ++q) m = max(m, mx4(cm[q]));
        SB();
#pragma unroll
        for (int q = 0; q < 5; ++q) cm[q] = LDM(q);
#pragma unroll
        for (int q = 0; q < 5; ++q) m = max(m, mx4(cm[q]));
        miss = __builtin_amdgcn_ballot_w64(m == SENT);
        asm volatile("s_sub_u32 %1, %1, 1\n\ts_cselect_b32 %1, 0, %1\n\ts_cselect_b64 %0, 0, %0" : "+s"(miss), "+s"(budget) :: "scc", "memory");
      } while (miss != 0);
      prime();
      NOPS(1);
      static_for<50>([&](auto N) {
        constexpr int n = decltype(N)::value;
        POS(n);
        if constexpr (n % 6 == 5 && n < 30) cm[5 + n / 6] = LDM(5 + n / 6);
        SB();
      });
    }
    if (!S_FINE) STAMP(2);
    CVT(ta[0], acc[0], 0);
    POS(50); asm volatile("" : "+v"(tl[0]), "+v"(th[0])); SB(); CVT(ta[1], acc[0], 1);
    POS(51); __builtin_amdgcn_raw_buffer_store_b128(ptk, rp, pt_st, pt_so, 16); SB(); CVT(ta[2], acc[1], 0);
    POS(52); jpend = LD4(rs, jp_vo, jp_so); SB(); CVT(ta[3], acc[1], 1);
    asm volatile("" :: "v"(ptk));
    POS(53); CVT(tc[0], acc[2], 0);
    mf32z(tr, ta, idn); SB(); CVT(tc[1], acc[2], 1);
    POS(54); CVT(tc[2], acc[3], 0);
    POS(55); CVT(tc[3], acc[3], 1);
    POS(56); CVT(X[0][0], tr, 0);
    mf32z(ts, tc, idn); SB(); CVT(X[0][1], tr, 1);
    POS(57); B_LO(0, 0);
    POS(58); B_LO(0, 1);
    POS(59); B_HI(0, 0);
    mf16z(dm, X[0], X[0]); SB(); B_HI(0, 1); CVT(X[1][0], ts, 0); CVT(X[1][1], ts, 1);
    CVT(ta[0], acc[4], 0); CVT(ta[1], acc[4], 1);
    mf32z(dx, kf[0], bq); SB(); CVT(ta[2], acc[5], 0); CVT(ta[3], acc[5], 1);
    B_LO(1, 0); B_LO(1, 1);
    mf32z(tr, ta, idn); SB(); B_HI(1, 0); B_HI(1, 1);
    mf16(dm, X[1], X[1]); SB();
    mf32(dx, kf[1], bq); SB();
    CVT(X[2][0], tr, 0); CVT(X[2][1], tr, 1); B_LO(2, 0); B_LO(2, 1);
    mf16(dm, X[2], X[2]); SB(); B_HI(2, 0); B_HI(2, 1);
    mf32_n(dx, kf[2], bq); SB();
    if (!S_FINE) STAMP(3);
    NOPS(2);
    LDS16(ex_w + 1024) = __builtin_bit_cast(u32x4, dm);
    NOPS(3);
    LDS16(ex_w) = __builtin_bit_cast(u32x4, dx);
    if constexpr (kind == 0) {
      asm volatile("s_waitcnt lgkmcnt(0)\n\ts_barrier ; softmax waves" ::: "memory");
      if (!S_FINE) STAMP(4);
      f32x4 e[4]; float em[4];
#pragma unroll
      for (int k = 0; k < 4; ++k) e[k] = __builtin_bit_cast(f32x4, LDS16(ex_r + k * 2048));
#pragma unroll
      for (int k = 0; k < 4; ++k) em[k] = *(LAS float*)(lds + ex_m + k * 2048);
      asm volatile("" :: "s"(sp + 4096u), "s"(sp + 8192u), "s"(sp + 12288u));
      SB();
      Softmax sm;
      sm.s = (e[0] + e[1]) + (e[2] + e[3]);
      const f32x2 u = (f32x2){em[0], em[1]} + (f32x2){em[2], em[3]};
      sm.m = u[0] + u[1];
      sm.a(); sm.b(); sm.c();
      const u32x4 v = sm.d();
#pragma unroll
      for (int k = 0; k < 8 / SM_W - 1; ++k) __builtin_amdgcn_raw_buffer_store_b128(v, rs, p_st, sp + k * 4096u, 16);
      ST16(v, rs, p_st, sp + (8 / SM_W - 1) * 4096u);
    } else if constexpr (kind == 1) {
      unsigned fl;
      const unsigned a_gp = a.g2t + s_mulshr16(a.g2c, ck.sig) + extra;
      asm volatile("s_waitcnt lgkmcnt(0)\n\ts_barrier ; clock\n\tds_read_b32 %0, %1" : "=&v"(fl) : "v"((unsigned)(uintptr_t)lds + S_CT + 8u), "s"(a_gp) : "memory");
      const unsigned m0 = ck.edge();
      PIN_V(fl);
      const bool failed = RFL(fl) == (unsigned)s + 1u;
      bq[0] = (ck.re + (unsigned)(((int)(m0 + a.tail - ck.me) * (int)ck.sig) >> 16) + (failed ? LATE_PAD : 0u)) & ~1u;
      bq[1] = a_gp;
      ST16(bq, rs, an_st, sl);
      *(LAS u32x2*)(lds + S_CT + 32) = lo2(bq);
      extra += failed && s > 0 && extra < 240u ? EX_UP + 1u : 0u; PIN_S(extra);
    } else {
      asm volatile("s_waitcnt lgkmcnt(0)\n\ts_barrier\n\ts_memtime %0" : "=s"(tb1) :: "memory");
      __builtin_amdgcn_raw_ptr_buffer_atomic_add_i32(1, rs, l16 << 20, s > 1 ? OFS_CW + CW_SS + (unsigned)(s - 2) * 128u : FAR, 0);
      asm volatile("s_waitcnt lgkmcnt(0)" : "+s"(tb1));
      if (owed) sd.next(an, a, false);
    }
    SB();
    STAMP(5);
    const unsigned sl0 = sl, mT0 = mT;
    {
      f32x4 d[5]; f32x2 zz[12];
      static_for<15>([&](auto N) {
        constexpr int n = decltype(N)::value, t = n / 3, p = n % 3;
        const u32x2 fa = t < 2 ? lo2(kf[p]) : t == 2 ? hi2(kf[p]) : X[p], fb = (t == 0 || t == 3) ? lo2(qf[p]) : hi2(qf[p]);
        if (p == 0) mf16z(d[t], fa, fb); else mf16(d[t], fa, fb);
        SB();
        if (n < 4) { const u32x4 v = LDS16(lw + n * 1024); if (n & 1) th[n >> 1] = v; else tl[n >> 1] = v; }
        if (n >= 5 && p == 2) LDS16(et_w + (t - 1) * 1024) = __builtin_bit_cast(u32x4, d[t > 0 ? t - 1 : 0]);
        if (n == 4) { sl = (sl + M_SLOT) & (4 * M_SLOT - 1); pin_s(sl); }
        if (n == 6) { soA = sl + kA; soB = sl + kB; pin_s(soA); pin_s(soB); }
        if (n == 7) { soA1 = soA + ROW; soB1 = soB + ROW; pin_s(soA1); pin_s(soB1); }
        if (n == 9) { sp = P_SLOT - sp; pt_so = (unsigned)s * PT_SEG; pin_s(sp); pin_s(pt_so); }
        if (n == 10) { qk_so += s + 1 < NSEG ? NH * 12 * 1024u : 0u; pin_s(qk_so); }
        SB();
        if (n < 12) zero2(zz[n < 12 ? n : 0]);
        if (n == 12) { jpend = min(jpend, dppu<0x4E>(jpend)); pin_v(jpend); }
        if (n == 13) { jpend = min(jpend, dppu<0xB1>(jpend)); pin_v(jpend); }
        SB();
      });
#pragma unroll
      for (int t = 0; t < 6; ++t) { acc[t] = __builtin_shufflevector(zz[2 * t], zz[2 * t + 1], 0, 1, 2, 3); pin_v(acc[t]); }
      jhave = max(jhave, RFL(jpend));
      jp_so = jhave < min((unsigned)NSEG, (unsigned)s + 5u) ? 0u : FAR; pin_s(jp_so);
      NOPS(1);
      LDS16(et_w + 4096) = __builtin_bit_cast(u32x4, d[4]);
      SB();
    }
    if (S_FINE) STAMP(1);
    if constexpr (kind == 2) {
      coarse_until((unsigned)tb1 + AN_AT);
      an = LD8(rs, an_ld, sl0);
      SB();
    }
    if constexpr (kind == 1) {
      ck.rate(); extra -= extra != 0u; PIN_S(extra);
      LDS16(S_CT + 16) = (u32x4){ck.me, ck.re, ck.rho, 0u};
    }
    if (__builtin_expect(jhave < min((unsigned)s + 2u, (unsigned)NSEG), 0)) need_progress(rs, jp_vo, min((unsigned)s + 2u, (unsigned)NSEG), jhave);
    STAMP(6);
    WG_BARRIER();
    if (S_FINE) STAMP(2);
    {
      f32x4 e[4]; float em[4]; u32x4 cc; u32x2 own;
      if constexpr (kind == 2) { cc = LDS16(S_CT + 16); own = *(LAS u32x2*)(lds + S_CT + 32); }
      token_parts(e, em);
      SB();
      PIN_V(l16);
      if constexpr (kind < 2) load_qk(qk_so);
      else {
        ck.me = RFL(cc[0]); ck.re = RFL(cc[1]); ck.rho = RFL(cc[2]);
        mT = ck.local(sd.on_time());
        u32x2 pub = {(unsigned)s + 1u, mT}; PIN_V(pub);
        const unsigned lim = sd.limit(a);
        SB();
        if (S_FINE) STAMP(3);
        if (lane == h) an = own;
        const u64 mis = __builtin_amdgcn_ballot_w64(max(an[0], an[1]) == SENT) | (__builtin_amdgcn_ballot_w64((int)(an[0] - lim) > 0) & 0xffffffffull);
        owed = s > 0 && mis == 0;
        if (__builtin_expect(!owed, 0)) {
          an = ann_valid(an, rs, an_ld, sl0, dead);
          sd.next(an, a, s == 0);
          mT = ck.local(sd.T); pub[1] = mT;
        }
        *(LAS u32x2*)(lds + S_CT) = pub;
        SB();
        load_qk(qk_so);
      }
      SB();
      token_sums(e, em);
    }
    SB();
    if (S_FINE) STAMP(4);
    if constexpr (kind < 2) {
      u32x2 v;
      do { v = *(volatile LAS u32x2*)(lds + S_CT); } while (RFL(v[0]) != (unsigned)s + 1u);
      mT = RFL(v[1]);
    }
    STAMP(7);
    STAMP_FLUSH(s, mT0, owed ? sd.on_time() : sd.T, bad != 0);
  }
  tk.a(); tk.b(); tk.c();
  ST16(tk.d(), rp, pt_st, (unsigned)(NSEG - 1) * PT_SEG);
  asm volatile("s_waitcnt vmcnt(0)" ::: "memory");
  WG_BARRIER();
  __builtin_amdgcn_raw_ptr_buffer_atomic_add_i32(1, rs, l16 << 20, w < 2 ? OFS_CW + CW_SS + (unsigned)(NSEG - 2 + w) * 128u : FAR, 0);
  TMARK(3);
  __builtin_amdgcn_endpgm();
}

constexpr int O_M = 0, O_RED = O_M + 10240, O_CT = O_RED + 3072;
#define P_GAP 3
#define B_GAP 15
#define OM_LAG 80u
__device__ __forceinline__ void out_role(const RecArgs& a, LAS char* lds, int rep, int k, unsigned posM, unsigned posP) {
  const int lane = fresh_lane(), w = __builtin_amdgcn_readfirstlane(threadIdx.x >> 6), c = lane & 15, g = lane >> 4, hf = c >> 3, i8 = c & 7;
  const int i = rep * 10 + k, iod = rep >> 2, ct = i >> 1, o = i & 1;
  const __amdgpu_buffer_rsrc_t rs = RSRC(a.sync, SYNC_LAYER), ry = RSRC(a.y, Y_IMG), rz = RSRC(a.z, Z_IMG);
  const unsigned l16 = lane * 16, l20 = l16 << 20;
  Clock ck; ck.init();
  const int t0 = 8 * iod + 2 * w, t2 = 8 * (1 - iod) + 2 * w;
  u32x8 UV[8];
  {
    const __amdgpu_buffer_rsrc_t rw = RSRC(a.uf, 80 * 16 * 20480);
    static_for<40>([&](auto F) {
      constexpr int f = decltype(F)::value, sl = f / 10, L = f % 10;
      const unsigned so = (unsigned)(i * 16 + (sl < 2 ? t0 : t2) + (sl & 1)) * 20480u + L * 2048u;
      if (f < 32) { ld_a<8 * (f & 31)>(rw, l16, so); ld_a<8 * (f & 31) + 4>(rw, l16, so + 1024u); VM_WAIT(40); }
      else UV[f & 7] = cat2(LDP16(rw, l16, so), LDP16(rw, l16 + 1024, so));
    });
    VM_WAIT(0);
#pragma unroll
    for (int f = 0; f < 8; ++f) PIN_V(UV[f]);
  }
  const int sidx = hf ? 0xEEEE : 0x4444;
  const int hq = 2 * ((g < 2 ? t0 : t2) + (g & 1)) + hf;
  const unsigned y_vo = hf * 40960u + g * 256u + o * 128u + i8 * 16u;
  const unsigned z_vo = hq * 256u + o * 128u + i8 * 16u;
  const unsigned p_vo = OFS_P + posP * 4096u + l16, pm_vo = OFS_P + 4 * ROW + posP * 4096u + hq * 128u + i8 * 16u;
  const unsigned pA = (unsigned)(t0 >> 2) * ROW + (t0 & 3) * 1024u, pB = (unsigned)(t2 >> 2) * ROW + (t2 & 3) * 1024u;
  const unsigned m_vo = posM * 4096u + l16;
  const unsigned an_ld = lane < 32 ? ((lane >> 4) * 2 + 1) * ROW + posM * 4096u + 1024u + c * 128u : OOB;
  const int Lm = i >> 3;
  const unsigned m_st = ((Lm / 5) * 2 + (Lm % 5 == 4)) * ROW + (lane >> 3) * 4096u + (Lm % 5 & 3) * 1024u + (i & 7) * 128u + i8 * 16u;
  unsigned mo[4], ml[4], mc[4];
#pragma unroll
  for (int q = 0; q < 4; ++q) {
    const int L = w == 1 ? q : 3 * w - 2 + q;
    const bool skip = (w != 1 && q == 3) || (L == Lm && (lane >> 3) == (i & 7));
    mo[q] = skip ? OOB : m_vo; ml[q] = skip ? O_RED + 2304 + (lane & 31) * 16 : O_M + L * 1024 + lane * 16;
    mc[q] = ((L / 5) * 2 + (L % 5 == 4)) * ROW + (L % 5 & 3) * 1024u;
  }
  const unsigned own_l = lane < 8 ? O_M + i * 128 + lane * 16 : O_RED + 2304 + (lane & 31) * 16;
  if (w == 0) { LDS16(own_l) = LD16(rs, m_st, 0); LDS16(O_CT) = (u32x4){0u, 0u, 1u, 0u}; *(LAS unsigned*)(lds + O_CT + 16) = 0u; }
  const unsigned rz_vo = w < 2 ? OFS_P + ((2 * k + w) >> 2) * ROW + posP * 4096u + ((2 * k + w) & 3) * 1024u + l16
                       : w == 2 ? ((k / 5) * 2 + (k % 5 == 4)) * ROW + (k % 5 & 3) * 1024u + m_vo
                       : k < 4 ? ((k >> 1) * 2 + 1) * ROW + 1024u + (k & 1) * 1024u + m_vo : OOB;
  int rz_b = w == 3 ? 2 : 1; unsigned rz_m = w < 2 ? 1u : 3u, rz_k = w < 2 ? P_SLOT : M_SLOT;
  asm volatile("" : "+s"(rz_b), "+s"(rz_m), "+s"(rz_k));
  const unsigned red_w = (g >> 1) == hf ? O_RED + i8 * 272 + (g & 1) * 128 + (w * 2 + hf) * 8 : O_RED + 2304 + lane * 8;
  const unsigned red_r = O_RED + i8 * 272 + g * 64 + hf * 32;
  const unsigned jp_vo = lane < 4 ? OFS_CW + CW_JP + (unsigned)(40 + rep * 8 + (16 * k + (lane & 1) * 15) / 20) * 128u : OOB;
  unsigned jhave = 0;
  bool dead = false;
  ck.edge(); ck.rate();
  Sched sd; sd.init(ck.global(mtc()), a);
  unsigned mM = mtc();
  TMARK(1);
  STAMP_DECL(8);
  u32x4 zf = {0u, 0u, 0u, 0u};
  auto step = [&](int s, auto LAST, auto KIND) {
    constexpr bool last = decltype(LAST)::value, kp = decltype(KIND)::value == 2, w0 = decltype(KIND)::value == 0;
    constexpr int NC = w0 ? 0 : kp ? 3 : 4;
    const unsigned sl = (unsigned)(s & 3) * M_SLOT, mT = mM;
    need_progress(rs, jp_vo, (unsigned)s + 1u, jhave);
    u32x4 yf[4];
    const unsigned ys = (unsigned)(s * NH * 40 + ct) * 1024u, z_so = s < 1 ? FAR : (unsigned)((s - 1) * 40 + ct) * 8192u;
    if constexpr (kp) static_for<8>([&](auto K) { ck.rho_step<decltype(K)::value>(); });
    f32x4 za[4];
#pragma unroll
    for (int t = 0; t < 4; ++t) zero4(za[t]);
    unsigned sv = SENT; PIN_V(sv);
    u32x4 pz = {sv, sv, sv, sv}; PIN_V(pz);
    const unsigned rz_so = s < rz_b ? FAR : ((unsigned)(s - rz_b) & rz_m) * rz_k, fl_so = s > 1 ? OFS_CW + CW_SO + rep * 128u + (unsigned)(s - 2) * 1024u : FAR;
    const unsigned sp = (unsigned)(s & 1) * P_SLOT, sn = (unsigned)((s + 1) & 3) * M_SLOT;
    asm volatile("" :: "s"(rz_so), "s"(fl_so), "s"(z_so), "s"(ys + t0 * 81920u), "s"(ys + t2 * 81920u), "s"(sl + mc[0]), "s"(sl + mc[1]), "s"(sl + mc[2]), "s"(sl + mc[3]),
                 "s"(sp + pA), "s"(sp + pB), "s"(sp), "s"(sn));
    STAMP(0);
    wait_until(mM);
    SB();
    u32x4 cm[4];
#pragma unroll
    for (int q = 0; q < NC; ++q) cm[q] = LD16(rs, mo[q], sl + mc[q]);
    SB();
    auto bulk = [&](auto K) {
      constexpr int k = decltype(K)::value;
      if constexpr (k < 4) { if constexpr (!last) yf[k] = LD16(ry, y_vo, ys + (k < 2 ? t0 : t2) * 81920u + (k & 1) * 81920u); }
      else __builtin_amdgcn_raw_buffer_store_b128(zf, rz, z_vo, z_so, 16);
    };
    if (w0) NOPS(15);
    NOPS(15); NOPS(B_GAP); static_for<5>(bulk); SB();
    unsigned jv;
    if constexpr (kp) { jv = LD4(rs, jp_vo, jhave < min((unsigned)NSEG, (unsigned)s + 6u) ? 0u : FAR); SB(); }
    unsigned fails = 0;
    if constexpr (!w0) {
      unsigned chk = 0u;
#pragma unroll
      for (int q = 0; q < NC; ++q) chk = mx3(chk, cm[q][0], cm[q][2]);
      if (__builtin_expect(__any(chk == SENT), 0)) {
        fails = 1;
        *(LAS unsigned*)(lds + O_CT + 8) = (unsigned)s + 1u;
        for (unsigned spins = 0; !dead;) {
          __builtin_amdgcn_s_sleep(4);
          bool nd[NC]; u32x4 n[NC]; unsigned m = 0;
#pragma unroll
          for (int q = 0; q < NC; ++q) { nd[q] = mx4(cm[q]) == SENT; n[q] = LD16(rs, nd[q] ? m_vo : OOB, sl + mc[q]); }
#pragma unroll
          for (int q = 0; q < NC; ++q) { if (nd[q]) cm[q] = n[q]; m = max(m, mx4(cm[q])); }
          if (!__any(m == SENT)) break;
          if (++spins > SPIN_CAP) dead = true;
          asm volatile("" ::: "memory");
        }
      }
#pragma unroll
      for (int q = 0; q < NC; ++q) LDS16(ml[q]) = cm[q];
    }
    WG_BARRIER();
    STAMP(1);
    unsigned lt = 0u;
    {
      u32x4 mf[4];
      mf[0] = LDS16(O_M + l16); mf[1] = LDS16(O_M + 1024 + l16); mf[2] = LDS16(O_M + 2048 + l16);
      if constexpr (kp) lt = *(LAS unsigned*)(lds + O_CT + 8);
      SB();
      static_for<kp ? 8 : 10>([&](auto LL) {
        constexpr int L = decltype(LL)::value;
        smf_a<8 * L>(za[0], mf[L % 4], sidx); SB();
        if (L + 3 < 10) mf[(L + 3) % 4] = LDS16(O_M + (L + 3) * 1024 + l16);
        SB();
        smf_a<80 + 8 * L>(za[1], mf[L % 4], sidx); SB();
        if (L == 0) __builtin_amdgcn_raw_buffer_store_b128(pz, rs, rz_vo, rz_so, 16);
        if (L == 1 && w0) __builtin_amdgcn_raw_ptr_buffer_atomic_add_i32(1, rs, l20, fl_so, 0);
        SB();
        smf_a<160 + 8 * L>(za[2], mf[L % 4], sidx);
        if (L < 2) smf_a<240 + 8 * (L & 1)>(za[3], mf[L % 4], sidx); else smf_v(za[3], mf[L % 4], UV[L < 2 ? 0 : L - 2], sidx);
        SB();
      });
      asm volatile("" :: "v"(pz), "v"(zf));
      if constexpr (kp) ck.edge_z(za, mf[0], mf[1], UV[6], UV[7], sidx); else NOPS(7);
    }
    STAMP(2);
    unsigned zp[4];
    auto zpost = [&](int q) {
      auto x = __builtin_amdgcn_permlane32_swap(f2u(za[q >> 1][2 * (q & 1)]), f2u(za[2 + (q >> 1)][2 * (q & 1)]), false, false);
      auto y = __builtin_amdgcn_permlane32_swap(f2u(za[q >> 1][2 * (q & 1) + 1]), f2u(za[2 + (q >> 1)][2 * (q & 1) + 1]), false, false);
      zp[q] = pack2(u2f(x[0]) + u2f(x[1]), u2f(y[0]) + u2f(y[1])); pin_v(zp[q]);
      if (q == 3) {
        auto u = __builtin_amdgcn_permlane16_swap(zp[0], zp[2], false, false);
        auto v = __builtin_amdgcn_permlane16_swap(zp[1], zp[3], false, false);
        zf = (u32x4){u[0], v[0], u[1], v[1]}; pin_v(zf);
      }
    };
    if constexpr (last) { zpost(0); zpost(1); zpost(2); zpost(3); ST16(zf, rz, z_vo, (unsigned)(s * 40 + ct) * 8192u); return; }
    SB();
    unsigned mP;
    if constexpr (kp) {
      mP = ck.local(sd.tP);
      if (__builtin_expect(RFL(lt) == (unsigned)s + 1u, 0)) {
        fails |= 4;
        const u32x2 r = ann_valid((u32x2){SENT, SENT}, rs, an_ld, sl, dead);
        ck.edge();
        mP = ck.local(sd.T + (unsigned)max32((int)(r[0] - sd.T)) + a.d0);
      }
      *(LAS u32x2*)(lds + O_CT) = (u32x2){(unsigned)s + 1u, mP};
    } else {
      u32x2 v;
      do { v = *(volatile LAS u32x2*)(lds + O_CT); } while (RFL(v[0]) != (unsigned)s + 1u);
      mP = RFL(v[1]);
    }
    STAMP(3);
    SB();
    wait_until(mP);
    SB();
    u32x4 pf[5];
    pf[0] = LD16(rs, p_vo, sp + pA); SB(); zpost(0); NOPS(P_GAP);
    pf[1] = LD16(rs, p_vo, sp + pA + 1024u); SB(); zpost(1); NOPS(P_GAP);
    pf[2] = LD16(rs, p_vo, sp + pB); SB(); zpost(2); NOPS(P_GAP);
    pf[3] = LD16(rs, p_vo, sp + pB + 1024u); SB(); zpost(3); SB();
    pf[4] = LD16(rs, pm_vo, sp); SB();
    u32x2 an;
    if constexpr (kp) { NOPS(15); NOPS(7); an = LD8(rs, an_ld, sl); }
    STAMP(4);
    SB();
    f32x4 d;
    unsigned chk;
    mf32z(d, yf[0], pf[0]); SB(); chk = max(pf[0][0], pf[0][2]); PIN_V(chk); SB();
    mf32(d, yf[1], pf[1]); SB(); chk = mx3(chk, pf[1][0], pf[1][2]); PIN_V(chk); SB();
    mf32(d, yf[2], pf[2]); SB(); chk = mx3(chk, pf[2][0], pf[2][2]); PIN_V(chk); SB();
    mf32(d, yf[3], pf[3]); SB(); chk = mx3(chk, pf[3][0], pf[3][2]); PIN_V(chk); SB();
    mf32(d, zf, pf[4]); SB(); chk = mx3(chk, pf[4][0], pf[4][2]);
    u64 bad = __builtin_amdgcn_ballot_w64(chk == SENT); asm volatile("" : "+s"(bad));
    NOPS(5);
    *(LAS f32x2*)(lds + red_w) = (f32x2){d[0], d[1]}; *(LAS f32x2*)(lds + red_w + 64) = (f32x2){d[2], d[3]};
    SB();
    if (__builtin_expect(bad != 0, 0)) {
      fails |= 2;
      for (unsigned spins = 0; !dead;) {
        __builtin_amdgcn_s_sleep(2);
        bool nd[5]; u32x4 n[5]; unsigned m = 0;
#pragma unroll
        for (int q = 0; q < 5; ++q) { nd[q] = mx4(pf[q]) == SENT; n[q] = LD16(rs, nd[q] ? (q < 4 ? p_vo : pm_vo) : OOB, sp + (q < 2 ? pA : q < 4 ? pB : 0u) + (q & 1) * 1024u); }
#pragma unroll
        for (int q = 0; q < 5; ++q) { if (nd[q]) pf[q] = n[q]; m = max(m, mx4(pf[q])); }
        if (!__any(m == SENT)) break;
        if (++spins > SPIN_CAP) dead = true;
        asm volatile("" ::: "memory");
      }
#pragma unroll
      for (int q = 0; q < 5; ++q) PIN_V(pf[q]);
      NOPS(1);
      mf32z(d, yf[0], pf[0]); mf32(d, yf[1], pf[1]); mf32(d, yf[2], pf[2]); mf32(d, yf[3], pf[3]); mf32(d, zf, pf[4]);
      NOPS(7);
      *(LAS f32x2*)(lds + red_w) = (f32x2){d[0], d[1]}; *(LAS f32x2*)(lds + red_w + 64) = (f32x2){d[2], d[3]};
    }
    STAMP(5);
    if constexpr (w0) {
      asm volatile("s_waitcnt lgkmcnt(0)\n\ts_barrier ; first wave" ::: "memory");
      const f32x4 e = __builtin_bit_cast(f32x4, LDS16(red_r)) + __builtin_bit_cast(f32x4, LDS16(red_r + 16));
      const f32x2 u = (f32x2){e[0], e[1]} + (f32x2){e[2], e[3]};
      unsigned o0, o1, o2, o3;
      asm volatile("s_nop 1\n\tv_add_f32_dpp %0, %4, %4 row_ror:8 row_mask:0xf bank_mask:0xf\n\tv_add_f32_dpp %1, %5, %5 row_ror:8 row_mask:0xf bank_mask:0xf\n\t"
                   "v_cvt_pk_bf16_f32 %2, %0, %1\n\tv_min_u32 %0, -2, %2\n\tv_min_u32 %1, -2, %2\n\ts_nop 1\n\tv_permlane16_swap_b32 %0, %1\n\t"
                   "v_mov_b32 %2, %0\n\tv_mov_b32 %3, %1\n\ts_nop 0\n\tv_permlane32_swap_b32 %0, %2\n\tv_permlane32_swap_b32 %1, %3"
                   : "=&v"(o0), "=&v"(o1), "=&v"(o2), "=&v"(o3) : "v"(u[0]), "v"(u[1]));
      ST16(((u32x4){o0, o1, o2, o3}), rs, m_st, sn);
      LDS16(own_l) = (u32x4){o0, o1, o2, o3};
    } else if (kp) asm volatile("s_waitcnt lgkmcnt(0)\n\ts_barrier" ::: "memory");
    else asm volatile("s_waitcnt lgkmcnt(0)\n\ts_barrier\n\ts_sleep 3" ::: "memory");
    SB();
    STAMP(6);
    if constexpr (kp) {
      sd.next(ann_valid(an, rs, an_ld, sl, dead), a, s == 0);
      mM = ck.local(sd.T + OM_LAG);
      jv = min(jv, dppu<0xB1>(jv)); jhave = max(jhave, RFL(jv));
      LDS16(O_CT + 16) = (u32x4){(unsigned)s + 1u, mM, jhave, 0u};
    } else {
      u32x4 v;
      do { v = *(volatile LAS u32x4*)(lds + O_CT + 16); } while (RFL(v[0]) != (unsigned)s + 1u);
      mM = RFL(v[1]); jhave = max(jhave, RFL(v[2]));
    }
    STAMP(7);
    STAMP_FLUSH(s, mT - OM_LAG * 21u / 8u, mP, fails);
  };
  auto run = [&](auto KIND) {
#pragma unroll 1
    for (int s = 0; s < NSEG - 1; ++s) step(s, std::false_type(), KIND);
    step(NSEG - 1, std::true_type(), KIND);
  };
  if (w == 0) run(std::integral_constant<int, 0>()); else if (w == 3) run(std::integral_constant<int, 2>()); else run(std::integral_constant<int, 1>());
  VM_WAIT(0);
  WG_BARRIER();
  __builtin_amdgcn_raw_ptr_buffer_atomic_add_i32(1, rs, l20, w < 2 ? OFS_CW + CW_SO + rep * 128u + (unsigned)(NSEG - 2 + w) * 1024u : FAR, 0);
  TMARK(3);
}

#define J_FLAG 10
#define J_PACE 0u
template <bool YO>
__device__ __forceinline__ void proj_role(const RecArgs& a, LAS char* lds, int jn) {
  const int lane = fresh_lane(), w = __builtin_amdgcn_readfirstlane(threadIdx.x >> 6), c = lane & 15, g = lane >> 4;
  const __amdgpu_buffer_rsrc_t rs = RSRC(a.sync, SYNC_LAYER), rx = RSRC(a.xf, S_ * DM * 2), ro = YO ? RSRC(a.y, Y_IMG) : RSRC(a.qk, 2 * QK_IMG);
  const unsigned l16 = lane * 16;
#define DMA_P(seg, p) dma1k<((p) & 3) * 1024>(rx, l16, (unsigned)(seg) * 40960u + w * 10240u + ((p) >> 2) * 4096u, (unsigned)(uintptr_t)lds + ((seg) & 1) * 40960u + w * 10240u + ((p) >> 2) * 4096u)
  static_for<10>([&](auto P_) { DMA_P(0, decltype(P_)::value); });
  const int T0 = 20 * (YO ? jn - 40 : jn) + 5 * w;
  u32x4 WV[36];
  {
    const __amdgpu_buffer_rsrc_t rw = YO ? RSRC(a.uf, 80 * 16 * 20480) : RSRC(a.wf, 768 * 20480);
    static_for<100>([&](auto F) {
      constexpr int f = decltype(F)::value;
      const unsigned so = (unsigned)T0 * 20480u + f * 1024u;
      if (f < 64) { ld_a<4 * (f & 63)>(rw, l16, so); VM_WAIT(24); } else WV[f < 64 ? 0 : f - 64] = LDP16(rw, l16, so);
    });
#pragma unroll
    for (int f = 0; f < 36; ++f) PIN_V(WV[f]);
  }
  const unsigned o_vo = YO ? (c >> 3) * 40960u + g * 256u + (c & 7) * 16u : l16;
  unsigned o_so[5];
#pragma unroll
  for (int t = 0; t < 5; ++t) {
    const unsigned T = T0 + t;
    o_so[t] = YO ? (T & 15u) * 81920u + (T >> 5) * 1024u + ((T >> 4) & 1u) * 128u : T < 384u ? T * 1024u : T < 768u ? QK_IMG + (T - 384u) * 1024u : FAR;
  }
  unsigned tnext = mtc();
  TMARK(1);
  u32x4 pend[5];
  auto put = [&](int s, int t) { ST16(pend[t], ro, o_vo, o_so[t] + (unsigned)s * (YO ? NH * 40960u : 384u * 1024u)); };
#pragma unroll 1
  for (int s = 0; s < NSEG; ++s) {
    VM_WAIT(0);
    WG_BARRIER();
    if (J_PACE && s >= 6) coarse_until(tnext);
    if (J_PACE) tnext = mtc() + J_PACE;
    const int sx = s + 1 < NSEG ? s + 1 : s - 1, sp = s > 0 ? s - 1 : (int)(FAR / (YO ? NH * 40960u : 384u * 1024u));
    const unsigned xb = (s & 1) * 40960 + l16;
    f32x4 acc[10]; u32x4 xr[3][2];
#pragma unroll
    for (int kb = 0; kb < 2; ++kb) { xr[kb][0] = LDS16(xb + kb * 1024); xr[kb][1] = LDS16(xb + 20480 + kb * 1024); }
    SB();
    static_for<20>([&](auto KB) {
      constexpr int kb = decltype(KB)::value;
      static_for<10>([&](auto Q) {
        constexpr int q = decltype(Q)::value, f = (q >> 1) * 20 + kb;
        if (f < 64) {
          if (kb == 0) { if (YO) mf32z_ba<4 * (f & 63)>(acc[q], xr[0][q & 1]); else mf32z_aa<4 * (f & 63)>(acc[q], xr[0][q & 1]); }
          else { if (YO) mf32_ba<4 * (f & 63)>(acc[q], xr[kb % 3][q & 1]); else mf32_aa<4 * (f & 63)>(acc[q], xr[kb % 3][q & 1]); }
        } else {
          const u32x4 wv = WV[f < 64 ? 0 : f - 64], xv = xr[kb % 3][q & 1];
          if (kb == 0) { if (YO) mf32z(acc[q], xv, wv); else mf32z(acc[q], wv, xv); }
          else { if (YO) mf32(acc[q], xv, wv); else mf32(acc[q], wv, xv); }
        }
        SB();
        if (q >= 2 && q < 4 && kb + 2 < 20) xr[(kb + 2) % 3][q & 1] = LDS16(xb + (q & 1) * 20480 + (kb + 2) * 1024);
        if constexpr (q == 5 && kb < 5) put(sp, kb);
        if constexpr (q == 5 && kb >= 5 && kb < 15) DMA_P(sx, kb - 5);
        if constexpr (q == 7 && kb == J_FLAG) {
          asm volatile("s_waitcnt vmcnt(%0)\n\ts_barrier" :: "n"(J_FLAG - 4) : "memory");
          __builtin_amdgcn_raw_buffer_store_b32((unsigned)s, rs, l16 << 20, w == 0 ? OFS_CW + CW_JP + jn * 128u : FAR, 16);
        }
        SB();
      });
    });
    NOPS(3);
#pragma unroll
    for (int t = 0; t < 5; ++t) { pend[t] = pack8(acc[2 * t], acc[2 * t + 1]); PIN_V(pend[t]); SB(); }
  }
#pragma unroll
  for (int t = 0; t < 5; ++t) put(NSEG - 1, t);
  VM_WAIT(0);
  WG_BARRIER();
  __builtin_amdgcn_raw_buffer_store_b32((unsigned)NSEG, rs, l16 << 20, w == 0 ? OFS_CW + CW_JP + jn * 128u : FAR, 16);
  TMARK(2);
}

constexpr int T_LD = 84;
#define T_SLP 60
#define T_GRP 5
#define T_FLY 0
__device__ __forceinline__ void token_role(const RecArgs& a, LAS char* lds, int rep) {
  const int tid = threadIdx.x, w = __builtin_amdgcn_readfirstlane(tid >> 6);
  const __amdgpu_buffer_rsrc_t rs = RSRC(a.sync, SYNC_LAYER), ry = RSRC(a.y, Y_IMG), rz = RSRC(a.z, Z_IMG), rp = RSRC(a.pt, PT_IMG);
  LAS float* part = (LAS float*)lds;
  LAS unsigned* tick = (LAS unsigned*)(lds + 4 * 32 * T_LD * 4);
  auto wait_count = [&](unsigned off, unsigned want, bool calm) {
    if (w == 0) {
      for (unsigned spins = 0; spins < SPIN_CAP; ++spins) {
        if (RFL(LD4(rs, off, 0)) >= want) break;
        if (calm) __builtin_amdgcn_s_sleep(T_SLP); else __builtin_amdgcn_s_sleep(6);
        asm volatile("" ::: "memory");
      }
    }
    WG_BARRIER();
  };
#pragma unroll 1
  for (;;) {
    WG_BARRIER();
    if (tid == 0) *tick = __hip_atomic_fetch_add((GAS unsigned*)(a.sync + OFS_CW + CW_TQ + rep * 128), 1u, RLX_AGENT);
    WG_BARRIER();
    const int n = __builtin_amdgcn_readfirstlane(*(volatile LAS unsigned*)tick);
    if (n >= 64) { TMARK(3); return; }
    const int s = rep + 8 * (n >> 3), cg = n & 7;
    const bool calm = s < NSEG - 3;
    int lane = tid & 63; PIN_V(lane);
    const int c = lane & 15, g = lane >> 4;
    u32x4 xo[2];
#pragma unroll
    for (int r = 0; r < 2; ++r) { const int p = r * 256 + tid; if (p < 320) xo[r] = *(const u32x4*)(a.xold + (size_t)(s * 32 + p / 10) * DM + 80 * cg + (p % 10) * 8); }
    wait_count(OFS_CW + CW_SS + max(s - 4, 0) * 128, 32u, true);
    wait_count(OFS_CW + CW_SO + (s * 8 + cg) * 128, 10u, calm);
    const unsigned l16 = lane * 16;
    static_for<50>([&](auto F) {
      constexpr int f = decltype(F)::value, kk = f / 5, t = f % 5;
      if (kk < 8) ld_a1<4 * f>(ry, l16, (unsigned)((s * NH + 8 * w + kk) * 40 + 5 * cg + t) * 1024u);
      else ld_a1<4 * f>(rz, l16, (unsigned)((s * 40 + 5 * cg + t) * NH + 8 * w + 4 * (kk - 8)) * 256u);
      if (f % T_GRP == T_GRP - 1 && calm) VM_WAIT(T_FLY);
    });
    VM_WAIT(40);
    wait_count(OFS_CW + CW_SS + s * 128, 32u, calm);
    u32x4 bf[10][2];
#pragma unroll
    for (int kk = 0; kk < 10; ++kk)
#pragma unroll
      for (int b = 0; b < 2; ++b) {
        bf[kk][b] = LD16(rp, l16, (unsigned)s * PT_SEG + (kk < 8 ? (unsigned)((8 * w + kk) * 2 + b) * 1024u : PT_M + (unsigned)(b * NH + 8 * w + 4 * (kk - 8)) * 256u));
        if (b == 1 && (kk & 1) && calm) VM_WAIT(T_FLY);
      }
    f32x4 acc[5][2];
    static_for<100>([&](auto N) {
      constexpr int n = decltype(N)::value, kk = n / 10, t = (n % 10) >> 1, b = n & 1;
      if (kk == 0) mf32z_aa<4 * t>(acc[t][b], bf[0][b]); else mf32_aa<4 * (5 * kk + t)>(acc[t][b], bf[kk][b]);
    });
    NOPS(7);
#pragma unroll
    for (int t = 0; t < 5; ++t)
#pragma unroll
      for (int b = 0; b < 2; ++b) {
        LAS float* p = part + (w * 32 + 16 * b + c) * T_LD + 16 * t + 8 * (g & 1) + 2 * (g >> 1);
        *(LAS f32x2*)p = (f32x2){acc[t][b][0], acc[t][b][1]}; *(LAS f32x2*)(p + 4) = (f32x2){acc[t][b][2], acc[t][b][3]};
      }
    WG_BARRIER();
#pragma unroll
    for (int r = 0; r < 2; ++r) {
      const int p = r * 256 + tid;
      if (p < 320) {
        const LAS float* q = part + (p / 10) * T_LD + (p % 10) * 8;
        f32x4 s0 = *(const LAS f32x4*)q, s1 = *(const LAS f32x4*)(q + 4);
#pragma unroll
        for (int k = 1; k < 4; ++k) { s0 += *(const LAS f32x4*)(q + k * 32 * T_LD); s1 += *(const LAS f32x4*)(q + k * 32 * T_LD + 4); }
        u32x4 o;
#pragma unroll
        for (int k = 0; k < 2; ++k) {
          o[k] = pack2(bf2f(xo[r][k] & 0xffff) + rbf(s0[2 * k]), bf2f(xo[r][k] >> 16) + rbf(s0[2 * k + 1]));
          o[2 + k] = pack2(bf2f(xo[r][2 + k] & 0xffff) + rbf(s1[2 * k]), bf2f(xo[r][2 + k] >> 16) + rbf(s1[2 * k + 1]));
        }
        *(u32x4*)(a.xnew + (size_t)(s * 32 + p / 10) * DM + 80 * cg + (p % 10) * 8) = o;
      }
    }
  }
}

__global__ __launch_bounds__(256, 1) void rec_kernel(RecArgs a) {
  __shared__ __attribute__((aligned(16))) char lds_[S_END];
  LAS char* lds = (LAS char*)lds_;
  const int b = blockIdx.x, rep = b & 7, il = b >> 3;
  AGPR_FENCE();
  TMARK(0);
  if (il < 14) {
    const int lane = threadIdx.x & 63;
    const unsigned* tab = (const unsigned*)a.cal;
    const unsigned x = tab[64 + (lane >> 3)] & 7u, xr = tab[64 + rep] & 7u;
    unsigned v = tab[(lane & 56) + (((((x & 1u) << 2) | (x >> 1)) ^ lane) & 7u)] & 0x1ffffu;
    v += __shfl_xor(v, 8); v += __shfl_xor(v, 16); v += __shfl_xor(v, 32);
    v = v * 8u + (lane & 7);
    v = min(v, (unsigned)__shfl_xor(v, 1)); v = min(v, (unsigned)__shfl_xor(v, 2)); v = min(v, (unsigned)__shfl_xor(v, 4));
    const unsigned posP = RFL(((((xr & 1u) << 2) | (xr >> 1)) ^ v ^ (unsigned)a.layer) & 7u), posM = posP ^ 4u;
    const int w = __builtin_amdgcn_readfirstlane(threadIdx.x >> 6);
    if (il < 4) {
      if (w < 2) score_role<0>(a, lds, rep, il, posM, posP); else if (w == 2) score_role<1>(a, lds, rep, il, posM, posP); else score_role<2>(a, lds, rep, il, posM, posP);
    } else out_role(a, lds, rep, il - 4, posM, posP);
    return;
  }
  if (il < 19) { if (b != 19 * 8 - 1) proj_role<false>(a, lds, b - 14 * 8); }
  else if (il < 27) proj_role<true>(a, lds, 40 + rep * 8 + il - 19);
  AGPR_FENCE();
  token_role(a, lds, rep);
}

constexpr int KQ = S_ / 4;
__global__ __launch_bounds__(1024, 4) void attb_kernel(const u16* __restrict__ Qp, const u16* __restrict__ Kp, const u16* __restrict__ VbT,
                                                       u16* __restrict__ O) {
  __shared__ float mrg_[4][3][2][13][64];
  const int lane = threadIdx.x & 63, wv = __builtin_amdgcn_readfirstlane(threadIdx.x >> 6), kh = wv >> 2, r = lane & 31, hh = lane >> 5;
  float (*mrg)[2][13][64] = mrg_[wv & 3];
  const int hd = (blockIdx.x & 7) * 4 + ((blockIdx.x >> 3) & 3), qb = blockIdx.x >> 5;
  const int q0 = qb * 256 + (wv & 3) * 64;
  u32x4 qf[2][2];
#pragma unroll
  for (int qt = 0; qt < 2; ++qt)
#pragma unroll
    for (int ks = 0; ks < 2; ++ks) qf[qt][ks] = *(const u32x4*)(Qp + ((size_t)hd * S_ + q0 + qt * 32 + r) * 32 + 16 * ks + 8 * hh);
  const u16* kp = Kp + ((size_t)hd * S_ + kh * KQ + r) * 32 + 8 * hh;
  const u16* vp = VbT + (((size_t)hd * 64 + kh * (KQ / 32)) * 2 * 64 + lane) * 8;
  f32x16 o[2];
#pragma unroll
  for (int qt = 0; qt < 2; ++qt)
#pragma unroll
    for (int i = 0; i < 16; ++i) o[qt][i] = 0.f;
  float m[2] = {0.f, 0.f};
  bf16x8 kv[2][4];
  kv[0][0] = *(const bf16x8*)(kp); kv[0][1] = *(const bf16x8*)(kp + 16); kv[0][2] = *(const bf16x8*)(vp); kv[0][3] = *(const bf16x8*)(vp + 512);
#pragma unroll 1
  for (int k2 = 0; k2 < KQ; k2 += 64) {
#pragma unroll
    for (int u = 0; u < 2; ++u) {
      const int k0 = k2 + 32 * u;
      {
        const int kn = (k0 + 32 < KQ) ? k0 + 32 : k0;
        kv[u ^ 1][0] = *(const bf16x8*)(kp + (size_t)kn * 32); kv[u ^ 1][1] = *(const bf16x8*)(kp + (size_t)kn * 32 + 16);
        kv[u ^ 1][2] = *(const bf16x8*)(vp + (size_t)kn * 32); kv[u ^ 1][3] = *(const bf16x8*)(vp + (size_t)kn * 32 + 512);
      }
      f32x16 sa[2];
#pragma unroll
      for (int qt = 0; qt < 2; ++qt) {
#pragma unroll
        for (int i = 0; i < 16; ++i) sa[qt][i] = 0.f;
        sa[qt] = __builtin_amdgcn_mfma_f32_32x32x16_bf16(kv[u][0], __builtin_bit_cast(bf16x8, qf[qt][0]), sa[qt], 0, 0, 0);
        sa[qt] = __builtin_amdgcn_mfma_f32_32x32x16_bf16(kv[u][1], __builtin_bit_cast(bf16x8, qf[qt][1]), sa[qt], 0, 0, 0);
      }
#pragma unroll
      for (int qt = 0; qt < 2; ++qt) {
        float mx = fmx(fmx(sa[qt][0], sa[qt][1]), sa[qt][2]);
#pragma unroll
        for (int i = 3; i < 15; i += 2) mx = fmx(fmx(mx, sa[qt][i]), sa[qt][i + 1]);
        mx = fmx(mx, sa[qt][15]);
        if (__builtin_expect(__any(mx > 6.f) || k0 == 0, 0)) {
          { auto rr = __builtin_amdgcn_permlane32_swap(f2u(mx), f2u(mx), false, false); mx = fmx(u2f(rr[0]), u2f(rr[1])); }
          if (k0 != 0) mx = fmx(mx, 0.f);
          const float mn = m[qt] + mx, hi = rbf(mn), lo = rbf(mn - hi), me = hi + lo;
          const float delta = me - m[qt], alpha = k0 == 0 ? 1.0f : __builtin_amdgcn_exp2f(-delta);
          m[qt] = me;
          if (hh == 0) qf[qt][1][2] = pack2(-hi, -lo);
#pragma unroll
          for (int i = 0; i < 16; ++i) { sa[qt][i] -= delta; o[qt][i] *= alpha; }
        }
        float p[16];
#pragma unroll
        for (int i = 0; i < 16; ++i) p[i] = __builtin_amdgcn_exp2f(sa[qt][i]);
        bf16x8 p0 = mk8(pack2(p[0], p[1]), pack2(p[2], p[3]), pack2(p[4], p[5]), pack2(p[6], p[7]));
        bf16x8 p1 = mk8(pack2(p[8], p[9]), pack2(p[10], p[11]), pack2(p[12], p[13]), pack2(p[14], p[15]));
        o[qt] = __builtin_amdgcn_mfma_f32_32x32x16_bf16(kv[u][2], p0, o[qt], 0, 0, 0);
        o[qt] = __builtin_amdgcn_mfma_f32_32x32x16_bf16(kv[u][3], p1, o[qt], 0, 0, 0);
      }
    }
  }
  if (kh > 0) {
#pragma unroll
    for (int qt = 0; qt < 2; ++qt) {
#pragma unroll
      for (int i = 0; i < 12; ++i) mrg[kh - 1][qt][i][lane] = o[qt][i];
      mrg[kh - 1][qt][12][lane] = m[qt];
    }
  }
  __syncthreads();
  if (kh == 0) {
#pragma unroll
    for (int qt = 0; qt < 2; ++qt) {
      float m1 = mrg[0][qt][12][lane], m2 = mrg[1][qt][12][lane], m3 = mrg[2][qt][12][lane];
      float mn = fmaxf(fmaxf(m[qt], m1), fmaxf(m2, m3));
      float a0 = __builtin_amdgcn_exp2f(m[qt] - mn), a1 = __builtin_amdgcn_exp2f(m1 - mn);
      float a2 = __builtin_amdgcn_exp2f(m2 - mn), a3 = __builtin_amdgcn_exp2f(m3 - mn);
      float v[12];
#pragma unroll
      for (int i = 0; i < 12; ++i) v[i] = (o[qt][i] * a0 + mrg[0][qt][i][lane] * a1) + (mrg[1][qt][i][lane] * a2 + mrg[2][qt][i][lane] * a3);
      auto lr = __builtin_amdgcn_permlane32_swap(f2u(v[8]), f2u(v[8]), false, false);
      float inv = 1.0f / u2f(lr[1]);
      u16* op = O + (size_t)(q0 + qt * 32 + r) * DM + hd * BD + 4 * hh;
      u32x2 x0 = {pack2(v[0] * inv, v[1] * inv), pack2(v[2] * inv, v[3] * inv)};
      *(u32x2*)(op) = x0;
      u32x2 x1 = {pack2(v[4] * inv, v[5] * inv), pack2(v[6] * inv, v[7] * inv)};
      *(u32x2*)(op + 8) = x1;
      if (hh == 0) {
        u32x2 x2 = {pack2(v[8] * inv, v[9] * inv), pack2(v[10] * inv, v[11] * inv)};
        *(u32x2*)(op + 16) = x2;
      }
    }
  }
}

#define P_TAIL 540
#define P_D0 75
#define P_G2C 700
#define P_G2T 240
#define P_DUR0 950
#define P_TOL 40
#define P_SLOW 800
extern "C" void kernel_launch(void* const* d_in, const int* in_sizes, int n_in, void* d_out, int out_size, void* d_ws,
                              size_t ws_size, hipStream_t stream) {
  const u16* A_x = (const u16*)d_in[0];
  const u16* A_mem0 = (const u16*)d_in[1];
  const u16* A_Wq = (const u16*)d_in[2];
  const u16* A_Wk = (const u16*)d_in[3];
  const u16* A_Wv = (const u16*)d_in[4];
  const u16* A_Wo = (const u16*)d_in[5];
  const u16* B_wqkv = (const u16*)d_in[8];
  const u16* B_wo = (const u16*)d_in[9];
  char* ws = (char*)(((uintptr_t)d_ws + ((size_t)2 << 20) - 1) & ~(uintptr_t)(((size_t)2 << 20) - 1));
  u16* WF = (u16*)(ws + OFF_WF); u16* UF = (u16*)(ws + OFF_UF);
  u16* WqkvBT = (u16*)(ws + OFF_WQKVBT); u16* WoBT = (u16*)(ws + OFF_WOBT);
  u16* xf = (u16*)(ws + OFF_XF); u16* xn = (u16*)(ws + OFF_XN); u16* xa = (u16*)(ws + OFF_XA); u16* xb = (u16*)(ws + OFF_XB);
  float* part = (float*)(ws + OFF_PART);
  u16* Qb = (u16*)(ws + OFF_QB); u16* Kb = (u16*)(ws + OFF_KB); u16* VbT = (u16*)(ws + OFF_VBT); u16* Ob = (u16*)(ws + OFF_OB);
  {
    ProArgs pa;
    TrJobs& jb = pa.jobs;
    int t0 = 0;
    auto add = [&](int i, const u16* s, u16* d, int R, int C, int frag) { jb.j[i].src = s; jb.j[i].dst = d; jb.j[i].R = R; jb.j[i].C = C; jb.j[i].tile0 = t0; jb.j[i].frag = frag; t0 += (R / 64) * (C / 64); };
    add(0, A_Wq, WF, DM, HD, 1);
    add(1, A_Wk, WF + (size_t)HD * DM, DM, HD, 1);
    add(2, B_wqkv, WqkvBT, DM, 3 * DM, 0);
    add(3, B_wo, WoBT, DM, DM, 0);
    pa.x = A_x; pa.xf = xf; pa.qb = Qb; pa.sync = ws; pa.wo = A_Wo; pa.wv = A_Wv; pa.uf = UF; pa.mem0 = A_mem0;
    pa.pad = 0; pa.nu = NH * 8;
    pa.ntr = t0; pa.npad = (BH * 64 * 2 * 24 + 255) / 256; pa.nsync = (int)(2 * SYNC_LAYER / 16384); pa.nln = S_ / 4;
    prologue_kernel<<<8 + pa.nu + pa.ntr + pa.npad + pa.nsync + pa.nln, 256, 0, stream>>>(pa);
  }
  const u16* xcur = A_x;
  for (int layer = 0; layer < 2; ++layer) {
    {
      RecArgs r{WF, UF, xf, ws + OFF_QK, ws + OFF_Y, ws + OFF_Z, ws + OFF_PT, ws + (size_t)layer * SYNC_LAYER, ws + OFS_CAL + ROW, xcur, xa, nullptr,
                layer, P_TAIL, P_D0, P_G2C, P_G2T, P_DUR0, P_TOL, P_SLOW};
      rec_kernel<<<256, 256, 0, stream>>>(r);
    }
    resln_kernel<<<S_ / 4, 256, 0, stream>>>(xa, part, 0, nullptr, xn, 0);
    {
      GemmArgs g{xn, WqkvBT, Qb, Kb, VbT, DM, DM, DM, 3 * DM / 64, S_ / 128, (S_ / 128) * (3 * DM / 64) / 8};
      gemm_kernel<EPI_QKV_B, 64><<<(S_ / 128) * (3 * DM / 64), 256, 0, stream>>>(g);
    }
    attb_kernel<<<BH * 8, 1024, 0, stream>>>(Qb, Kb, VbT, Ob);
    {
      GemmArgs g{Ob, WoBT, part, nullptr, nullptr, DM, DM, DM / 2, DM / 64, S_ / 128, (S_ / 128) * (DM / 64) * 2 / 8};
      gemm_kernel<EPI_PART, 64><<<(S_ / 128) * (DM / 64) * 2, 256, 0, stream>>>(g);
    }
    if (layer == 0) {
      resln_kernel<<<S_ / 4, 256, 0, stream>>>(xa, part, 2, xb, xf, 1);
      xcur = xb;
    } else {
      resln_kernel<<<S_ / 4, 256, 0, stream>>>(xa, part, 2, (u16*)d_out, nullptr, 0);
    }
  }
}
```

```cpp
#include <hip/hip_runtime.h>
#include <hip/hip_bf16.h>
#include <stdint.h>
#include <type_traits>
#include <utility>

typedef unsigned short u16;
typedef unsigned long long u64;
typedef __attribute__((ext_vector_type(8))) short bf16x8;
typedef __attribute__((ext_vector_type(4))) short bf16x4;
typedef __attribute__((ext_vector_type(4))) float f32x4;
typedef __attribute__((ext_vector_type(2))) float f32x2;
typedef __attribute__((ext_vector_type(16))) float f32x16;
typedef __attribute__((ext_vector_type(4))) unsigned u32x4;
typedef __attribute__((ext_vector_type(2))) unsigned u32x2;
#define GAS __attribute__((address_space(1)))
#define LAS __attribute__((address_space(3)))

constexpr int S_ = 2048, DM = 640;
constexpr int NH = 32, DH = 192, HD = NH * DH;
constexpr int MEM = 8, SEG = 32, NSEG = S_ / SEG;
constexpr int BH = 32, BD = 20;

constexpr size_t al256(size_t x) { return (x + 255) & ~(size_t)255; }
constexpr unsigned ROW = 32768;
constexpr unsigned M_SLOT = 4 * ROW;
constexpr unsigned OFS_P = 16 * ROW;
constexpr unsigned P_SLOT = 5 * ROW;
constexpr unsigned OFS_CAL = 26 * ROW;
constexpr unsigned OFS_CW = 28 * ROW;
constexpr unsigned CW_JP = 0;
constexpr unsigned CW_SS = CW_JP + 104 * 128;
constexpr unsigned CW_SO = CW_SS + 64 * 128;
constexpr unsigned CW_TQ = CW_SO + 512 * 128;
constexpr unsigned CW_TC = CW_TQ + 8 * 128;
constexpr unsigned CW_END = CW_TC + 64 * 128;
static_assert(CW_END <= 4 * ROW, "control words");
constexpr unsigned SYNC_LAYER = 32 * ROW;
constexpr size_t OFF_WF = 2 * (size_t)SYNC_LAYER;
constexpr size_t OFF_UF = OFF_WF + (size_t)768 * 20480;
constexpr size_t OFF_WQKVBT = OFF_UF + (size_t)80 * 16 * 20480;
constexpr size_t OFF_WOBT = OFF_WQKVBT + al256((size_t)3 * DM * DM * 2);
constexpr size_t OFF_XF = OFF_WOBT + al256((size_t)DM * DM * 2);
constexpr size_t OFF_XN = OFF_XF + al256((size_t)S_ * DM * 2);
constexpr size_t OFF_XA = OFF_XN + al256((size_t)S_ * DM * 2);
constexpr size_t OFF_XB = OFF_XA + al256((size_t)S_ * DM * 2);
constexpr unsigned QK_IMG = NSEG * NH * 12 * 1024;
constexpr size_t OFF_QK = OFF_XB + al256((size_t)S_ * DM * 2);
constexpr unsigned Y_IMG = NSEG * NH * 40 * 1024;
constexpr size_t OFF_Y = OFF_QK + 2 * (size_t)QK_IMG;
constexpr unsigned Z_IMG = NSEG * 40 * NH * 256;
constexpr size_t OFF_Z = OFF_Y + Y_IMG;
constexpr unsigned PT_M = NH * 2048, PT_SEG = PT_M + 2 * NH * 256, PT_IMG = NSEG * PT_SEG;
constexpr size_t OFF_PT = OFF_Z + Z_IMG;
constexpr size_t OFF_QB = OFF_PT + PT_IMG;
constexpr size_t OFF_KB = OFF_QB + al256((size_t)BH * S_ * 32 * 2);
constexpr size_t OFF_VBT = OFF_KB + al256((size_t)BH * S_ * 32 * 2);
constexpr size_t WS_TOTAL = OFF_VBT + al256((size_t)BH * S_ * 32 * 2);
constexpr size_t OFF_PART = OFF_Y;
constexpr size_t OFF_OB = OFF_QK;
static_assert(WS_TOTAL + ((size_t)2 << 20) <= ((size_t)256 << 20), "workspace budget");

__device__ __forceinline__ float u2f(unsigned v) { return __builtin_bit_cast(float, v); }
__device__ __forceinline__ unsigned f2u(float v) { return __builtin_bit_cast(unsigned, v); }
__device__ __forceinline__ float bf2f(unsigned v) { return __builtin_bit_cast(float, v << 16); }
__device__ __forceinline__ unsigned f2bf(float f) { __bf16 b = (__bf16)f; return (unsigned)__builtin_bit_cast(u16, b); }
__device__ __forceinline__ float rbf(float f) { return bf2f(f2bf(f)); }
typedef __attribute__((ext_vector_type(2))) __bf16 hbf16x2;
__device__ __forceinline__ unsigned pack2(float lo, float hi) { f32x2 v = {lo, hi}; hbf16x2 b = __builtin_convertvector(v, hbf16x2); return __builtin_bit_cast(unsigned, b); }
__device__ __forceinline__ f32x4 mfma32(bf16x8 a, bf16x8 b, f32x4 c) { return __builtin_amdgcn_mfma_f32_16x16x32_bf16(a, b, c, 0, 0, 0); }
__device__ __forceinline__ f32x4 mfma16(bf16x4 a, bf16x4 b, f32x4 c) { return __builtin_amdgcn_mfma_f32_16x16x16bf16_1k(a, b, c, 0, 0, 0); }
typedef __attribute__((ext_vector_type(8))) unsigned u32x8;
typedef __attribute__((ext_vector_type(8))) __bf16 hbf16x8;
typedef __attribute__((ext_vector_type(16))) __bf16 hbf16x16;
template <typename R>
__device__ __forceinline__ void store16_(u32x4 v, R r, unsigned vo, unsigned so, bool sc1) {
  if (sc1) __builtin_amdgcn_raw_buffer_store_b128(v, r, vo, so, 16); else __builtin_amdgcn_raw_buffer_store_b128(v, r, vo, so, 0);
  asm volatile("s_nop 1" :: "v"(v));
}
#define store16(v, r, vo, so, aux) store16_((v), (r), (vo), (so), (aux) != 0)
__device__ __forceinline__ bf16x4 mk4(unsigned lo, unsigned hi) { u32x2 v = {lo, hi}; return __builtin_bit_cast(bf16x4, v); }
__device__ __forceinline__ bf16x8 mk8(unsigned a, unsigned b, unsigned c, unsigned d) { u32x4 v = {a, b, c, d}; return __builtin_bit_cast(bf16x8, v); }
__device__ __forceinline__ unsigned dpp_ror8(unsigned v) { return (unsigned)__builtin_amdgcn_update_dpp(0, (int)v, 0x128, 0xf, 0xf, false); }

__device__ __forceinline__ void resln_rows(int blk, const u16* __restrict__ xold, const float* __restrict__ part, int nsplit,
                                           u16* __restrict__ xnew, u16* __restrict__ xn, int frag) {
  const int row = blk * 4 + (threadIdx.x >> 6), lane = threadIdx.x & 63;
  const bool two = lane < 16;
  float v[16];
#pragma unroll
  for (int hf = 0; hf < 2; ++hf) {
    if (hf == 1 && !two) {
#pragma unroll
      for (int i = 0; i < 8; ++i) v[8 + i] = 0.f;
      continue;
    }
    const int col = (hf * 64 + lane) * 8;
    u32x4 xo = *(const u32x4*)(xold + (size_t)row * DM + col);
    float a[8];
#pragma unroll
    for (int i = 0; i < 4; ++i) { unsigned u = xo[i]; a[2 * i] = bf2f(u & 0xffff); a[2 * i + 1] = bf2f(u >> 16); }
    if (nsplit > 0) {
      f32x4 s0 = {0.f, 0.f, 0.f, 0.f}, s1 = {0.f, 0.f, 0.f, 0.f};
      for (int s = 0; s < nsplit; ++s) {
        const float* pp = part + ((size_t)s * S_ + row) * DM + col;
        s0 += *(const f32x4*)pp; s1 += *(const f32x4*)(pp + 4);
      }
#pragma unroll
      for (int i = 0; i < 4; ++i) { a[i] = rbf(a[i] + rbf(s0[i])); a[4 + i] = rbf(a[4 + i] + rbf(s1[i])); }
      if (xnew) {
        u32x4 o = {pack2(a[0], a[1]), pack2(a[2], a[3]), pack2(a[4], a[5]), pack2(a[6], a[7])};
        *(u32x4*)(xnew + (size_t)row * DM + col) = o;
      }
    }
#pragma unroll
    for (int i = 0; i < 8; ++i) v[hf * 8 + i] = a[i];
  }
  if (!xn) return;
  float s = 0.f;
#pragma unroll
  for (int i = 0; i < 16; ++i) s += v[i];
#pragma unroll
  for (int o = 32; o >= 1; o >>= 1) s += __shfl_xor(s, o);
  const float mean = s * (1.0f / DM);
  float q = 0.f;
#pragma unroll
  for (int i = 0; i < 8; ++i) { float d = v[i] - mean; q += d * d; }
  if (two) {
#pragma unroll
    for (int i = 8; i < 16; ++i) { float d = v[i] - mean; q += d * d; }
  }
#pragma unroll
  for (int o = 32; o >= 1; o >>= 1) q += __shfl_xor(q, o);
  const float var = rbf(q * (1.0f / DM));
  const float mb = rbf(mean);
  const float den = rbf(sqrtf(rbf(var + 1e-5f)));
#pragma unroll
  for (int hf = 0; hf < 2; ++hf) {
    if (hf == 1 && !two) continue;
    const int ch = hf * 64 + lane;
    float y[8];
#pragma unroll
    for (int i = 0; i < 8; ++i) y[i] = rbf(v[hf * 8 + i] - mb) / den;
    u32x4 o = {pack2(y[0], y[1]), pack2(y[2], y[3]), pack2(y[4], y[5]), pack2(y[6], y[7])};
    if (frag) *(u32x4*)(xn + ((((size_t)(row >> 4)) * 20 + (ch >> 2)) * 64 + (ch & 3) * 16 + (row & 15)) * 8) = o;
    else *(u32x4*)(xn + (size_t)row * DM + ch * 8) = o;
  }
}

__global__ __launch_bounds__(256) void resln_kernel(const u16* __restrict__ xold, const float* __restrict__ part, int nsplit,
                                                    u16* __restrict__ xnew, u16* __restrict__ xn, int frag) {
  resln_rows(blockIdx.x, xold, part, nsplit, xnew, xn, frag);
}
__device__ __forceinline__ void pad_images(u16* __restrict__ qb, int blk) {
  u16* kb = qb + (size_t)BH * S_ * 32; u16* vbt = kb + (size_t)BH * S_ * 32;
  const int i = blk * 256 + threadIdx.x;
  if (i < BH * S_) {
    const size_t o = (size_t)(i & ~31) * 32 + 512 + (i & 31) * 8;
    *(u32x2*)(qb + o + 4) = (u32x2){0u, 0u}; *(u32x4*)(qb + o + 256) = (u32x4){0u, 0u, 0u, 0u};
    *(u32x2*)(kb + o + 4) = (u32x2){0x3f803f80u, 0u}; *(u32x4*)(kb + o + 256) = (u32x4){0u, 0u, 0u, 0u};
  }
  if (i < BH * 64 * 2 * 24) {
    int blk = i / 24, r = i - blk * 24;
    int e = 20 + (r % 12), hh = r / 12;
    unsigned f = (e == 20) ? 0x3f803f80u : 0u;
    u32x4 z = {f, f, f, f};
    *(u32x4*)(vbt + ((size_t)blk * 64 + e + 32 * hh) * 8) = z;
  }
}

enum { EPI_PART = 1, EPI_QKV_B = 2 };
struct GemmArgs {
  const u16* A; const u16* Bt; void* o0; void* o1; void* o2;
  int lda; int ldb; int Ksplit; int ntn;
  int ntm; int nblk8;
};
template <int EPI, int BN>
__global__ __launch_bounds__(256, 2) void gemm_kernel(GemmArgs g) {
  constexpr int NT = BN / 32;
  __shared__ __attribute__((aligned(16))) u16 smem[2][(128 + BN) * 64];
  const int tid = threadIdx.x, lane = tid & 63, w = tid >> 6;
  const int wr = w >> 1, wc = w & 1, c = lane & 15, gq = lane >> 4;
  int b = blockIdx.x;
  if (g.nblk8) b = (b & 7) * g.nblk8 + (b >> 3);
  const int tiles = g.ntm * g.ntn;
  const int split = b / tiles; b -= split * tiles;
  const int bm = b % g.ntm, bn = b / g.ntm;
  const u16* Ag = g.A + (size_t)bm * 128 * g.lda + (size_t)split * g.Ksplit;
  const u16* Bg = g.Bt + (size_t)bn * BN * g.ldb + (size_t)split * g.Ksplit;
  const int nk = g.Ksplit / 64;
  auto stage = [&](int buf, int kt) {
#pragma unroll
    for (int i = 0; i < 4; ++i) {
      int p = i * 256 + tid, row = p >> 3, ch = (p & 7) ^ (row & 7);
      __builtin_amdgcn_global_load_lds((const unsigned*)(Ag + (size_t)row * g.lda + kt * 64 + ch * 8),
                                       (__attribute__((address_space(3))) unsigned*)&smem[buf][p * 8], 16, 0, 0);
    }
#pragma unroll
    for (int i = 0; i < BN / 32; ++i) {
      int p = i * 256 + tid, row = p >> 3, ch = (p & 7) ^ (row & 7);
      __builtin_amdgcn_global_load_lds((const unsigned*)(Bg + (size_t)row * g.ldb + kt * 64 + ch * 8),
                                       (__attribute__((address_space(3))) unsigned*)&smem[buf][128 * 64 + p * 8], 16, 0, 0);
    }
  };
  f32x4 acc[4][NT];
#pragma unroll
  for (int m = 0; m < 4; ++m)
#pragma unroll
    for (int n = 0; n < NT; ++n) acc[m][n] = (f32x4){0.f, 0.f, 0.f, 0.f};
  bool swapped;
  if (EPI == EPI_QKV_B) swapped = (bn * BN < 2 * DM);
  else swapped = true;
  stage(0, 0);
  for (int kt = 0; kt < nk; ++kt) {
    int cur = kt & 1;
    asm volatile("s_waitcnt vmcnt(0)" ::: "memory");
    __syncthreads();
    if (kt + 1 < nk) stage(cur ^ 1, kt + 1);
    const u16* sa = smem[cur];
    const u16* sb = smem[cur] + 128 * 64;
#pragma unroll
    for (int kk = 0; kk < 2; ++kk) {
      bf16x8 af[4], bfr[NT];
#pragma unroll
      for (int m = 0; m < 4; ++m) {
        int row = wr * 64 + m * 16 + c;
        af[m] = *(const bf16x8*)(sa + row * 64 + (((kk * 4 + gq) ^ (row & 7)) * 8));
      }
#pragma unroll
      for (int n = 0; n < NT; ++n) {
        int row = wc * (BN / 2) + n * 16 + c;
        bfr[n] = *(const bf16x8*)(sb + row * 64 + (((kk * 4 + gq) ^ (row & 7)) * 8));
      }
      if (swapped) {
#pragma unroll
        for (int m = 0; m < 4; ++m)
#pragma unroll
          for (int n = 0; n < NT; ++n) acc[m][n] = mfma32(bfr[n], af[m], acc[m][n]);
      } else {
#pragma unroll
        for (int m = 0; m < 4; ++m)
#pragma unroll
          for (int n = 0; n < NT; ++n) acc[m][n] = mfma32(af[m], bfr[n], acc[m][n]);
      }
    }
  }
  const int row0 = bm * 128 + wr * 64, col0 = bn * BN + wc * (BN / 2);
  if (EPI == EPI_PART) {
    float* dst = (float*)g.o0 + (size_t)split * S_ * DM;
#pragma unroll
    for (int m = 0; m < 4; ++m)
#pragma unroll
      for (int n = 0; n < NT; ++n)
        *(f32x4*)(dst + (size_t)(row0 + m * 16 + c) * DM + col0 + n * 16 + gq * 4) = acc[m][n];
  } else {
    if (swapped) {
#pragma unroll
      for (int m = 0; m < 4; ++m)
#pragma unroll
        for (int n = 0; n < NT; ++n) {
          int nn = col0 + n * 16 + gq * 4;
          u16* dst = (nn < DM) ? (u16*)g.o0 : (u16*)g.o1;
          int cc = (nn < DM) ? nn : nn - DM;
          int hd = cc / BD, e = cc - hd * BD;
          const float qs = (nn < DM) ? 0.22360679774997896f * 1.4426950408889634f : 1.0f;
          u32x2 v = {pack2(acc[m][n][0] * qs, acc[m][n][1] * qs), pack2(acc[m][n][2] * qs, acc[m][n][3] * qs)};
          const int t = row0 + m * 16 + c;
          *(u32x2*)(dst + ((size_t)hd * S_ + (t & ~31)) * 32 + (e >> 3) * 256 + (t & 31) * 8 + (e & 4)) = v;
        }
    } else {
      u16* dst = (u16*)g.o2;
#pragma unroll
      for (int m = 0; m < 4; ++m)
#pragma unroll
        for (int n = 0; n < NT; ++n) {
          int cc = col0 + n * 16 + c - 2 * DM;
          int hd = cc / BD, e = cc - hd * BD;
          int t0 = row0 + m * 16;
          u32x2 v = {pack2(acc[m][n][0], acc[m][n][1]), pack2(acc[m][n][2], acc[m][n][3])};
          *(u32x2*)(dst + ((((size_t)hd * 64 + (t0 >> 5)) * 2 + ((t0 >> 4) & 1)) * 64 + e + 32 * (gq >> 1)) * 8 + (gq & 1) * 4) = v;
        }
    }
  }
}

__device__ __forceinline__ bf16x4 lds_tr(const u16* p) { return __builtin_amdgcn_ds_read_tr16_b64_v4i16((LAS bf16x4*)p); }
__device__ __forceinline__ bf16x8 cat8(bf16x4 a, bf16x4 b) { return __builtin_shufflevector(a, b, 0, 1, 2, 3, 4, 5, 6, 7); }
__device__ __forceinline__ unsigned rtc() { return (unsigned)__builtin_amdgcn_s_memrealtime(); }
__device__ __forceinline__ unsigned mtc() { return (unsigned)__builtin_amdgcn_s_memtime(); }
constexpr unsigned OOB = 0x80000000u;
constexpr unsigned FAR = 0x40000000u;
constexpr unsigned SENT = 0xffffffffu;
struct TrJob { const u16* src; u16* dst; int R; int C; int tile0; int frag; };
struct TrJobs { TrJob j[4]; };
__device__ __forceinline__ void transpose_tile(const TrJobs& jobs, int b, u16 (*tile)[66]) {
  int ji = 0;
#pragma unroll
  for (int i = 1; i < 4; ++i) if (b >= jobs.j[i].tile0) ji = i;
  const u16* src = jobs.j[ji].src; u16* dst = jobs.j[ji].dst;
  const int R = jobs.j[ji].R, C = jobs.j[ji].C, frag = jobs.j[ji].frag;
  const int t = b - jobs.j[ji].tile0, tc = C / 64;
  const int r0 = (t / tc) * 64, c0 = (t % tc) * 64;
  const int tid = threadIdx.x;
#pragma unroll
  for (int i = 0; i < 2; ++i) {
    const int p = i * 256 + tid, r = p >> 3, c = (p & 7) * 8;
    bf16x8 v = *(const bf16x8*)(src + (size_t)(r0 + r) * C + c0 + c);
#pragma unroll
    for (int k = 0; k < 8; ++k) tile[r][c + k] = (u16)v[k];
  }
  __syncthreads();
#pragma unroll
  for (int i = 0; i < 2; ++i) {
    const int p = i * 256 + tid;
    const int c = frag ? (p >> 7) * 16 + (p & 15) : p >> 3, r = frag ? ((p >> 4) & 7) * 8 : (p & 7) * 8;
    bf16x8 v;
#pragma unroll
    for (int k = 0; k < 8; ++k) v[k] = (short)tile[r + k][c];
    if (frag) *(bf16x8*)(dst + ((size_t)((c0 >> 4) + (p >> 7)) * (R >> 5) + (r0 >> 5)) * 512 + (p & 127) * 8) = v;
    else *(bf16x8*)(dst + (size_t)(c0 + c) * R + r0 + r) = v;
  }
}
struct ProArgs { TrJobs jobs; const u16* x; u16* xf; u16* qb; char* sync; const u16* wo; const u16* wv; u16* uf; const u16* mem0;
                 int ntr; int npad; int nsync; int nln; int nu; int pad; };
#define CAL_N 6
constexpr int WO_LD = 88;
__global__ __launch_bounds__(256, 2) void prologue_kernel(ProArgs a) {
  __shared__ __attribute__((aligned(16))) u16 smem[192 * WO_LD];
  int b = blockIdx.x;
  const int tid = threadIdx.x, lane = tid & 63, wq = __builtin_amdgcn_readfirstlane(tid >> 6), c = lane & 15, g = lane >> 4;
  if (b < 32) {
    const __amdgpu_buffer_rsrc_t rs = __builtin_amdgcn_make_buffer_rsrc(a.sync, 0, (int)SYNC_LAYER, 0x00020000);
    if (wq) return;
    unsigned best = 65535u;
    const u32x4 z = {0u, 0u, 0u, 0u};
    const unsigned l0 = OFS_CAL + (b >> 3) * 8192u + (b & 7) * 256u, st = lane < 32 ? l0 + (lane >> 3) * 2048u + (lane & 7) * 16u : OOB;
    store16(z, rs, st, 0, 16);
#pragma unroll 1
    for (int r = 0; r < CAL_N; ++r) {
      store16(z, rs, st, ((r + 1) & 1) * 128u, 16);
      asm volatile("s_waitcnt vmcnt(0)" ::: "memory");
#pragma unroll 1
      for (int q = 0; q < 4; ++q) {
        unsigned t0, t1, x;
        const unsigned vo = l0 + (r & 1) * 128u + q * 2048u;
        asm volatile("s_memtime vcc\n\ts_waitcnt lgkmcnt(0)\n\ts_mov_b32 %0, vcc_lo\n\tbuffer_load_dword %2, %3, %4, 0 offen sc1\n\ts_waitcnt vmcnt(0)\n\t"
                     "s_memtime vcc\n\ts_waitcnt lgkmcnt(0)\n\ts_mov_b32 %1, vcc_lo" : "=&s"(t0), "=&s"(t1), "=&v"(x) : "v"(vo), "s"(rs) : "vcc", "memory");
        if (lane == q) best = min(best, t1 - t0);
      }
    }
    unsigned* tab = (unsigned*)(a.sync + OFS_CAL + ROW);
    best += __shfl_xor(best, 1);
    if (lane < 4 && !(lane & 1)) tab[(b & 7) * 8 + (b >> 3) * 2 + (lane >> 1)] = best;
    if (tid == 0 && b < 8) tab[64 + b] = (unsigned)__builtin_amdgcn_s_getreg(20 | (3 << 11)) & 7u;
    return;
  }
  b -= 32;
  if (b < a.nu) {
    const int h = b >> 3, cg = b & 7;
    const u16* ap = a.wv + (size_t)(160 * wq + 8 * (c >> 2) + (c & 3)) * HD + h * DH + 8 * g;
    bf16x8 af[2][2][6];
    auto load_a = [&](int pr, int buf) {
#pragma unroll
      for (int kp = 0; kp < 6; ++kp) {
        af[buf][0][kp] = *(const bf16x8*)(ap + (size_t)(32 * pr) * HD + 32 * kp);
        af[buf][1][kp] = *(const bf16x8*)(ap + (size_t)(32 * pr + 4) * HD + 32 * kp);
      }
    };
    bf16x8 st[8];
#pragma unroll
    for (int i = 0; i < 8; ++i) {
      const int p = i * 256 + tid, row = p / 10, ch = p - row * 10;
      if (p < 1920) st[i] = *(const bf16x8*)(a.wo + (size_t)(h * DH + row) * DM + 80 * cg + ch * 8);
    }
    load_a(0, 0);
#pragma unroll
    for (int i = 0; i < 8; ++i) {
      const int p = i * 256 + tid, row = p / 10, ch = p - row * 10;
      if (p < 1920) *(bf16x8*)(smem + row * WO_LD + ch * 8) = st[i];
    }
    __syncthreads();
    load_a(1, 1);
    u16* dst = a.uf + ((size_t)((10 * cg + ((c >> 1) & 1)) * 16 + (h >> 1)) * 20 + 5 * wq) * 512 + (g * 16 + (h & 1) * 8 + 2 * (c >> 2) + (c & 1)) * 8;
    const u16* bp = smem + (8 * g + (c >> 2)) * WO_LD + 4 * (c & 3);
    bf16x8 bw[5][6];
#pragma unroll
    for (int t = 0; t < 5; ++t)
#pragma unroll
      for (int kp = 0; kp < 6; ++kp) bw[t][kp] = cat8(lds_tr(bp + 32 * kp * WO_LD + 16 * t), lds_tr(bp + (32 * kp + 4) * WO_LD + 16 * t));
    __builtin_amdgcn_sched_barrier(0);
#pragma unroll
    for (int pr = 0; pr < 5; ++pr) {
#pragma unroll
      for (int t = 0; t < 5; ++t) {
        f32x4 d0 = {0.f, 0.f, 0.f, 0.f}, d1 = {0.f, 0.f, 0.f, 0.f};
#pragma unroll
        for (int kp = 0; kp < 6; ++kp) {
          d0 = mfma32(af[pr & 1][0][kp], bw[t][kp], d0); d1 = mfma32(af[pr & 1][1][kp], bw[t][kp], d1);
        }
        *(u32x4*)(dst + ((size_t)t * 2 * 16 * 20 + pr) * 512) = (u32x4){pack2(d0[0], d0[1]), pack2(d0[2], d0[3]), pack2(d1[0], d1[1]), pack2(d1[2], d1[3])};
        __builtin_amdgcn_sched_barrier(0);
      }
      __builtin_amdgcn_sched_barrier(0);
      if (pr + 2 < 5) load_a(pr + 2, pr & 1);
      __builtin_amdgcn_sched_barrier(0);
    }
    return;
  }
  b -= a.nu;
  if (b < a.nln) { resln_rows(b, a.x, nullptr, 0, nullptr, a.xf, 1); return; }
  b -= a.nln;
  if (b < a.nsync) {
    const __amdgpu_buffer_rsrc_t rs = __builtin_amdgcn_make_buffer_rsrc(a.sync, 0, (int)(2 * SYNC_LAYER), 0x00020000);
#pragma unroll
    for (int i = 0; i < 4; ++i) {
      const unsigned off = (unsigned)b * 16384u + (unsigned)(i * 256 + tid) * 16u;
      const unsigned o = off & (SYNC_LAYER - 1), rr = o >> 15, in = o & 4095u;
      u32x4 z = {SENT, SENT, SENT, SENT};
      if (o >= OFS_CW) z = (u32x4){0u, 0u, 0u, 0u};
      else if (rr < 4 && (!(rr & 1) || in < 1024u)) {
        const int L = 5 * (rr >> 1) + 4 * (rr & 1) + (in >> 10), ln = (in & 1023u) >> 4;
        const unsigned* row = (const unsigned*)(a.mem0 + (ln & 7) * DM + 64 * L + 16 * (ln >> 4) + 2 * ((ln >> 3) & 1));
        z = (u32x4){min(row[0], SENT - 1), min(row[2], SENT - 1), min(row[4], SENT - 1), min(row[6], SENT - 1)};
      }
      store16(z, rs, (o >= OFS_CAL && o < OFS_CW) ? OOB : off, 0, 16);
    }
    return;
  }
  b -= a.nsync;
  if (b < a.ntr) { transpose_tile(a.jobs, b, (u16 (*)[66])smem); return; }
  pad_images(a.qb, b - a.ntr);
}

struct RecArgs {
  const u16* wf; const u16* uf; const u16* xf; char* qk; char* y; char* z; char* pt; char* sync; const char* cal;
  const u16* xold; u16* xnew; u16* xr; const u16* wb; u16* qkvb; unsigned long long* dbg;
  int layer, tail, d0, g2c, g2t, dur0, tol, slow;
};
template <typename F, int... I> __device__ __forceinline__ void static_for_(F&& f, std::integer_sequence<int, I...>) { (f(std::integral_constant<int, I>()), ...); }
template <int N, typename F> __device__ __forceinline__ void static_for(F&& f) { static_for_(f, std::make_integer_sequence<int, N>()); }
#define RLX_AGENT __ATOMIC_RELAXED, __HIP_MEMORY_SCOPE_AGENT
#define SPIN_LOG 20
constexpr unsigned SPIN_CAP = 1u << SPIN_LOG;
#define PIN_A(x) asm volatile("" : "+a"(x))
#define PIN_V(x) asm volatile("" : "+v"(x))
#define PIN_S(x) do { (x) = __builtin_amdgcn_readfirstlane(x); } while (0)
#define SB() __builtin_amdgcn_sched_barrier(0)
#define NOPS(n) do { SB(); asm volatile("s_nop %0" :: "n"(n)); SB(); } while (0)
#define WG_BARRIER() do { asm volatile("s_waitcnt lgkmcnt(0)" ::: "memory"); __builtin_amdgcn_s_barrier(); asm volatile("" ::: "memory"); } while (0)
#define RFL(x) ((unsigned)__builtin_amdgcn_readfirstlane(x))
template <int CTRL> __device__ __forceinline__ unsigned dppu(unsigned v) { return (unsigned)__builtin_amdgcn_update_dpp(0, (int)v, CTRL, 0xf, 0xf, true); }
template <int CTRL> __device__ __forceinline__ float dppf(float v) { return u2f(dppu<CTRL>(f2u(v))); }
template <int BANKS> __device__ __forceinline__ unsigned ror8_banks(unsigned v) { return (unsigned)__builtin_amdgcn_update_dpp((int)v, (int)v, 0x128, 0xf, BANKS, false); }
constexpr int ROR1 = 0x121, ROR2 = 0x122, ROR4 = 0x124, ROR8 = 0x128, SHR8 = 0x118;
__device__ __forceinline__ float fmx(float a, float b) { return __builtin_elementwise_maximum(a, b); }
__device__ __forceinline__ unsigned mx3(unsigned a, unsigned b, unsigned c) { return max(max(a, b), c); }
__device__ __forceinline__ unsigned mx4(u32x4 v) { return max(mx3(v[0], v[1], v[2]), v[3]); }
__device__ __forceinline__ u32x8 cat2(u32x4 lo, u32x4 hi) { return __builtin_shufflevector(lo, hi, 0, 1, 2, 3, 4, 5, 6, 7); }
__device__ __forceinline__ u32x4 lo4(u32x8 v) { return __builtin_shufflevector(v, v, 0, 1, 2, 3); }
__device__ __forceinline__ u32x4 hi4(u32x8 v) { return __builtin_shufflevector(v, v, 4, 5, 6, 7); }
__device__ __forceinline__ u32x2 lo2(u32x4 v) { return __builtin_shufflevector(v, v, 0, 1); }
__device__ __forceinline__ u32x2 hi2(u32x4 v) { return __builtin_shufflevector(v, v, 2, 3); }
__device__ __forceinline__ u32x4 pack8(f32x4 a, f32x4 b) { return (u32x4){pack2(a[0], a[1]), pack2(a[2], a[3]), pack2(b[0], b[1]), pack2(b[2], b[3])}; }
#define DEV __device__ __forceinline__
DEV void smf_v(f32x4& acc, u32x4 a, u32x8 b, int idx) { asm volatile("v_smfmac_f32_16x16x64_bf16 %0, %1, %2, %3" : "+v"(acc) : "v"(a), "v"(b), "v"(idx)); }
template <int R> DEV void smf_a(f32x4& acc, u32x4 a, int idx) { asm volatile("v_smfmac_f32_16x16x64_bf16 %0, %1, a[%c3:%c4], %2" : "+v"(acc) : "v"(a), "v"(idx), "n"(R), "n"(R + 7)); }
template <int R> DEV void mf32_ba(f32x4& acc, u32x4 a) { asm volatile("v_mfma_f32_16x16x32_bf16 %0, %1, a[%c2:%c3], %0" : "+v"(acc) : "v"(a), "n"(R), "n"(R + 3)); }
template <int R> DEV void mf32z_ba(f32x4& d, u32x4 a) { asm volatile("v_mfma_f32_16x16x32_bf16 %0, %1, a[%c2:%c3], 0" : "=&v"(d) : "v"(a), "n"(R), "n"(R + 3)); }
template <int R> DEV void mf32_aa(f32x4& acc, u32x4 b) { asm volatile("v_mfma_f32_16x16x32_bf16 %0, a[%c2:%c3], %1, %0" : "+v"(acc) : "v"(b), "n"(R), "n"(R + 3)); }
template <int R> DEV void mf32z_aa(f32x4& d, u32x4 b) { asm volatile("v_mfma_f32_16x16x32_bf16 %0, a[%c2:%c3], %1, 0" : "=&v"(d) : "v"(b), "n"(R), "n"(R + 3)); }
template <int R> DEV void ld_a(__amdgpu_buffer_rsrc_t r, unsigned vo, unsigned so) { asm volatile("buffer_load_dwordx4 a[%c3:%c4], %0, %1, %2 offen" :: "v"(vo), "s"(r), "s"(so), "n"(R), "n"(R + 3) : "memory"); }
template <int R> DEV void ld_a1(__amdgpu_buffer_rsrc_t r, unsigned vo, unsigned so) { asm volatile("buffer_load_dwordx4 a[%c3:%c4], %0, %1, %2 offen sc1" :: "v"(vo), "s"(r), "s"(so), "n"(R), "n"(R + 3) : "memory"); }
DEV void mf32(f32x4& acc, u32x4 a, u32x4 b) { asm volatile("v_mfma_f32_16x16x32_bf16 %0, %1, %2, %0" : "+v"(acc) : "v"(a), "v"(b)); }
DEV void mf32z(f32x4& d, u32x4 a, u32x4 b) { asm volatile("v_mfma_f32_16x16x32_bf16 %0, %1, %2, 0" : "=&v"(d) : "v"(a), "v"(b)); }
DEV void mf32_n(f32x4& acc, u32x4 a, u32x4 b) { asm volatile("s_nop 1\n\tv_mfma_f32_16x16x32_bf16 %0, %1, %2, %0" : "+v"(acc) : "v"(a), "v"(b)); }
DEV void mf32z_n(f32x4& d, u32x4 a, u32x4 b) { asm volatile("s_nop 1\n\tv_mfma_f32_16x16x32_bf16 %0, %1, %2, 0" : "=&v"(d) : "v"(a), "v"(b)); }
DEV void mf16(f32x4& acc, u32x2 a, u32x2 b) { asm volatile("v_mfma_f32_16x16x16_bf16 %0, %1, %2, %0" : "+v"(acc) : "v"(a), "v"(b)); }
DEV void mf16z(f32x4& d, u32x2 a, u32x2 b) { asm volatile("v_mfma_f32_16x16x16_bf16 %0, %1, %2, 0" : "=&v"(d) : "v"(a), "v"(b)); }
DEV void zero4(f32x4& d) { f32x2 x, y; asm volatile("v_mov_b64 %0, 0\n\tv_mov_b64 %1, 0" : "=v"(x), "=v"(y)); d = __builtin_shufflevector(x, y, 0, 1, 2, 3); asm volatile("" : "+v"(d)); }
DEV void clock_now(u64& t) { asm volatile("s_memtime %0" : "=s"(t)); }
DEV void zero2(f32x2& x) { asm volatile("v_mov_b64 %0, 0" : "=v"(x)); }
DEV int fresh_lane() { int l; asm volatile("v_mbcnt_lo_u32_b32 %0, -1, 0\n\tv_mbcnt_hi_u32_b32 %0, -1, %0" : "=v"(l)); __builtin_assume(l >= 0 && l < 64); return l; }
DEV void keep_v(unsigned x) { asm volatile("" :: "v"(x)); }
DEV void pin_s(unsigned& x) { asm volatile("" : "+s"(x)); }
template <typename T> DEV void pin_v(T& x) { asm volatile("" : "+v"(x)); }
#define AGPR_FENCE() asm volatile("" ::: \
    "a0", "a1", "a2", "a3", "a4", "a5", "a6", "a7", "a8", "a9", "a10", "a11", "a12", "a13", "a14", "a15", "a16", "a17", "a18", "a19", "a20", "a21", "a22", \
    "a23", "a24", "a25", "a26", "a27", "a28", "a29", "a30", "a31", "a32", "a33", "a34", "a35", "a36", "a37", "a38", "a39", "a40", "a41", "a42", "a43", \
    "a44", "a45", "a46", "a47", "a48", "a49", "a50", "a51", "a52", "a53", "a54", "a55", "a56", "a57", "a58", "a59", "a60", "a61", "a62", "a63", "a64", \
    "a65", "a66", "a67", "a68", "a69", "a70", "a71", "a72", "a73", "a74", "a75", "a76", "a77", "a78", "a79", "a80", "a81", "a82", "a83", "a84", "a85", \
    "a86", "a87", "a88", "a89", "a90", "a91", "a92", "a93", "a94", "a95", "a96", "a97", "a98", "a99", "a100", "a101", "a102", "a103", "a104", "a105", \
    "a106", "a107", "a108", "a109", "a110", "a111", "a112", "a113", "a114", "a115", "a116", "a117", "a118", "a119", "a120", "a121", "a122", "a123", \
    "a124", "a125", "a126", "a127", "a128", "a129", "a130", "a131", "a132", "a133", "a134", "a135", "a136", "a137", "a138", "a139", "a140", "a141", \
    "a142", "a143", "a144", "a145", "a146", "a147", "a148", "a149", "a150", "a151", "a152", "a153", "a154", "a155", "a156", "a157", "a158", "a159", \
    "a160", "a161", "a162", "a163", "a164", "a165", "a166", "a167", "a168", "a169", "a170", "a171", "a172", "a173", "a174", "a175", "a176", "a177", \
    "a178", "a179", "a180", "a181", "a182", "a183", "a184", "a185", "a186", "a187", "a188", "a189", "a190", "a191", "a192", "a193", "a194", "a195", \
    "a196", "a197", "a198", "a199", "a200", "a201", "a202", "a203", "a204", "a205", "a206", "a207", "a208", "a209", "a210", "a211", "a212", "a213", \
    "a214", "a215", "a216", "a217", "a218", "a219", "a220", "a221", "a222", "a223", "a224", "a225", "a226", "a227", "a228", "a229", "a230", "a231", \
    "a232", "a233", "a234", "a235", "a236", "a237", "a238", "a239", "a240", "a241", "a242", "a243", "a244", "a245", "a246", "a247", "a248", "a249", \
    "a250", "a251", "a252", "a253", "a254", "a255")
#define LDP16(r, off, so) __builtin_amdgcn_raw_buffer_load_b128((r), (off), (so), 0)
#define VM_WAIT(n) asm volatile("s_waitcnt vmcnt(%0)" :: "n"(n) : "memory")
#define LD16(r, off, so) __builtin_amdgcn_raw_buffer_load_b128((r), (off), (so), 16)
#define LD8(r, off, so) __builtin_amdgcn_raw_buffer_load_b64((r), (off), (so), 16)
#define LD4(r, off, so) __builtin_amdgcn_raw_buffer_load_b32((r), (off), (so), 16)
#define ST16(v, r, off, so) store16((v), (r), (off), (so), 16)
#define RSRC(p, n) __builtin_amdgcn_make_buffer_rsrc((void*)(p), 0, (int)(n), 0x00020000)
#define LDS16(off) (*(LAS u32x4*)(lds + (off)))
template <int IMM> DEV void dma1k(__amdgpu_buffer_rsrc_t r, unsigned vo, unsigned so, unsigned ldsoff) {
  asm volatile("s_mov_b32 m0, %0\n\ts_nop 0\n\tbuffer_load_dwordx4 %1, %2, %3 offen offset:%c4 lds" :: "s"(ldsoff), "v"(vo), "s"(r), "s"(so), "n"(IMM) : "memory");
}

DEV int s_clamp(int v, int lim) { int r; asm("s_min_i32 %0, %1, %2\n\ts_max_i32 %0, %0, %3" : "=&s"(r) : "s"(v), "n"(lim), "n"(-lim) : "scc"); return r; }
DEV unsigned s_mulshr16(unsigned x, unsigned y) { unsigned r; asm("s_mul_i32 %0, %1, %2\n\ts_lshr_b32 %0, %0, 16" : "=s"(r) : "s"(x), "s"(y) : "scc"); return r; }
struct Clock {
  unsigned me, re, dm, dt, rho, sig;
  float fm, ft, fx, fy;
  DEV unsigned edge() {
    u64 m, r0, r1, r2, r3, r4, r5, r6, r7; unsigned n;
#define CNT_(k) "s_cmp_eq_u64 %" #k ", %1\n\ts_addc_u32 %9, %9, 0\n\t"
    asm volatile("s_memtime %0\n\ts_memrealtime %1\n\ts_nop 2\n\ts_memrealtime %2\n\ts_nop 2\n\ts_memrealtime %3\n\ts_nop 2\n\ts_memrealtime %4\n\ts_nop 2\n\t"
                 "s_memrealtime %5\n\ts_nop 2\n\ts_memrealtime %6\n\ts_nop 2\n\ts_memrealtime %7\n\ts_nop 2\n\ts_memrealtime %8\n\t"
                 "s_mov_b32 %9, 0\n\ts_waitcnt lgkmcnt(0)\n\t" CNT_(2) CNT_(3) CNT_(4) CNT_(5) CNT_(6) CNT_(7) CNT_(8)
                 : "=&s"(m), "=&s"(r0), "=&s"(r1), "=&s"(r2), "=&s"(r3), "=&s"(r4), "=&s"(r5), "=&s"(r6), "=&s"(r7), "=&s"(n) :: "scc", "memory");
    take(m, r0, n);
    return (unsigned)m;
  }
  DEV void take(u64 m, u64 r0, unsigned n) {
    const unsigned m1 = (unsigned)m + 16u * n + 12u, t1 = ((unsigned)r0 + 4u) * 8u;
    dm = m1 - me; dt = t1 - re; me = m1; re = t1;
  }
  DEV void edge_z(f32x4 (&z)[4], u32x4 x0, u32x4 x1, u32x8 u0, u32x8 u1, int idx) {
    u64 m, r0, r1, r2, r3, r4, r5, r6, r7; unsigned n;
#define EZ_(acc, x, b) "v_smfmac_f32_16x16x64_bf16 %" #acc ", %" #x ", " b ", %18\n\t"
    asm volatile(EZ_(10, 14, "a[64:71]") "s_memtime %0\n\ts_memrealtime %1\n\t" EZ_(11, 14, "a[144:151]") "s_nop 0\n\ts_memrealtime %2\n\t" EZ_(12, 14, "a[224:231]") "s_nop 0\n\ts_memrealtime %3\n\t"
                 EZ_(13, 14, "%16") "s_nop 0\n\ts_memrealtime %4\n\t" EZ_(10, 15, "a[72:79]") "s_nop 0\n\ts_memrealtime %5\n\t" EZ_(11, 15, "a[152:159]") "s_nop 0\n\ts_memrealtime %6\n\t"
                 EZ_(12, 15, "a[232:239]") "s_nop 0\n\ts_memrealtime %7\n\t" EZ_(13, 15, "%17") "s_nop 0\n\ts_memrealtime %8\n\t"
                 "s_mov_b32 %9, 0\n\ts_waitcnt lgkmcnt(0)\n\t" CNT_(2) CNT_(3) CNT_(4) CNT_(5) CNT_(6) CNT_(7) CNT_(8)
                 : "=&s"(m), "=&s"(r0), "=&s"(r1), "=&s"(r2), "=&s"(r3), "=&s"(r4), "=&s"(r5), "=&s"(r6), "=&s"(r7), "=&s"(n), "+v"(z[0]), "+v"(z[1]), "+v"(z[2]), "+v"(z[3])
                 : "v"(x0), "v"(x1), "v"(u0), "v"(u1), "v"(idx) : "scc", "memory");
    take(m, r0, n);
  }
  template <int K> DEV void rate_step() {
    if (K == 0) { fm = (float)dm; pin_v(fm); }
    if (K == 1) { ft = (float)dt; pin_v(ft); }
    if (K == 2) { fy = __builtin_amdgcn_rcpf(fm); pin_v(fy); }
    if (K == 3) { fx = __builtin_amdgcn_rcpf(ft); pin_v(fx); }
    if (K == 4) { fm *= 2048.0f; pin_v(fm); }
    if (K == 5) { ft *= 65536.0f; pin_v(ft); }
    if (K == 6) { fm *= fx; pin_v(fm); }
    if (K == 7) { ft *= fy; pin_v(ft); }
    if (K == 8) { fm = u2f((unsigned)fm); pin_v(fm); }
    if (K == 9) { ft = u2f((unsigned)ft); pin_v(ft); }
    if (K == 10) { dm = RFL(f2u(fm)); asm volatile("" : "+s"(dm)); }
    if (K == 11) { dt = RFL(f2u(ft)); asm volatile("" : "+s"(dt)); }
    if (K == 12) { const bool ok = dm - 12u * 256u < 14u * 256u; rho = ok ? dm : rho; sig = ok ? dt : sig; asm volatile("" : "+s"(rho), "+s"(sig)); }
  }
  template <int K> DEV void rho_step() {
    if (K < 2) rate_step<K>(); else if (K < 4) rate_step<K + 1>(); else if (K < 7) rate_step<2 * K - 2>();
    else { rho = dm - 12u * 256u < 14u * 256u ? dm : rho; asm volatile("" : "+s"(rho)); }
  }
  DEV void rate() { static_for<13>([&](auto K) { rate_step<decltype(K)::value>(); }); }
  DEV void init() { rho = 22u * 256u; sig = 65536u * 8u / 22u; me = 0u; re = 0u; edge(); }
  DEV unsigned local(unsigned t) const { unsigned r = me + (unsigned)((s_clamp((int)(t - re), 100000) * (int)rho) >> 11); PIN_S(r); return r; }
  DEV unsigned global(unsigned m) const { int p = s_clamp((int)(m - me), 40000) * (int)sig; PIN_S(p); p >>= 16; PIN_S(p); return re + (unsigned)p; }
};
__device__ __forceinline__ void wait_until(unsigned tgt) {
  unsigned t;
  asm volatile("1:\n\ts_memtime vcc\n\ts_waitcnt lgkmcnt(0)\n\ts_sub_i32 %0, %1, vcc_lo\n\ts_sub_u32 vcc_hi, %0, 209\n\ts_cmp_lt_u32 vcc_hi, 60000\n\ts_cbranch_scc1 1b\n\t"
               "s_sub_i32 %0, %0, 112\n\ts_max_i32 %0, %0, 0\n\ts_min_i32 %0, %0, 96\n\ts_and_b32 %0, %0, 0x7c\n\t"
               "s_getpc_b64 vcc\n2:\n\ts_sub_u32 %0, 3f-2b, %0\n\ts_add_u32 vcc_lo, vcc_lo, %0\n\ts_addc_u32 vcc_hi, vcc_hi, 0\n\ts_setpc_b64 vcc\n\t"
               ".rept 24\n\ts_nop 0\n\t.endr\n3:" : "=&s"(t) : "s"(tgt) : "vcc", "scc", "memory");
}
__device__ __forceinline__ void coarse_until(unsigned tgt) {
  for (;;) { const unsigned rem = tgt - mtc(); if (rem - 1u >= 60000u) break; }
}
#define DUR_UP 6
#define DUR_DN 2
DEV int max32(int d) {
  d = max(d, (int)dppu<ROR8>(d)); d = max(d, (int)dppu<ROR4>(d)); d = max(d, (int)dppu<ROR2>(d)); d = max(d, (int)dppu<ROR1>(d));
  return max(__builtin_amdgcn_readlane(d, 0), __builtin_amdgcn_readlane(d, 16));
}
struct Sched {
  unsigned T, tP, dur, gap, more;
  DEV void init(unsigned t, const RecArgs& a) { T = t; tP = t; dur = a.dur0; gap = 0u; more = 0u; }
  DEV unsigned on_time() const { return tP + gap; }
  DEV unsigned limit(const RecArgs& a) const { return T + dur + a.tol; }
  DEV void next(u32x2 an, const RecArgs& a, bool start) {
    const int dm = max32((int)(an[0] - T)), gm = max32((int)an[1]), over = dm - (int)dur;
    const bool late = start || over > a.tol;
    more = late ? more + 150u : 0u;
    T = late ? T + (unsigned)dm + a.slow + more : tP + gap;
    int dd = late ? (start ? 0 : DUR_UP) : max(over, over >> DUR_DN);
    PIN_S(dd);
    dur += (unsigned)dd; gap = (unsigned)gm;
    tP = T + dur + a.d0;
    PIN_S(T); PIN_S(tP); PIN_S(dur); PIN_S(more); PIN_S(gap);
  }
};
template <typename R>
__device__ __forceinline__ u32x2 ann_valid(u32x2 an, R rs, unsigned vo, unsigned so, bool& dead) {
  if (__builtin_expect(__any(max(an[0], an[1]) == SENT), 0)) {
    for (unsigned spins = 0; !dead;) {
      __builtin_amdgcn_s_sleep(3);
      const bool need = max(an[0], an[1]) == SENT;
      const u32x2 n = LD8(rs, need ? vo : OOB, so);
      if (need) an = n;
      if (!__any(max(an[0], an[1]) == SENT)) break;
      if (++spins > SPIN_CAP) dead = true;
      asm volatile("" ::: "memory");
    }
  }
  return an;
}
template <typename R>
__device__ __forceinline__ void need_progress(R rs, unsigned vo, unsigned want, unsigned& have) {
  for (unsigned spins = 0; have < want && spins < SPIN_CAP; ++spins) {
    unsigned v = LD4(rs, vo, 0);
    v = min(v, dppu<0x4E>(v)); v = min(v, dppu<0xB1>(v));
    have = RFL(v);
    if (have < want) __builtin_amdgcn_s_sleep(8);
    asm volatile("" ::: "memory");
  }
}
struct Softmax {
  f32x4 s; float m, x;
  DEV void a() {
    float t;
    x = fmx(fmx(s[0], s[1]), s[2]); pin_v(x); x = fmx(fmx(x, s[3]), m);
    asm volatile("s_nop 1\n\tv_max_f32_dpp %0, %0, %0 row_ror:8 row_mask:0xf bank_mask:0xf\n\ts_nop 1\n\tv_max_f32_dpp %0, %0, %0 row_ror:4 row_mask:0xf bank_mask:0xf\n\t"
                 "v_mov_b32 %1, %0\n\ts_nop 1\n\tv_permlane32_swap_b32 %0, %1\n\tv_max_f32 %0, %0, %1\n\tv_mul_f32 %0, 0xbdd53b94, %0" : "+v"(x), "=&v"(t));
  }
  DEV void b() {
    const float C = 0.07216878364870322f * 1.4426950408889634f;
    s = __builtin_elementwise_fma(s, (f32x4){C, C, C, C}, (f32x4){x, x, x, x}); m = __builtin_fmaf(m, C, x);
#pragma unroll
    for (int i = 0; i < 4; ++i) s[i] = __builtin_amdgcn_exp2f(s[i]);
    m = __builtin_amdgcn_exp2f(m);
    pin_v(s); pin_v(m);
  }
  DEV void c() {
    float t;
    const f32x2 u = (f32x2){s[0], s[1]} + (f32x2){s[2], s[3]};
    x = (u[0] + m) + u[1];
    asm volatile("s_nop 1\n\tv_add_f32_dpp %0, %0, %0 row_ror:8 row_mask:0xf bank_mask:0xf\n\ts_nop 1\n\tv_add_f32_dpp %0, %0, %0 row_ror:4 row_mask:0xf bank_mask:0xf\n\t"
                 "v_mov_b32 %1, %0\n\ts_nop 1\n\tv_permlane32_swap_b32 %0, %1\n\tv_add_f32 %0, %0, %1\n\tv_rcp_f32 %0, %0" : "+v"(x), "=&v"(t));
  }
  DEV u32x4 d() {
    unsigned o0, o1, o2, o3; float t, u;
    asm volatile("s_nop 0\n\tv_mul_f32 %4, %10, %11\n\tv_mul_f32 %5, %10, %11\n\tv_mul_f32 %0, %6, %11\n\tv_mul_f32 %2, %7, %11\n\tv_permlane32_swap_b32 %4, %5\n\t"
                 "v_mul_f32 %1, %8, %11\n\tv_mul_f32 %3, %9, %11\n\tv_cvt_pk_bf16_f32 %0, %0, |%2|\n\tv_cvt_pk_bf16_f32 %1, %1, |%3|\n\tv_cvt_pk_bf16_f32 %4, %4, |%5|\n\t"
                 "v_mov_b32_dpp %2, %0 row_ror:8 row_mask:0xf bank_mask:0xf\n\tv_mov_b32_dpp %3, %1 row_ror:8 row_mask:0xf bank_mask:0xf\n\t"
                 "v_mov_b32_dpp %0, %4 row_shr:8 row_mask:0xf bank_mask:0x4\n\tv_mov_b32_dpp %1, %4 row_shr:4 row_mask:0xf bank_mask:0x4\n\t"
                 "v_mov_b32_dpp %2, %4 quad_perm:[0,1,2,3] row_mask:0xf bank_mask:0x4\n\tv_mov_b32_dpp %3, %4 row_shl:4 row_mask:0xf bank_mask:0x4"
                 : "=&v"(o0), "=&v"(o1), "=&v"(o2), "=&v"(o3), "=&v"(t), "=&v"(u) : "v"(s[0]), "v"(s[1]), "v"(s[2]), "v"(s[3]), "v"(m), "v"(x));
    return (u32x4){o0, o1, o2, o3};
  }
};
#define TMARK(k)
#define STAMP(k)
#define STAMP_DECL(sh)
#define STAMP_FLUSH(s, ref, x0, x1)
#define STAMP_PUT(s)
#define STAMP_DUMP()
#define DBG_LDS 0

#define AN_AT 980u
#define LATE_PAD 480u
#define HEAD 1u
#define S_FINE 0
#define PFQK 0
#define EX_UP 16u
#define SM_W 2
constexpr int S_EX = 0, S_ET = S_EX + 4 * 2048, S_CT = S_ET + 4 * 5120, S_LW = S_CT + 48, S_NL = 13, S_END = S_LW + 4 * S_NL * 2048;
__host__ __device__ constexpr int s_q(int n) { return n < 30 ? n / 6 : n < 50 ? 5 + (n - 30) / 4 : 5 + (n - 50) / 2; }
__host__ __device__ constexpr int s_t(int n) { return n < 30 ? n % 6 : n < 50 ? (n - 30) % 4 : 4 + (n & 1); }
__host__ __device__ constexpr bool s_lds(int n) { return n % 4 == 3 && n / 4 < S_NL; }
__host__ __device__ constexpr int s_ri(int n) { return n - ((n + 1) / 4 < S_NL ? (n + 1) / 4 : S_NL); }
template <int kind>
__device__ __forceinline__ void score_role(const RecArgs& a, LAS char* lds, int rep, int il, unsigned posM, unsigned posP) {
  const int lane = fresh_lane(), w = __builtin_amdgcn_readfirstlane(threadIdx.x >> 6), c = lane & 15, g = lane >> 4, hf = c >> 3, i8 = c & 7;
  const int h = rep * 4 + il, iod = rep >> 2;
  const __amdgpu_buffer_rsrc_t rs = RSRC(a.sync, SYNC_LAYER), rq = RSRC(a.qk, 2 * QK_IMG), rp = RSRC(a.pt, PT_IMG);
  Clock ck; ck.init();
  u32x8 WV[28 - S_NL];
  {
    const __amdgpu_buffer_rsrc_t rw = RSRC(a.wf, 768 * 20480);
    const unsigned vo = lane * 16, wb = (unsigned)(h * 12 + 3 * w) * 20480u;
    static_for<60>([&](auto N) {
      constexpr int n = decltype(N)::value, q = s_q(n), t = s_t(n), ri = s_ri(n);
      const unsigned so = wb + (unsigned)((t & 1) * 384 + (t >> 1)) * 20480u + (unsigned)(q < 5 ? q + 5 * iod : q - 5 * iod) * 2048u;
      if (s_lds(n)) { LDS16(S_LW + (w * S_NL + n / 4) * 2048 + lane * 16) = LDP16(rw, vo, so); LDS16(S_LW + (w * S_NL + n / 4) * 2048 + 1024 + lane * 16) = LDP16(rw, vo + 1024, so); }
      else if (ri >= 32) WV[ri < 32 ? 0 : ri - 32] = cat2(LDP16(rw, vo, so), LDP16(rw, vo + 1024, so));
    });
    SB();
    static_for<60>([&](auto N) {
      constexpr int n = decltype(N)::value, q = s_q(n), t = s_t(n), ri = s_ri(n);
      const unsigned so = wb + (unsigned)((t & 1) * 384 + (t >> 1)) * 20480u + (unsigned)(q < 5 ? q + 5 * iod : q - 5 * iod) * 2048u;
      if (!s_lds(n) && ri < 32) { ld_a<8 * (ri & 31)>(rw, vo, so); ld_a<8 * (ri & 31) + 4>(rw, vo, so + 1024u); VM_WAIT(40); }
    });
    VM_WAIT(0);
#pragma unroll
    for (int i = 0; i < 28 - S_NL; ++i) PIN_V(WV[i]);
  }
  const int sidx = hf ? 0xEEEE : 0x4444;
  u32x4 idn;
#pragma unroll
  for (int jj = 0; jj < 4; ++jj) idn[jj] = (((c >> 2) & 1) == (g & 1) && jj == ((c & 3) >> 1) + 2 * hf) ? ((c & 1) ? 0x3f800000u : 0x3f80u) : 0u;
  const unsigned lomask = hf ? 0u : ~0u;
  unsigned l16 = lane * 16u;
  const unsigned an_ld = lane < 32 && lane != h ? ((lane >> 4) * 2 + 1) * ROW + posM * 4096u + 1024u + c * 128u : OOB;
  const unsigned an_st = ((h >> 4) * 2 + 1) * ROW + (lane >> 3) * 4096u + 1024u + (h & 15) * 128u + i8 * 16u;
  const int sr = (lane & 3) | ((lane >> 2) & 4), sg = ((lane >> 2) & 1) | ((lane >> 4) & 2), sk = ((lane >> 1) & 6) | (lane >> 5), cq = 8 * (w & 1) + sr;
  const bool smem_lane = (lane & 44) == 8;
  const unsigned p_st = !hf ? OFS_P + (h >> 3) * ROW + (8 / SM_W) * w * 4096u + ((h >> 1) & 3) * 1024u + sg * 256u + (h & 1) * 128u + sr * 16u
                            : smem_lane ? OFS_P + 4 * ROW + (8 / SM_W) * w * 4096u + h * 128u + sr * 16u : OOB;
  const unsigned pt_st = !hf ? (unsigned)(h * 2048 + (w >> 1) * 1024 + (sg * 16 + cq) * 16) : smem_lane ? PT_M + ((w >> 1) * NH + h) * 256u + cq * 16u : OOB;
  const unsigned lw = S_LW + w * S_NL * 2048 + lane * 16;
  unsigned ex_w = S_EX + w * 2048 + lane * 16; PIN_V(ex_w);
  const unsigned et_w = S_ET + w * 5120 + lane * 16, et_r = S_ET + ((w >> 1) ? 1 + hf : 0) * 1024 + (sg * 16 + cq) * 16,
                 et_m = S_ET + (3 + (w >> 1)) * 1024 + ((2 + (sk >> 2)) * 16 + cq) * 16 + (sk & 3) * 4;
  const unsigned ex_r = S_EX + (sg * 16 + hf * 8 + sr) * 16, ex_m = S_EX + 1024 + ((2 + (sk >> 2)) * 16 + sr) * 16 + (sk & 3) * 4;
  const unsigned jp_vo = lane < 4 ? OFS_CW + CW_JP + (unsigned)((((lane & 1) * 384 + h * 12 + ((lane >> 1) & 1) * 11) / 20) * 128) : OOB;
  const unsigned pf_vo = lane < 48 ? (lane >= 24 ? QK_IMG + (lane - 24) * 128u : lane * 128u) : OOB;
  unsigned jhave = 0;
  u32x4 kf[3], qf[3];
  auto load_qk = [&](unsigned so) {
#pragma unroll
    for (int p = 0; p < 3; ++p) { kf[p] = LD16(rq, l16 + p * 1024, so + QK_IMG); qf[p] = LD16(rq, l16 + p * 1024, so); }
  };
  TMARK(1);
  need_progress(rs, jp_vo, HEAD, jhave);
  TMARK(2);
  unsigned qk_so = (unsigned)(h * 12 + 3 * w) * 1024u;
  load_qk(qk_so);
  bool dead = false;
  unsigned extra = 0;
  Softmax tk; tk.s = (f32x4){0.f, 0.f, 0.f, 0.f}; tk.m = 0.f;
  ck.edge(); ck.rate();
  Sched sd; sd.init(ck.global(mtc()), a);
  if (kind == 2) LDS16(S_CT) = (u32x4){0u, 0u, 1u, 0u};
  unsigned mT = mtc();
  u32x2 an = {0u, 0u}; bool owed = false;
  const unsigned kA = posM * 4096u + iod * (2 * ROW), kB = posM * 4096u + (1 - iod) * (2 * ROW);
  unsigned sl = 0u, soA = kA, soB = kB, soA1 = kA + ROW, soB1 = kB + ROW, sp = 0u, jp_so = 0u, pt_so = FAR, pf_so = FAR;
  f32x4 acc[6];
  u32x4 tl[2], th[2];
  auto prime = [&]() {
    tl[0] = LDS16(lw); th[0] = LDS16(lw + 1024); tl[1] = LDS16(lw + 2048); th[1] = LDS16(lw + 3072);
#pragma unroll
    for (int t = 0; t < 6; ++t) zero4(acc[t]);
    SB();
  };
  prime();
  auto token_parts = [&](f32x4 (&e)[4], float (&em)[4]) {
#pragma unroll
    for (int k = 0; k < 4; ++k) e[k] = __builtin_bit_cast(f32x4, LDS16(et_r + k * 5120));
#pragma unroll
    for (int k = 0; k < 4; ++k) em[k] = *(LAS float*)(lds + et_m + k * 5120);
  };
  auto token_sums = [&](f32x4 (&e)[4], float (&em)[4]) {
    f32x4 es = (e[0] + e[1]) + (e[2] + e[3]);
    PIN_V(es);
#pragma unroll
    for (int r = 0; r < 4; ++r) tk.s[r] = 16 * hf + 4 * sg + r <= 8 * w + sr ? es[r] : -INFINITY;
    const f32x2 u = (f32x2){em[0], em[1]} + (f32x2){em[2], em[3]};
    tk.m = u[0] + u[1];
    pin_v(tk.s); pin_v(tk.m);
  };
  unsigned budget = SPIN_CAP;
  STAMP_DECL(0);
#pragma unroll 1
  for (int s = 0; s < NSEG; ++s) {
    PIN_V(l16);
    f32x4 dx, dm, tr, ts;
    u32x4 cm[10], ta, tc, bq; u32x2 X[3];
#define LDM(q) LD16(rs, l16 + ((q) % 5 < 4 ? ((q) % 5) * 1024 : 0), (q) < 4 ? soA : (q) == 4 ? soA1 : (q) < 9 ? soB : soB1)
#define POS(n) do { constexpr int q_ = s_q(n), t_ = s_t(n), ri_ = s_ri(n); \
      if (s_lds(n)) smf_v(acc[t_], cm[q_], cat2(tl[((n) / 4) & 1], th[((n) / 4) & 1]), sidx); \
      else if (ri_ < 32) smf_a<8 * (ri_ & 31)>(acc[t_], cm[q_], sidx); else smf_v(acc[t_], cm[q_], WV[ri_ < 32 ? 0 : ri_ - 32], sidx); \
      SB(); \
      if ((n) % 4 == 3 && (n) / 4 + 2 < S_NL) tl[((n) / 4) & 1] = LDS16(lw + ((n) / 4 + 2) * 2048); \
      if ((n) % 4 == 0 && (n) > 0 && (n) / 4 + 1 < S_NL) th[((n) / 4 + 1) & 1] = LDS16(lw + ((n) / 4 + 1) * 2048 + 1024); \
      SB(); } while (0)
#define CVT(dst, src, k) do { unsigned v_ = pack2((src)[2 * (k)], (src)[2 * (k) + 1]); PIN_V(v_); dst = v_; SB(); } while (0)
#define B_LO(p, k) do { unsigned v_ = X[p][k] & lomask; PIN_V(v_); bq[k] = v_; SB(); } while (0)
#define B_HI(p, k) do { unsigned v_ = dppu<SHR8>(X[p][k]); PIN_V(v_); bq[2 + (k)] = v_; SB(); } while (0)
    unsigned jpend; u64 tb1 = 0;
    SB();
    wait_until(mT);
    SB();
    STAMP(0);
    cm[0] = LDM(0); SB(); tk.a(); SB();
    cm[1] = LDM(1); SB(); tk.b(); SB();
    cm[2] = LDM(2); SB(); tk.c(); SB();
    cm[3] = LDM(3); SB();
    u32x4 ptk = tk.d(); PIN_V(ptk); SB();
    cm[4] = LDM(4); SB();
    unsigned chk = 0;
    static_for<48>([&](auto N) {
      constexpr int n = decltype(N)::value, q = s_q(n);
      POS(n);
      if (n == 0 && !S_FINE) STAMP(1);
      if constexpr (n % 4 == 2 && 5 + n / 4 < 10) cm[5 + n / 4] = LDM(5 + n / 4);
      SB();
      if (n == 1 || n == 9 || n == 13 || n == 21 || n == 25 || n == 32 || n == 36 || n == 40 || n == 44 || n == 47) { chk = mx3(chk, cm[q][0], cm[q][2]); pin_v(chk); }
      SB();
    });
    POS(48);
    u64 bad = __builtin_amdgcn_ballot_w64(chk == SENT); asm volatile("" : "+s"(bad)); SB();
    POS(49);
    if (__builtin_expect(bad != 0, 0)) {
      *(LAS unsigned*)(lds + S_CT + 8) = (unsigned)s + 1u;
      u64 miss;
      do {
        __builtin_amdgcn_s_sleep(1);
        unsigned m = 0;
#pragma unroll
        for (int q = 5; q < 10; ++q) cm[q] = LDM(q);
#pragma unroll
        for (int q = 5; q < 10; ++q) m = max(m, mx4(cm[q]));
        SB();
#pragma unroll
        for (int q = 0; q < 5; ++q) cm[q] = LDM(q);
#pragma unroll
        for (int q = 0; q < 5; ++q) m = max(m, mx4(cm[q]));
        miss = __builtin_amdgcn_ballot_w64(m == SENT);
        asm volatile("s_sub_u32 %1, %1, 1\n\ts_cselect_b32 %1, 0, %1\n\ts_cselect_b64 %0, 0, %0" : "+s"(miss), "+s"(budget) :: "scc", "memory");
      } while (miss != 0);
      prime();
      NOPS(1);
      static_for<50>([&](auto N) {
        constexpr int n = decltype(N)::value;
        POS(n);
        if constexpr (n % 6 == 5 && n < 30) cm[5 + n / 6] = LDM(5 + n / 6);
        SB();
      });
    }
    if (!S_FINE) STAMP(2);
    CVT(ta[0], acc[0], 0);
    POS(50); asm volatile("" : "+v"(tl[0]), "+v"(th[0])); SB(); CVT(ta[1], acc[0], 1);
    POS(51); __builtin_amdgcn_raw_buffer_store_b128(ptk, rp, pt_st, pt_so, 16); SB(); CVT(ta[2], acc[1], 0);
    POS(52); jpend = LD4(rs, jp_vo, jp_so); SB(); CVT(ta[3], acc[1], 1);
    asm volatile("" :: "v"(ptk));
    POS(53); CVT(tc[0], acc[2], 0);
    mf32z(tr, ta, idn); SB(); CVT(tc[1], acc[2], 1);
    POS(54);
    unsigned junk = 0u;
    if (PFQK) { junk = LD4(rq, pf_vo, pf_so); SB(); }
    CVT(tc[2], acc[3], 0);
    POS(55); CVT(tc[3], acc[3], 1);
    POS(56); CVT(X[0][0], tr, 0);
    mf32z(ts, tc, idn); SB(); CVT(X[0][1], tr, 1);
    POS(57); B_LO(0, 0);
    POS(58); B_LO(0, 1);
    POS(59); B_HI(0, 0);
    mf16z(dm, X[0], X[0]); SB(); B_HI(0, 1); CVT(X[1][0], ts, 0); CVT(X[1][1], ts, 1);
    CVT(ta[0], acc[4], 0); CVT(ta[1], acc[4], 1);
    mf32z(dx, kf[0], bq); SB(); CVT(ta[2], acc[5], 0); CVT(ta[3], acc[5], 1);
    B_LO(1, 0); B_LO(1, 1);
    mf32z(tr, ta, idn); SB(); B_HI(1, 0); B_HI(1, 1);
    mf16(dm, X[1], X[1]); SB();
    mf32(dx, kf[1], bq); SB();
    CVT(X[2][0], tr, 0); CVT(X[2][1], tr, 1); B_LO(2, 0); B_LO(2, 1);
    mf16(dm, X[2], X[2]); SB(); B_HI(2, 0); B_HI(2, 1);
    mf32_n(dx, kf[2], bq); SB();
    if (!S_FINE) STAMP(3);
    NOPS(2);
    LDS16(ex_w + 1024) = __builtin_bit_cast(u32x4, dm);
    NOPS(3);
    LDS16(ex_w) = __builtin_bit_cast(u32x4, dx);
    if constexpr (kind == 0) {
      asm volatile("s_waitcnt lgkmcnt(0)\n\ts_barrier ; softmax waves" ::: "memory");
      if (!S_FINE) STAMP(4);
      f32x4 e[4]; float em[4];
#pragma unroll
      for (int k = 0; k < 4; ++k) e[k] = __builtin_bit_cast(f32x4, LDS16(ex_r + k * 2048));
#pragma unroll
      for (int k = 0; k < 4; ++k) em[k] = *(LAS float*)(lds + ex_m + k * 2048);
      asm volatile("" :: "s"(sp + 4096u), "s"(sp + 8192u), "s"(sp + 12288u));
      SB();
      Softmax sm;
      sm.s = (e[0] + e[1]) + (e[2] + e[3]);
      const f32x2 u = (f32x2){em[0], em[1]} + (f32x2){em[2], em[3]};
      sm.m = u[0] + u[1];
      sm.a(); sm.b(); sm.c();
      const u32x4 v = sm.d();
#pragma unroll
      for (int k = 0; k < 8 / SM_W - 1; ++k) __builtin_amdgcn_raw_buffer_store_b128(v, rs, p_st, sp + k * 4096u, 16);
      ST16(v, rs, p_st, sp + (8 / SM_W - 1) * 4096u);
    } else if constexpr (kind == 1) {
      unsigned fl;
      const unsigned a_gp = a.g2t + s_mulshr16(a.g2c, ck.sig) + extra;
      asm volatile("s_waitcnt lgkmcnt(0)\n\ts_barrier ; clock\n\tds_read_b32 %0, %1" : "=&v"(fl) : "v"((unsigned)(uintptr_t)lds + S_CT + 8u), "s"(a_gp) : "memory");
      const unsigned m0 = ck.edge();
      PIN_V(fl);
      const bool failed = RFL(fl) == (unsigned)s + 1u;
      bq[0] = (ck.re + (unsigned)(((int)(m0 + a.tail - ck.me) * (int)ck.sig) >> 16) + (failed ? LATE_PAD : 0u)) & ~1u;
      bq[1] = a_gp;
      ST16(bq, rs, an_st, sl);
      *(LAS u32x2*)(lds + S_CT + 32) = lo2(bq);
      extra += failed && s > 0 && extra < 240u ? EX_UP + 1u : 0u; PIN_S(extra);
    } else {
      asm volatile("s_waitcnt lgkmcnt(0)\n\ts_barrier\n\ts_memtime %0" : "=s"(tb1) :: "memory");
      __builtin_amdgcn_raw_ptr_buffer_atomic_add_i32(1, rs, l16 << 20, s > 1 ? OFS_CW + CW_SS + (unsigned)(s - 2) * 128u : FAR, 0);
      asm volatile("s_waitcnt lgkmcnt(0)" : "+s"(tb1));
      if (owed) sd.next(an, a, false);
    }
    SB();
    STAMP(5);
    const unsigned sl0 = sl, mT0 = mT;
    {
      f32x4 d[5]; f32x2 zz[12];
      static_for<15>([&](auto N) {
        constexpr int n = decltype(N)::value, t = n / 3, p = n % 3;
        const u32x2 fa = t < 2 ? lo2(kf[p]) : t == 2 ? hi2(kf[p]) : X[p], fb = (t == 0 || t == 3) ? lo2(qf[p]) : hi2(qf[p]);
        if (p == 0) mf16z(d[t], fa, fb); else mf16(d[t], fa, fb);
        SB();
        if (n < 4) { const u32x4 v = LDS16(lw + n * 1024); if (n & 1) th[n >> 1] = v; else tl[n >> 1] = v; }
        if (n >= 5 && p == 2) LDS16(et_w + (t - 1) * 1024) = __builtin_bit_cast(u32x4, d[t > 0 ? t - 1 : 0]);
        if (n == 4) { sl = (sl + M_SLOT) & (4 * M_SLOT - 1); pin_s(sl); }
        if (n == 6) { soA = sl + kA; soB = sl + kB; pin_s(soA); pin_s(soB); }
        if (n == 7) { soA1 = soA + ROW; soB1 = soB + ROW; pin_s(soA1); pin_s(soB1); }
        if (n == 9) { sp = P_SLOT - sp; pt_so = (unsigned)s * PT_SEG; pin_s(sp); pin_s(pt_so); }
        if (n == 10) { qk_so += s + 1 < NSEG ? NH * 12 * 1024u : 0u; pin_s(qk_so); }
        SB();
        if (n < 12) zero2(zz[n < 12 ? n : 0]);
        if (n == 12) keep_v(junk);
        if (n == 12) { jpend = min(jpend, dppu<0x4E>(jpend)); pin_v(jpend); }
        if (n == 13) { jpend = min(jpend, dppu<0xB1>(jpend)); pin_v(jpend); }
        SB();
      });
#pragma unroll
      for (int t = 0; t < 6; ++t) { acc[t] = __builtin_shufflevector(zz[2 * t], zz[2 * t + 1], 0, 1, 2, 3); pin_v(acc[t]); }
      jhave = max(jhave, RFL(jpend));
      jp_so = jhave < min((unsigned)NSEG, (unsigned)s + 6u) ? 0u : FAR; pin_s(jp_so);
      pf_so = jhave > (unsigned)s + 2u ? qk_so + NH * 12 * 1024u : FAR; pin_s(pf_so);
      NOPS(1);
      LDS16(et_w + 4096) = __builtin_bit_cast(u32x4, d[4]);
      SB();
    }
    if (S_FINE) STAMP(1);
    if constexpr (kind == 2) {
      coarse_until((unsigned)tb1 + AN_AT);
      an = LD8(rs, an_ld, sl0);
      SB();
    }
    if constexpr (kind == 1) {
      ck.rate(); extra -= extra != 0u; PIN_S(extra);
      LDS16(S_CT + 16) = (u32x4){ck.me, ck.re, ck.rho, 0u};
    }
    if (__builtin_expect(jhave < min((unsigned)s + 2u, (unsigned)NSEG), 0)) need_progress(rs, jp_vo, min((unsigned)s + 2u, (unsigned)NSEG), jhave);
    STAMP(6);
    WG_BARRIER();
    if (S_FINE) STAMP(2);
    {
      f32x4 e[4]; float em[4]; u32x4 cc; u32x2 own;
      if constexpr (kind == 2) { cc = LDS16(S_CT + 16); own = *(LAS u32x2*)(lds + S_CT + 32); }
      token_parts(e, em);
      SB();
      PIN_V(l16);
      if constexpr (kind < 2) load_qk(qk_so);
      else {
        ck.me = RFL(cc[0]); ck.re = RFL(cc[1]); ck.rho = RFL(cc[2]);
        mT = ck.local(sd.on_time());
        u32x2 pub = {(unsigned)s + 1u, mT}; PIN_V(pub);
        const unsigned lim = sd.limit(a);
        SB();
        if (S_FINE) STAMP(3);
        if (lane == h) an = own;
        const u64 mis = __builtin_amdgcn_ballot_w64(max(an[0], an[1]) == SENT) | (__builtin_amdgcn_ballot_w64((int)(an[0] - lim) > 0) & 0xffffffffull);
        owed = s > 0 && mis == 0;
        if (__builtin_expect(!owed, 0)) {
          an = ann_valid(an, rs, an_ld, sl0, dead);
          sd.next(an, a, s == 0);
          mT = ck.local(sd.T); pub[1] = mT;
        }
        *(LAS u32x2*)(lds + S_CT) = pub;
        SB();
        load_qk(qk_so);
      }
      SB();
      token_sums(e, em);
    }
    SB();
    if (S_FINE) STAMP(4);
    if constexpr (kind < 2) {
      u32x2 v;
      do { v = *(volatile LAS u32x2*)(lds + S_CT); } while (RFL(v[0]) != (unsigned)s + 1u);
      mT = RFL(v[1]);
    }
    STAMP(7);
    STAMP_FLUSH(s, mT0, owed ? sd.on_time() : sd.T, bad != 0);
  }
  tk.a(); tk.b(); tk.c();
  ST16(tk.d(), rp, pt_st, (unsigned)(NSEG - 1) * PT_SEG);
  asm volatile("s_waitcnt vmcnt(0)" ::: "memory");
  WG_BARRIER();
  __builtin_amdgcn_raw_ptr_buffer_atomic_add_i32(1, rs, l16 << 20, w < 2 ? OFS_CW + CW_SS + (unsigned)(NSEG - 2 + w) * 128u : FAR, 0);
  TMARK(3);
  STAMP_DUMP();
  __builtin_amdgcn_endpgm();
}

constexpr int O_M = 0, O_RED = O_M + 10240, O_CT = O_RED + 3072;
#define P_GAP 3
#define B_GAP 15
#define OM_LAG 80u
__device__ __forceinline__ void out_role(const RecArgs& a, LAS char* lds, int rep, int k, unsigned posM, unsigned posP) {
  const int lane = fresh_lane(), w = __builtin_amdgcn_readfirstlane(threadIdx.x >> 6), c = lane & 15, g = lane >> 4, hf = c >> 3, i8 = c & 7;
  const int i = rep * 10 + k, iod = rep >> 2, ct = i >> 1, o = i & 1;
  const __amdgpu_buffer_rsrc_t rs = RSRC(a.sync, SYNC_LAYER), ry = RSRC(a.y, Y_IMG), rz = RSRC(a.z, Z_IMG);
  const unsigned l16 = lane * 16, l20 = l16 << 20;
  Clock ck; ck.init();
  const int t0 = 8 * iod + 2 * w, t2 = 8 * (1 - iod) + 2 * w;
  u32x8 UV[8];
  {
    const __amdgpu_buffer_rsrc_t rw = RSRC(a.uf, 80 * 16 * 20480);
    static_for<40>([&](auto F) {
      constexpr int f = decltype(F)::value, sl = f / 10, L = f % 10;
      const unsigned so = (unsigned)(i * 16 + (sl < 2 ? t0 : t2) + (sl & 1)) * 20480u + L * 2048u;
      if (f < 32) { ld_a<8 * (f & 31)>(rw, l16, so); ld_a<8 * (f & 31) + 4>(rw, l16, so + 1024u); VM_WAIT(40); }
      else UV[f & 7] = cat2(LDP16(rw, l16, so), LDP16(rw, l16 + 1024, so));
    });
    VM_WAIT(0);
#pragma unroll
    for (int f = 0; f < 8; ++f) PIN_V(UV[f]);
  }
  const int sidx = hf ? 0xEEEE : 0x4444;
  const int hq = 2 * ((g < 2 ? t0 : t2) + (g & 1)) + hf;
  const unsigned y_vo = hf * 40960u + g * 256u + o * 128u + i8 * 16u;
  const unsigned z_vo = hq * 256u + o * 128u + i8 * 16u;
  const unsigned p_vo = OFS_P + posP * 4096u + l16, pm_vo = OFS_P + 4 * ROW + posP * 4096u + hq * 128u + i8 * 16u;
  const unsigned pA = (unsigned)(t0 >> 2) * ROW + (t0 & 3) * 1024u, pB = (unsigned)(t2 >> 2) * ROW + (t2 & 3) * 1024u;
  const unsigned m_vo = posM * 4096u + l16;
  const unsigned an_ld = lane < 32 ? ((lane >> 4) * 2 + 1) * ROW + posM * 4096u + 1024u + c * 128u : OOB;
  const int Lm = i >> 3;
  const unsigned m_st = ((Lm / 5) * 2 + (Lm % 5 == 4)) * ROW + (lane >> 3) * 4096u + (Lm % 5 & 3) * 1024u + (i & 7) * 128u + i8 * 16u;
  unsigned mo[4], ml[4], mc[4];
#pragma unroll
  for (int q = 0; q < 4; ++q) {
    const int L = w == 1 ? q : 3 * w - 2 + q;
    const bool skip = (w != 1 && q == 3) || (L == Lm && (lane >> 3) == (i & 7));
    mo[q] = skip ? OOB : m_vo; ml[q] = skip ? O_RED + 2304 + (lane & 31) * 16 : O_M + L * 1024 + lane * 16;
    mc[q] = ((L / 5) * 2 + (L % 5 == 4)) * ROW + (L % 5 & 3) * 1024u;
  }
  const unsigned own_l = lane < 8 ? O_M + i * 128 + lane * 16 : O_RED + 2304 + (lane & 31) * 16;
  if (w == 0) { LDS16(own_l) = LD16(rs, m_st, 0); LDS16(O_CT) = (u32x4){0u, 0u, 1u, 0u}; *(LAS unsigned*)(lds + O_CT + 16) = 0u; }
  const unsigned rz_vo = w < 2 ? OFS_P + ((2 * k + w) >> 2) * ROW + posP * 4096u + ((2 * k + w) & 3) * 1024u + l16
                       : w == 2 ? ((k / 5) * 2 + (k % 5 == 4)) * ROW + (k % 5 & 3) * 1024u + m_vo
                       : k < 4 ? ((k >> 1) * 2 + 1) * ROW + 1024u + (k & 1) * 1024u + m_vo : OOB;
  int rz_b = w == 3 ? 2 : 1; unsigned rz_m = w < 2 ? 1u : 3u, rz_k = w < 2 ? P_SLOT : M_SLOT;
  asm volatile("" : "+s"(rz_b), "+s"(rz_m), "+s"(rz_k));
  const unsigned red_w = (g >> 1) == hf ? O_RED + i8 * 272 + (g & 1) * 128 + (w * 2 + hf) * 8 : O_RED + 2304 + lane * 8;
  const unsigned red_r = O_RED + i8 * 272 + g * 64 + hf * 32;
  const unsigned jp_vo = lane < 4 ? OFS_CW + CW_JP + (unsigned)(40 + rep * 8 + (16 * k + (lane & 1) * 15) / 20) * 128u : OOB;
  unsigned jhave = 0;
  bool dead = false;
  ck.edge(); ck.rate();
  Sched sd; sd.init(ck.global(mtc()), a);
  unsigned mM = mtc();
  TMARK(1);
  STAMP_DECL(8);
  u32x4 zf = {0u, 0u, 0u, 0u};
  auto step = [&](int s, auto LAST, auto KIND) {
    constexpr bool last = decltype(LAST)::value, kp = decltype(KIND)::value == 2, w0 = decltype(KIND)::value == 0;
    constexpr int NC = w0 ? 0 : kp ? 3 : 4;
    const unsigned sl = (unsigned)(s & 3) * M_SLOT, mT = mM;
    need_progress(rs, jp_vo, (unsigned)s + 1u, jhave);
    u32x4 yf[4];
    const unsigned ys = (unsigned)(s * NH * 40 + ct) * 1024u, z_so = s < 1 ? FAR : (unsigned)((s - 1) * 40 + ct) * 8192u;
    if constexpr (kp) static_for<8>([&](auto K) { ck.rho_step<decltype(K)::value>(); });
    f32x4 za[4];
#pragma unroll
    for (int t = 0; t < 4; ++t) zero4(za[t]);
    unsigned sv = SENT; PIN_V(sv);
    u32x4 pz = {sv, sv, sv, sv}; PIN_V(pz);
    const unsigned rz_so = s < rz_b ? FAR : ((unsigned)(s - rz_b) & rz_m) * rz_k, fl_so = s > 1 ? OFS_CW + CW_SO + rep * 128u + (unsigned)(s - 2) * 1024u : FAR;
    const unsigned sp = (unsigned)(s & 1) * P_SLOT, sn = (unsigned)((s + 1) & 3) * M_SLOT;
    asm volatile("" :: "s"(rz_so), "s"(fl_so), "s"(z_so), "s"(ys + t0 * 81920u), "s"(ys + t2 * 81920u), "s"(sl + mc[0]), "s"(sl + mc[1]), "s"(sl + mc[2]), "s"(sl + mc[3]),
                 "s"(sp + pA), "s"(sp + pB), "s"(sp), "s"(sn));
    STAMP(0);
    wait_until(mM);
    SB();
    u32x4 cm[4];
#pragma unroll
    for (int q = 0; q < NC; ++q) cm[q] = LD16(rs, mo[q], sl + mc[q]);
    SB();
    auto bulk = [&](auto K) {
      constexpr int k = decltype(K)::value;
      if constexpr (k < 4) { if constexpr (!last) yf[k] = LD16(ry, y_vo, ys + (k < 2 ? t0 : t2) * 81920u + (k & 1) * 81920u); }
      else __builtin_amdgcn_raw_buffer_store_b128(zf, rz, z_vo, z_so, 16);
    };
    if (w0) NOPS(15);
    NOPS(15); NOPS(B_GAP); static_for<5>(bulk); SB();
    unsigned jv;
    if constexpr (kp) { jv = LD4(rs, jp_vo, jhave < min((unsigned)NSEG, (unsigned)s + 6u) ? 0u : FAR); SB(); }
    unsigned fails = 0;
    if constexpr (!w0) {
      unsigned chk = 0u;
#pragma unroll
      for (int q = 0; q < NC; ++q) chk = mx3(chk, cm[q][0], cm[q][2]);
      if (__builtin_expect(__any(chk == SENT), 0)) {
        fails = 1;
        *(LAS unsigned*)(lds + O_CT + 8) = (unsigned)s + 1u;
        for (unsigned spins = 0; !dead;) {
          __builtin_amdgcn_s_sleep(4);
          bool nd[NC]; u32x4 n[NC]; unsigned m = 0;
#pragma unroll
          for (int q = 0; q < NC; ++q) { nd[q] = mx4(cm[q]) == SENT; n[q] = LD16(rs, nd[q] ? m_vo : OOB, sl + mc[q]); }
#pragma unroll
          for (int q = 0; q < NC; ++q) { if (nd[q]) cm[q] = n[q]; m = max(m, mx4(cm[q])); }
          if (!__any(m == SENT)) break;
          if (++spins > SPIN_CAP) dead = true;
          asm volatile("" ::: "memory");
        }
      }
#pragma unroll
      for (int q = 0; q < NC; ++q) LDS16(ml[q]) = cm[q];
    }
    WG_BARRIER();
    STAMP(1);
    unsigned lt = 0u;
    {
      u32x4 mf[4];
      mf[0] = LDS16(O_M + l16); mf[1] = LDS16(O_M + 1024 + l16); mf[2] = LDS16(O_M + 2048 + l16);
      if constexpr (kp) lt = *(LAS unsigned*)(lds + O_CT + 8);
      SB();
      static_for<kp ? 8 : 10>([&](auto LL) {
        constexpr int L = decltype(LL)::value;
        smf_a<8 * L>(za[0], mf[L % 4], sidx); SB();
        if (L + 3 < 10) mf[(L + 3) % 4] = LDS16(O_M + (L + 3) * 1024 + l16);
        SB();
        smf_a<80 + 8 * L>(za[1], mf[L % 4], sidx); SB();
        if (L == 0) __builtin_amdgcn_raw_buffer_store_b128(pz, rs, rz_vo, rz_so, 16);
        if (L == 1 && w0) __builtin_amdgcn_raw_ptr_buffer_atomic_add_i32(1, rs, l20, fl_so, 0);
        SB();
        smf_a<160 + 8 * L>(za[2], mf[L % 4], sidx);
        if (L < 2) smf_a<240 + 8 * (L & 1)>(za[3], mf[L % 4], sidx); else smf_v(za[3], mf[L % 4], UV[L < 2 ? 0 : L - 2], sidx);
        SB();
      });
      asm volatile("" :: "v"(pz), "v"(zf));
      if constexpr (kp) ck.edge_z(za, mf[0], mf[1], UV[6], UV[7], sidx); else NOPS(7);
    }
    STAMP(2);
    unsigned zp[4];
    auto zpost = [&](int q) {
      auto x = __builtin_amdgcn_permlane32_swap(f2u(za[q >> 1][2 * (q & 1)]), f2u(za[2 + (q >> 1)][2 * (q & 1)]), false, false);
      auto y = __builtin_amdgcn_permlane32_swap(f2u(za[q >> 1][2 * (q & 1) + 1]), f2u(za[2 + (q >> 1)][2 * (q & 1) + 1]), false, false);
      zp[q] = pack2(u2f(x[0]) + u2f(x[1]), u2f(y[0]) + u2f(y[1])); pin_v(zp[q]);
      if (q == 3) {
        auto u = __builtin_amdgcn_permlane16_swap(zp[0], zp[2], false, false);
        auto v = __builtin_amdgcn_permlane16_swap(zp[1], zp[3], false, false);
        zf = (u32x4){u[0], v[0], u[1], v[1]}; pin_v(zf);
      }
    };
    if constexpr (last) { zpost(0); zpost(1); zpost(2); zpost(3); ST16(zf, rz, z_vo, (unsigned)(s * 40 + ct) * 8192u); return; }
    SB();
    unsigned mP;
    if constexpr (kp) {
      mP = ck.local(sd.tP);
      if (__builtin_expect(RFL(lt) == (unsigned)s + 1u, 0)) {
        fails |= 4;
        const u32x2 r = ann_valid((u32x2){SENT, SENT}, rs, an_ld, sl, dead);
        ck.edge();
        mP = ck.local(sd.T + (unsigned)max32((int)(r[0] - sd.T)) + a.d0);
      }
      *(LAS u32x2*)(lds + O_CT) = (u32x2){(unsigned)s + 1u, mP};
    } else {
      u32x2 v;
      do { v = *(volatile LAS u32x2*)(lds + O_CT); } while (RFL(v[0]) != (unsigned)s + 1u);
      mP = RFL(v[1]);
    }
    STAMP(3);
    SB();
    wait_until(mP);
    SB();
    u32x4 pf[5];
    pf[0] = LD16(rs, p_vo, sp + pA); SB(); zpost(0); NOPS(P_GAP);
    pf[1] = LD16(rs, p_vo, sp + pA + 1024u); SB(); zpost(1); NOPS(P_GAP);
    pf[2] = LD16(rs, p_vo, sp + pB); SB(); zpost(2); NOPS(P_GAP);
    pf[3] = LD16(rs, p_vo, sp + pB + 1024u); SB(); zpost(3); SB();
    pf[4] = LD16(rs, pm_vo, sp); SB();
    u32x2 an;
    if constexpr (kp) { NOPS(15); NOPS(7); an = LD8(rs, an_ld, sl); }
    STAMP(4);
    SB();
    f32x4 d;
    unsigned chk;
    mf32z(d, yf[0], pf[0]); SB(); chk = max(pf[0][0], pf[0][2]); PIN_V(chk); SB();
    mf32(d, yf[1], pf[1]); SB(); chk = mx3(chk, pf[1][0], pf[1][2]); PIN_V(chk); SB();
    mf32(d, yf[2], pf[2]); SB(); chk = mx3(chk, pf[2][0], pf[2][2]); PIN_V(chk); SB();
    mf32(d, yf[3], pf[3]); SB(); chk = mx3(chk, pf[3][0], pf[3][2]); PIN_V(chk); SB();
    mf32(d, zf, pf[4]); SB(); chk = mx3(chk, pf[4][0], pf[4][2]);
    u64 bad = __builtin_amdgcn_ballot_w64(chk == SENT); asm volatile("" : "+s"(bad));
    NOPS(5);
    *(LAS f32x2*)(lds + red_w) = (f32x2){d[0], d[1]}; *(LAS f32x2*)(lds + red_w + 64) = (f32x2){d[2], d[3]};
    SB();
    if (__builtin_expect(bad != 0, 0)) {
      fails |= 2;
      for (unsigned spins = 0; !dead;) {
        __builtin_amdgcn_s_sleep(2);
        bool nd[5]; u32x4 n[5]; unsigned m = 0;
#pragma unroll
        for (int q = 0; q < 5; ++q) { nd[q] = mx4(pf[q]) == SENT; n[q] = LD16(rs, nd[q] ? (q < 4 ? p_vo : pm_vo) : OOB, sp + (q < 2 ? pA : q < 4 ? pB : 0u) + (q & 1) * 1024u); }
#pragma unroll
        for (int q = 0; q < 5; ++q) { if (nd[q]) pf[q] = n[q]; m = max(m, mx4(pf[q])); }
        if (!__any(m == SENT)) break;
        if (++spins > SPIN_CAP) dead = true;
        asm volatile("" ::: "memory");
      }
#pragma unroll
      for (int q = 0; q < 5; ++q) PIN_V(pf[q]);
      NOPS(1);
      mf32z(d, yf[0], pf[0]); mf32(d, yf[1], pf[1]); mf32(d, yf[2], pf[2]); mf32(d, yf[3], pf[3]); mf32(d, zf, pf[4]);
      NOPS(7);
      *(LAS f32x2*)(lds + red_w) = (f32x2){d[0], d[1]}; *(LAS f32x2*)(lds + red_w + 64) = (f32x2){d[2], d[3]};
    }
    STAMP(5);
    if constexpr (w0) {
      asm volatile("s_waitcnt lgkmcnt(0)\n\ts_barrier ; first wave" ::: "memory");
      const f32x4 e = __builtin_bit_cast(f32x4, LDS16(red_r)) + __builtin_bit_cast(f32x4, LDS16(red_r + 16));
      const f32x2 u = (f32x2){e[0], e[1]} + (f32x2){e[2], e[3]};
      unsigned o0, o1, o2, o3;
      asm volatile("s_nop 1\n\tv_add_f32_dpp %0, %4, %4 row_ror:8 row_mask:0xf bank_mask:0xf\n\tv_add_f32_dpp %1, %5, %5 row_ror:8 row_mask:0xf bank_mask:0xf\n\t"
                   "v_cvt_pk_bf16_f32 %2, %0, %1\n\tv_min_u32 %0, -2, %2\n\tv_min_u32 %1, -2, %2\n\ts_nop 1\n\tv_permlane16_swap_b32 %0, %1\n\t"
                   "v_mov_b32 %2, %0\n\tv_mov_b32 %3, %1\n\ts_nop 0\n\tv_permlane32_swap_b32 %0, %2\n\tv_permlane32_swap_b32 %1, %3"
                   : "=&v"(o0), "=&v"(o1), "=&v"(o2), "=&v"(o3) : "v"(u[0]), "v"(u[1]));
      ST16(((u32x4){o0, o1, o2, o3}), rs, m_st, sn);
      LDS16(own_l) = (u32x4){o0, o1, o2, o3};
    } else if (kp) asm volatile("s_waitcnt lgkmcnt(0)\n\ts_barrier" ::: "memory");
    else asm volatile("s_waitcnt lgkmcnt(0)\n\ts_barrier\n\ts_sleep 3" ::: "memory");
    SB();
    STAMP(6);
    if constexpr (kp) {
      sd.next(ann_valid(an, rs, an_ld, sl, dead), a, s == 0);
      mM = ck.local(sd.T + OM_LAG);
      jv = min(jv, dppu<0xB1>(jv)); jhave = max(jhave, RFL(jv));
      LDS16(O_CT + 16) = (u32x4){(unsigned)s + 1u, mM, jhave, 0u};
    } else {
      u32x4 v;
      do { v = *(volatile LAS u32x4*)(lds + O_CT + 16); } while (RFL(v[0]) != (unsigned)s + 1u);
      mM = RFL(v[1]); jhave = max(jhave, RFL(v[2]));
    }
    STAMP(7);
    STAMP_FLUSH(s, mT - OM_LAG * 21u / 8u, mP, fails);
  };
  auto run = [&](auto KIND) {
#pragma unroll 1
    for (int s = 0; s < NSEG - 1; ++s) step(s, std::false_type(), KIND);
    step(NSEG - 1, std::true_type(), KIND);
  };
  if (w == 0) run(std::integral_constant<int, 0>()); else if (w == 3) run(std::integral_constant<int, 2>()); else run(std::integral_constant<int, 1>());
  VM_WAIT(0);
  WG_BARRIER();
  __builtin_amdgcn_raw_ptr_buffer_atomic_add_i32(1, rs, l20, w < 2 ? OFS_CW + CW_SO + rep * 128u + (unsigned)(NSEG - 2 + w) * 1024u : FAR, 0);
  TMARK(3);
  STAMP_DUMP();
}

#define J_FLAG 11
#define J_ST0 12
#define J_STAG 2
__host__ __device__ constexpr int j_slot(int kb) { return (kb + 1) % 3; }
template <bool YO>
__device__ __forceinline__ void proj_role(const RecArgs& a, LAS char* lds, int jn) {
  const int lane = fresh_lane(), w = __builtin_amdgcn_readfirstlane(threadIdx.x >> 6), c = lane & 15, g = lane >> 4;
  const __amdgpu_buffer_rsrc_t rs = RSRC(a.sync, SYNC_LAYER), rx = RSRC(a.xf, S_ * DM * 2), ro = YO ? RSRC(a.y, Y_IMG) : RSRC(a.qk, 2 * QK_IMG);
  const unsigned l16 = lane * 16;
#define DMA_P(so, lo, p) dma1k<((p) & 3) * 1024>(rx, l16, (so) + ((p) >> 2) * 4096u, (lo) + ((p) >> 2) * 4096u)
  const unsigned lb = (unsigned)(uintptr_t)lds + w * 10240u;
  static_for<20>([&](auto P_) { constexpr int p = decltype(P_)::value; DMA_P(w * 10240u + (p / 10) * 40960u, lb + (p / 10) * 40960u, p % 10); });
  const int T0 = 20 * (YO ? jn - 40 : jn) + 5 * w;
  u32x4 WV[36];
  {
    const __amdgpu_buffer_rsrc_t rw = YO ? RSRC(a.uf, 80 * 16 * 20480) : RSRC(a.wf, 768 * 20480);
    static_for<100>([&](auto F) {
      constexpr int f = decltype(F)::value;
      const unsigned so = (unsigned)T0 * 20480u + f * 1024u;
      if (f < 64) { ld_a<4 * (f & 63)>(rw, l16, so); VM_WAIT(24); } else WV[f < 64 ? 0 : f - 64] = LDP16(rw, l16, so);
    });
#pragma unroll
    for (int f = 0; f < 36; ++f) PIN_V(WV[f]);
  }
  const unsigned o_vo = YO ? (c >> 3) * 40960u + g * 256u + (c & 7) * 16u : l16;
  const unsigned l20 = l16 << 20, f_so = w == 0 ? OFS_CW + CW_JP + jn * 128u : FAR;
  unsigned o_so[5];
#pragma unroll
  for (int t = 0; t < 5; ++t) {
    const unsigned T = T0 + t;
    o_so[t] = YO ? (T & 15u) * 81920u + (T >> 5) * 1024u + ((T >> 4) & 1u) * 128u : T < 384u ? T * 1024u : T < 768u ? QK_IMG + (T - 384u) * 1024u : FAR;
  }
  TMARK(1);
  constexpr unsigned OSEG = YO ? NH * 40960u : 384u * 1024u;
  u32x4 pend[5], xr[3][2];
  f32x4 acc[10];
#pragma unroll
  for (int q = 0; q < 10; ++q) zero4(acc[q]);
#pragma unroll
  for (int t = 0; t < 5; ++t) pend[t] = (u32x4){0u, 0u, 0u, 0u};
  auto pack = [&](int q) { unsigned u = pack2(acc[q][0], acc[q][1]), v = pack2(acc[q][2], acc[q][3]); pin_v(u); pin_v(v); pend[q >> 1][2 * (q & 1)] = u; pend[q >> 1][2 * (q & 1) + 1] = v; };
  VM_WAIT(0);
  WG_BARRIER();
  unsigned bo = 0u;
  xr[1][0] = LDS16(l16); xr[1][1] = LDS16(l16 + 20480);
#pragma unroll 1
  for (int s = 0; s < NSEG; ++s) {
    if (J_STAG && s >= J_ST0 && s < J_ST0 + 8) { for (int d = ((int)(blockIdx.x >> 3) * 5 + (int)(blockIdx.x & 7) * 2) % 13; d > 0; --d) NOPS(J_STAG); }
    const unsigned bn = bo == 81920u ? 0u : bo + 40960u, bf = bn == 81920u ? 0u : bn + 40960u;
    const unsigned pso = s > 0 ? (unsigned)(s - 1) * OSEG : FAR, dso = (unsigned)min(s + 2, NSEG - 1) * 40960u + w * 10240u, dlo = lb + bf;
    unsigned xb = bo + l16, xn = bn + l16, sv = (unsigned)s; PIN_V(xb); PIN_V(xn); PIN_V(sv);
    asm volatile("" :: "s"(pso), "s"(dso), "s"(dlo));
    SB();
    static_for<200>([&](auto N) {
      constexpr int n = decltype(N)::value, kb = n / 10, q = n % 10, f = (q >> 1) * 20 + kb;
      constexpr int sl = j_slot(kb);
      if (f < 64) {
        if (kb == 0) { if (YO) mf32z_ba<4 * (f & 63)>(acc[q], xr[sl][q & 1]); else mf32z_aa<4 * (f & 63)>(acc[q], xr[sl][q & 1]); }
        else { if (YO) mf32_ba<4 * (f & 63)>(acc[q], xr[sl][q & 1]); else mf32_aa<4 * (f & 63)>(acc[q], xr[sl][q & 1]); }
      } else {
        const u32x4 wv = WV[f < 64 ? 0 : f - 64], xv = xr[sl][q & 1];
        if (kb == 0) { if (YO) mf32z(acc[q], xv, wv); else mf32z(acc[q], wv, xv); }
        else { if (YO) mf32(acc[q], xv, wv); else mf32(acc[q], wv, xv); }
      }
      SB();
      if constexpr (kb < 18) { if (q == 3 || q == 4) xr[j_slot(kb + 2)][q - 3] = LDS16(xb + (q - 3) * 20480 + (kb + 2) * 1024); }
      else if constexpr (kb == 19) { if (q < 2) xr[1][q] = LDS16(xn + q * 20480); }
      if constexpr (kb == 0) { if (q < 2) xr[2][q] = LDS16(xb + q * 20480 + 1024); }
      if constexpr (q == 7 && kb >= 1 && kb < 6) __builtin_amdgcn_raw_buffer_store_b128(pend[kb - 1], ro, o_vo, o_so[kb - 1] + pso, 16);
      if constexpr (q == 7 && kb == J_FLAG) {
        asm volatile("s_waitcnt vmcnt(0)\n\ts_barrier" ::: "memory");
        __builtin_amdgcn_raw_buffer_store_b32(sv, rs, l20, f_so, 16);
      }
      if constexpr ((q == 6 || q == 8) && kb > J_FLAG && 2 * (kb - J_FLAG - 1) < 10) DMA_P(dso, dlo, 2 * (kb - J_FLAG - 1) + (q == 8));
      SB();
      if constexpr (kb == 19 && q >= 3) pack(q - 3);
      if constexpr (kb == 0 && q < 3) pack(7 + q);
      SB();
    });
    bo = bn;
  }
  NOPS(7);
  pack(7); pack(8); pack(9);
#pragma unroll
  for (int t = 0; t < 5; ++t) ST16(pend[t], ro, o_vo, o_so[t] + (NSEG - 1) * OSEG);
  VM_WAIT(0);
  WG_BARRIER();
  __builtin_amdgcn_raw_buffer_store_b32((unsigned)NSEG, rs, l16 << 20, w == 0 ? OFS_CW + CW_JP + jn * 128u : FAR, 16);
  TMARK(2);
}

constexpr int T_LD = 84;
#define T_SLP 60
#define T_GRP 5
#define T_FLY 0
__device__ __forceinline__ void token_role(const RecArgs& a, LAS char* lds, int rep) {
  const int tid = threadIdx.x, w = __builtin_amdgcn_readfirstlane(tid >> 6);
  const __amdgpu_buffer_rsrc_t rs = RSRC(a.sync, SYNC_LAYER), ry = RSRC(a.y, Y_IMG), rz = RSRC(a.z, Z_IMG), rp = RSRC(a.pt, PT_IMG), rx = RSRC(a.xr, S_ * DM * 2);
  LAS float* part = (LAS float*)lds;
  LAS unsigned* tick = (LAS unsigned*)(lds + 4 * 32 * T_LD * 4);
  auto wait_count = [&](unsigned off, unsigned want, bool calm) {
    if (w == 0) {
      for (unsigned spins = 0; spins < SPIN_CAP; ++spins) {
        if (RFL(LD4(rs, off, 0)) >= want) break;
        if (calm) __builtin_amdgcn_s_sleep(T_SLP); else __builtin_amdgcn_s_sleep(6);
        asm volatile("" ::: "memory");
      }
    }
    WG_BARRIER();
  };
#pragma unroll 1
  for (;;) {
    WG_BARRIER();
    if (tid == 0) *tick = __hip_atomic_fetch_add((GAS unsigned*)(a.sync + OFS_CW + CW_TQ + rep * 128), 1u, RLX_AGENT);
    WG_BARRIER();
    const int n = __builtin_amdgcn_readfirstlane(*(volatile LAS unsigned*)tick);
    if (n >= 64) { TMARK(3); return; }
    const int s = rep + 8 * (n >> 3), cg = n & 7;
    const bool calm = s < NSEG - 3;
    int lane = tid & 63; PIN_V(lane);
    const int c = lane & 15, g = lane >> 4;
    u32x4 xo[2];
#pragma unroll
    for (int r = 0; r < 2; ++r) { const int p = r * 256 + tid; if (p < 320) xo[r] = *(const u32x4*)(a.xold + (size_t)(s * 32 + (p & 31)) * DM + 80 * cg + (p >> 5) * 8); }
    if (!calm) wait_count(OFS_CW + CW_SS + (s - 4) * 128, 32u, true);
    wait_count(OFS_CW + CW_SO + (s * 8 + cg) * 128, 10u, calm);
    const unsigned l16 = lane * 16;
    static_for<50>([&](auto F) {
      constexpr int f = decltype(F)::value, kk = f / 5, t = f % 5;
      if (kk < 8) ld_a1<4 * f>(ry, l16, (unsigned)((s * NH + 8 * w + kk) * 40 + 5 * cg + t) * 1024u);
      else ld_a1<4 * f>(rz, l16, (unsigned)((s * 40 + 5 * cg + t) * NH + 8 * w + 4 * (kk - 8)) * 256u);
      if (f % T_GRP == T_GRP - 1 && calm) VM_WAIT(T_FLY);
    });
    VM_WAIT(40);
    wait_count(OFS_CW + CW_SS + s * 128, 32u, calm);
    u32x4 bf[10][2];
#pragma unroll
    for (int kk = 0; kk < 10; ++kk)
#pragma unroll
      for (int b = 0; b < 2; ++b) {
        bf[kk][b] = LD16(rp, l16, (unsigned)s * PT_SEG + (kk < 8 ? (unsigned)((8 * w + kk) * 2 + b) * 1024u : PT_M + (unsigned)(b * NH + 8 * w + 4 * (kk - 8)) * 256u));
        if (b == 1 && (kk & 1) && calm) VM_WAIT(T_FLY);
      }
    f32x4 acc[5][2];
    static_for<100>([&](auto N) {
      constexpr int n = decltype(N)::value, kk = n / 10, t = (n % 10) >> 1, b = n & 1;
      if (kk == 0) mf32z_aa<4 * t>(acc[t][b], bf[0][b]); else mf32_aa<4 * (5 * kk + t)>(acc[t][b], bf[kk][b]);
    });
    NOPS(7);
#pragma unroll
    for (int t = 0; t < 5; ++t)
#pragma unroll
      for (int b = 0; b < 2; ++b) {
        LAS float* p = part + (w * 32 + 16 * b + c) * T_LD + 16 * t + 8 * (g & 1) + 2 * (g >> 1);
        *(LAS f32x2*)p = (f32x2){acc[t][b][0], acc[t][b][1]}; *(LAS f32x2*)(p + 4) = (f32x2){acc[t][b][2], acc[t][b][3]};
      }
    WG_BARRIER();
#pragma unroll
    for (int r = 0; r < 2; ++r) {
      const int p = r * 256 + tid;
      if (p < 320) {
        const LAS float* q = part + (p & 31) * T_LD + (p >> 5) * 8;
        f32x4 s0 = *(const LAS f32x4*)q, s1 = *(const LAS f32x4*)(q + 4);
#pragma unroll
        for (int k = 1; k < 4; ++k) { s0 += *(const LAS f32x4*)(q + k * 32 * T_LD); s1 += *(const LAS f32x4*)(q + k * 32 * T_LD + 4); }
        u32x4 o;
#pragma unroll
        for (int k = 0; k < 2; ++k) {
          o[k] = pack2(bf2f(xo[r][k] & 0xffff) + rbf(s0[2 * k]), bf2f(xo[r][k] >> 16) + rbf(s0[2 * k + 1]));
          o[2 + k] = pack2(bf2f(xo[r][2 + k] & 0xffff) + rbf(s1[2 * k]), bf2f(xo[r][2 + k] >> 16) + rbf(s1[2 * k + 1]));
        }
        *(u32x4*)(a.xnew + (size_t)(s * 32 + (p & 31)) * DM + 80 * cg + (p >> 5) * 8) = o;
        ST16(o, rx, (unsigned)(((p >> 4) & 1) * 20480 + (10 * cg + (p >> 5)) * 256 + (p & 15) * 16), (unsigned)s * 40960u);
      }
    }
    VM_WAIT(0);
    WG_BARRIER();
    __builtin_amdgcn_raw_ptr_buffer_atomic_add_i32(1, rs, l16 << 20, w == 0 ? OFS_CW + CW_TC + (unsigned)s * 128u : FAR, 0);
  }
}

#define B_SETS 6
#define B_CUT 46
constexpr int B_ONE = 2 * 40960, B_ST = B_ONE + 20480;
template <bool VO>
__device__ __forceinline__ void bproj_role(const RecArgs& a, LAS char* lds, int rep, int s0, int s1, int ds) {
  const int lane = fresh_lane(), w = __builtin_amdgcn_readfirstlane(threadIdx.x >> 6), c = lane & 15, g = lane >> 4;
  const __amdgpu_buffer_rsrc_t rs = RSRC(a.sync, SYNC_LAYER), rx = RSRC(a.xr, S_ * DM * 2), ro = RSRC(a.qkvb, 3 * BH * S_ * 64);
  const unsigned l16 = lane * 16;
  const int T0 = 16 * rep + 4 * w;
  u32x4 WV[16];
  {
    const __amdgpu_buffer_rsrc_t rw = RSRC(a.wb, 120 * 20480);
    static_for<80>([&](auto F) {
      constexpr int f = decltype(F)::value;
      const unsigned so = (unsigned)T0 * 20480u + f * 1024u;
      if (f < 64) { ld_a<4 * (f & 63)>(rw, l16, so); VM_WAIT(40); } else WV[f & 15] = LDP16(rw, l16, so);
    });
    VM_WAIT(0);
#pragma unroll
    for (int f = 0; f < 16; ++f) PIN_V(WV[f]);
  }
  auto mm = [&](auto F, auto Z, f32x4& acc, u32x4 x) {
    constexpr int f = decltype(F)::value; constexpr bool z = decltype(Z)::value;
    if constexpr (f < 64) { if (z) { if (VO) mf32z_ba<4 * f>(acc, x); else mf32z_aa<4 * f>(acc, x); } else { if (VO) mf32_ba<4 * f>(acc, x); else mf32_aa<4 * f>(acc, x); } }
    else { if (VO) mf32(acc, x, WV[f - 64]); else mf32(acc, WV[f - 64], x); }
  };
  u32x4 one = {0x3f803f80u, 0x3f803f80u, 0x3f803f80u, 0x3f803f80u}; PIN_V(one);
#pragma unroll
  for (int k = 0; k < 5; ++k) LDS16(B_ONE + (w * 5 + k) * 1024 + l16) = one;
  f32x4 cs[4];
  NOPS(1);
  static_for<80>([&](auto N) { constexpr int n = decltype(N)::value; mm(std::integral_constant<int, (n % 4) * 20 + n / 4>(), std::bool_constant<(n < 4)>(), cs[n % 4], one); });
  unsigned o_vo[4];
#pragma unroll
  for (int t = 0; t < 4; ++t) {
    const int n = 16 * (T0 + t) + (VO ? c : 4 * g), im = n / DM, hd = (n - im * DM) / BD, e = n - im * DM - hd * BD;
    o_vo[t] = n >= 3 * DM ? OOB : (unsigned)(im * BH + hd) * (S_ * 64u) + (VO ? ((g >> 1) * 32 + e) * 16 + (g & 1) * 8 : (e >> 3) * 512 + c * 16 + (e & 4) * 2);
  }
  const float qs = T0 < 40 ? 0.22360679774997896f * 1.4426950408889634f : 1.0f;
  const unsigned sa_ = (w & 1) * 20480 + l16, st_w = (c >> 2) == g ? B_ST + (w * 16 + c) * 4 : B_ST + 256 + lane * 4;
  u32x4 xq[10];
  auto ready = [&](int s) {
    for (unsigned spins = 0; spins < SPIN_CAP; ++spins) {
      if (RFL(LD4(rs, l16 << 20, OFS_CW + CW_TC + (unsigned)s * 128u)) >= 8u) break;
      if (s < NSEG - 4) __builtin_amdgcn_s_sleep(40); else __builtin_amdgcn_s_sleep(6);
      asm volatile("" ::: "memory");
    }
  };
  bool have = false;
  unsigned xb = 40960;
#pragma unroll 1
  for (int s = s0; s < s1; s += ds) {
    xb = 40960 - xb;
    const unsigned xn = 40960 - xb + w * 10240 + l16;
    if (!have) {
      ready(s);
#pragma unroll
      for (int k = 0; k < 10; ++k) xq[k] = LD16(rx, l16, (unsigned)s * 40960u + w * 10240u + k * 1024u);
#pragma unroll
      for (int k = 0; k < 10; ++k) LDS16(xb + w * 10240 + k * 1024 + l16) = xq[k];
    }
    const unsigned nx = LD4(rs, l16 << 20, s + ds < s1 ? OFS_CW + CW_TC + (unsigned)(s + ds) * 128u : FAR);
    WG_BARRIER();
    f32x4 acc[8], as; u32x4 xr[3][4];
    const unsigned xa = xb + l16, sa = xb + sa_, sb = w < 2 ? sa : B_ONE + l16;
    unsigned nso = FAR;
#define B_GET(kb, q) (xr[(kb) % 3][q] = LDS16(((q) < 2 ? xa + (q) * 20480 : (q) == 2 ? sa : sb) + (kb) * 1024))
#pragma unroll
    for (int q = 0; q < 8; ++q) B_GET(q >> 2, q & 3);
    SB();
    static_for<180>([&](auto N) {
      constexpr int n = decltype(N)::value, kb = n / 9, q = n % 9;
      if constexpr (q < 8) mm(std::integral_constant<int, (q >> 1) * 20 + kb>(), std::bool_constant<kb == 0>(), acc[q], xr[kb % 3][q & 1]);
      else { if (kb == 0) mf32z(as, xr[0][2], xr[0][3]); else mf32(as, xr[kb % 3][2], xr[kb % 3][3]); }
      SB();
      if (q >= 1 && q < 5 && kb + 2 < 20) B_GET(kb + 2, q - 1);
      if (n == 5 * 9 + 5) { have = RFL(nx) >= 8u; nso = have ? (unsigned)(s + ds) * 40960u + w * 10240u : FAR; pin_s(nso); }
      if constexpr (kb >= 5 && kb < 10 && (q == 6 || q == 7)) xq[2 * (kb - 5) + q - 6] = LD16(rx, l16, nso + (2 * (kb - 5) + q - 6) * 1024u);
      if constexpr (kb >= 15 && (q == 6 || q == 7)) LDS16(xn + (2 * (kb - 15) + q - 6) * 1024) = xq[2 * (kb - 15) + q - 6];
      SB();
    });
    NOPS(7);
    *(LAS float*)(lds + st_w) = (c & 2) ? ((c & 1) ? as[3] : as[2]) : ((c & 1) ? as[1] : as[0]);
    WG_BARRIER();
#pragma unroll
    for (int tt = 0; tt < 2; ++tt) {
      f32x4 mu, rr;
      if (VO) {
        const f32x4 sq = *(LAS f32x4*)(lds + B_ST + tt * 64 + g * 16), sm = *(LAS f32x4*)(lds + B_ST + 128 + tt * 64 + g * 16);
        mu = sm * (1.0f / DM);
#pragma unroll
        for (int r = 0; r < 4; ++r) rr[r] = __builtin_amdgcn_rsqf(fmaxf(sq[r] * (1.0f / DM) - mu[r] * mu[r], 0.f) + 1e-5f);
      } else {
        const float sq = *(LAS float*)(lds + B_ST + tt * 64 + c * 4), m1 = *(LAS float*)(lds + B_ST + 128 + tt * 64 + c * 4) * (1.0f / DM);
        const float r1 = __builtin_amdgcn_rsqf(fmaxf(sq * (1.0f / DM) - m1 * m1, 0.f) + 1e-5f) * qs;
        mu = (f32x4){m1, m1, m1, m1}; rr = (f32x4){r1, r1, r1, r1};
      }
#pragma unroll
      for (int t = 0; t < 4; ++t) {
        const f32x4 y = (acc[2 * t + tt] - mu * cs[t]) * rr;
        __builtin_amdgcn_raw_buffer_store_b64((u32x2){pack2(y[0], y[1]), pack2(y[2], y[3])}, ro, o_vo[t], (unsigned)s * 2048u + tt * (VO ? 1024u : 256u), 0);
      }
    }
  }
  TMARK(3);
  __builtin_amdgcn_endpgm();
}

__global__ __launch_bounds__(256, 1) void rec_kernel(RecArgs a) {
  __shared__ __attribute__((aligned(16))) char lds_[S_END + DBG_LDS];
  LAS char* lds = (LAS char*)lds_;
  const int b = blockIdx.x, rep = b & 7, il = b >> 3;
  AGPR_FENCE();
  TMARK(0);
  if (il < 14) {
    const int lane = threadIdx.x & 63;
    const unsigned* tab = (const unsigned*)a.cal;
    const unsigned x = tab[64 + (lane >> 3)] & 7u, xr = tab[64 + rep] & 7u;
    unsigned v = tab[(lane & 56) + (((((x & 1u) << 2) | (x >> 1)) ^ lane) & 7u)] & 0x1ffffu;
    v += __shfl_xor(v, 8); v += __shfl_xor(v, 16); v += __shfl_xor(v, 32);
    v = v * 8u + (lane & 7);
    v = min(v, (unsigned)__shfl_xor(v, 1)); v = min(v, (unsigned)__shfl_xor(v, 2)); v = min(v, (unsigned)__shfl_xor(v, 4));
    const unsigned posP = RFL(((((xr & 1u) << 2) | (xr >> 1)) ^ v ^ (unsigned)a.layer) & 7u), posM = posP ^ 4u;
    const int w = __builtin_amdgcn_readfirstlane(threadIdx.x >> 6);
    if (il < 4) {
      if (w < 2) score_role<0>(a, lds, rep, il, posM, posP); else if (w == 2) score_role<1>(a, lds, rep, il, posM, posP); else score_role<2>(a, lds, rep, il, posM, posP);
    } else out_role(a, lds, rep, il - 4, posM, posP);
    return;
  }
  if (il < 19) { if (b != 19 * 8 - 1) proj_role<false>(a, lds, b - 14 * 8); }
  else if (il < 27) proj_role<true>(a, lds, 40 + rep * 8 + il - 19);
  AGPR_FENCE();
  if (il > 27 - B_SETS - 1 && il <= 27) {
    const int s0 = il == 27 ? 0 : B_CUT + 26 - il, s1 = il == 27 ? B_CUT : NSEG, ds = il == 27 ? 1 : B_SETS;
    if (rep < 5) bproj_role<false>(a, lds, rep, s0, s1, ds); else bproj_role<true>(a, lds, rep, s0, s1, ds);
  }
  AGPR_FENCE();
  token_role(a, lds, rep);
}

#define NKS 2
constexpr int KQ = S_ / NKS;
DEV f32x16 qk32(u32x4 k, u32x4 q, f32x16 c) { return __builtin_amdgcn_mfma_f32_32x32x16_bf16(__builtin_bit_cast(bf16x8, k), __builtin_bit_cast(bf16x8, q), c, 0, 0, 0); }
template <int H> DEV f32x16 pv32(u32x4 v, f32x16 p, f32x16 c) {
  typedef __attribute__((ext_vector_type(8))) float f32x8;
  const f32x8 h = __builtin_shufflevector(p, p, 8 * H, 8 * H + 1, 8 * H + 2, 8 * H + 3, 8 * H + 4, 8 * H + 5, 8 * H + 6, 8 * H + 7);
  return __builtin_amdgcn_smfmac_f32_32x32x32_bf16(__builtin_bit_cast(hbf16x8, v), __builtin_bit_cast(hbf16x16, h), c, 0xDDDD, 0, 0);
}
DEV float max16(f32x16 s) {
  float mx = fmx(fmx(s[0], s[1]), s[2]);
#pragma unroll
  for (int i = 3; i < 15; i += 2) mx = fmx(fmx(mx, s[i]), s[i + 1]);
  mx = fmx(mx, s[15]);
  auto rr = __builtin_amdgcn_permlane32_swap(f2u(mx), f2u(mx), false, false);
  return fmx(u2f(rr[0]), u2f(rr[1]));
}
__global__ __launch_bounds__(256 * NKS, NKS) void attb_kernel(const u16* __restrict__ Qp, const u16* __restrict__ Kp, const u16* __restrict__ VbT,
                                                       u16* __restrict__ O) {
  __shared__ float mrg_[4][NKS][2][13][64];
  const int lane = threadIdx.x & 63, wv = __builtin_amdgcn_readfirstlane(threadIdx.x >> 6), kh = wv >> 2, r = lane & 31, hh = lane >> 5;
  float (*mrg)[2][13][64] = mrg_[wv & 3];
  const int hd = (blockIdx.x & 7) * 4 + ((blockIdx.x >> 3) & 3), qb = blockIdx.x >> 5;
  const int q0 = qb * 256 + (wv & 3) * 64;
  const __amdgpu_buffer_rsrc_t rk = RSRC(Kp, BH * S_ * 64), rv = RSRC(VbT, BH * S_ * 64);
  unsigned l16 = lane * 16;
  const unsigned so0 = (unsigned)(hd * S_ + kh * KQ) * 64u;
  u32x4 kf[4][2], vf[4][2], qf[2][2];
  kf[0][0] = LDP16(rk, l16, so0); kf[0][1] = LDP16(rk, l16 + 1024, so0);
#pragma unroll
  for (int qt = 0; qt < 2; ++qt)
#pragma unroll
    for (int ks = 0; ks < 2; ++ks) qf[qt][ks] = *(const u32x4*)(Qp + ((size_t)hd * S_ + q0 + qt * 32) * 32 + ks * 512 + lane * 8);
#pragma unroll
  for (int u = 0; u < 3; ++u) {
    vf[u][0] = LDP16(rv, l16, so0 + u * 2048u); vf[u][1] = LDP16(rv, l16 + 1024, so0 + u * 2048u);
    kf[u + 1][0] = LDP16(rk, l16, so0 + (u + 1) * 2048u); kf[u + 1][1] = LDP16(rk, l16 + 1024, so0 + (u + 1) * 2048u);
  }
  f32x16 Z;
#pragma unroll
  for (int i = 0; i < 16; ++i) Z[i] = 0.f;
  f32x16 o[2] = {Z, Z}, sa[2];
  float m[2];
#pragma unroll
  for (int qt = 0; qt < 2; ++qt) {
    const float mn = max16(qk32(kf[0][1], qf[qt][1], qk32(kf[0][0], qf[qt][0], Z))), hi = rbf(mn), lo = rbf(mn - hi);
    m[qt] = hi + lo;
    if (hh == 0) qf[qt][1][2] = pack2(-hi, -lo);
  }
  sa[0] = qk32(kf[0][1], qf[0][1], qk32(kf[0][0], qf[0][0], Z));
  sa[1] = Z; vf[3][1] = (u32x4){0u, 0u, 0u, 0u};
  {
    unsigned so = so0 + 6144u;
    vf[3][0] = vf[3][1];
    asm volatile("s_nop 11\n1:\n\t"
      "s_nop 1\n\t" "v_smfmac_f32_32x32x32_bf16 v[24:39], v[148:151], v[64:71], %[ix]\n\t"
      "buffer_load_dwordx4 v[144:147], %[l16], %[rv], %[so] offen offset:0\n\t"
      "v_exp_f32 v40, v40\n\t" "v_exp_f32 v41, v41\n\t" "v_exp_f32 v42, v42\n\t" "v_exp_f32 v43, v43\n\t"
      "v_mfma_f32_32x32x16_bf16 v[56:71], v[88:91], v[80:83], 0\n\t"
      "buffer_load_dwordx4 v[148:151], %[l16], %[rv], %[so] offen offset:1024\n\t"
      "v_exp_f32 v44, v44\n\t" "v_exp_f32 v45, v45\n\t" "v_exp_f32 v46, v46\n\t" "v_exp_f32 v47, v47\n\t"
      "v_mfma_f32_32x32x16_bf16 v[56:71], v[92:95], v[84:87], v[56:71]\n\t"
      "buffer_load_dwordx4 v[88:91], %[l16], %[rk], %[so] offen offset:2048\n\t"
      "v_exp_f32 v48, v48\n\t" "v_exp_f32 v49, v49\n\t" "v_exp_f32 v50, v50\n\t" "v_exp_f32 v51, v51\n\t"
      "s_waitcnt vmcnt(13)\n\t" "v_smfmac_f32_32x32x32_bf16 v[8:23], v[96:99], v[40:47], %[ix]\n\t"
      "buffer_load_dwordx4 v[92:95], %[l16], %[rk], %[so] offen offset:3072\n\t"
      "v_exp_f32 v52, v52\n\t" "v_exp_f32 v53, v53\n\t" "v_exp_f32 v54, v54\n\t" "v_exp_f32 v55, v55\n\t"
      "s_add_u32 %[so], %[so], 0x800\n\t" "s_waitcnt vmcnt(12)\n\t" "v_smfmac_f32_32x32x32_bf16 v[8:23], v[100:103], v[48:55], %[ix]\n\t"
      "v_exp_f32 v56, v56\n\t" "v_exp_f32 v57, v57\n\t" "v_exp_f32 v58, v58\n\t" "v_exp_f32 v59, v59\n\t"
      "v_mfma_f32_32x32x16_bf16 v[40:55], v[104:107], v[72:75], 0\n\t"
      "v_exp_f32 v60, v60\n\t" "v_exp_f32 v61, v61\n\t" "v_exp_f32 v62, v62\n\t" "v_exp_f32 v63, v63\n\t"
      "v_mfma_f32_32x32x16_bf16 v[40:55], v[108:111], v[76:79], v[40:55]\n\t"
      "v_exp_f32 v64, v64\n\t" "v_exp_f32 v65, v65\n\t" "v_exp_f32 v66, v66\n\t" "v_exp_f32 v67, v67\n\t"
      "v_smfmac_f32_32x32x32_bf16 v[24:39], v[96:99], v[56:63], %[ix]\n\t"
      "v_exp_f32 v68, v68\n\t" "v_exp_f32 v69, v69\n\t" "v_exp_f32 v70, v70\n\t" "v_exp_f32 v71, v71\n\t"
      "s_nop 1\n\t" "v_smfmac_f32_32x32x32_bf16 v[24:39], v[100:103], v[64:71], %[ix]\n\t"
      "buffer_load_dwordx4 v[96:99], %[l16], %[rv], %[so] offen offset:0\n\t"
      "v_exp_f32 v40, v40\n\t" "v_exp_f32 v41, v41\n\t" "v_exp_f32 v42, v42\n\t" "v_exp_f32 v43, v43\n\t"
      "v_mfma_f32_32x32x16_bf16 v[56:71], v[104:107], v[80:83], 0\n\t"
      "buffer_load_dwordx4 v[100:103], %[l16], %[rv], %[so] offen offset:1024\n\t"
      "v_exp_f32 v44, v44\n\t" "v_exp_f32 v45, v45\n\t" "v_exp_f32 v46, v46\n\t" "v_exp_f32 v47, v47\n\t"
      "v_mfma_f32_32x32x16_bf16 v[56:71], v[108:111], v[84:87], v[56:71]\n\t"
      "buffer_load_dwordx4 v[104:107], %[l16], %[rk], %[so] offen offset:2048\n\t"
      "v_exp_f32 v48, v48\n\t" "v_exp_f32 v49, v49\n\t" "v_exp_f32 v50, v50\n\t" "v_exp_f32 v51, v51\n\t"
      "s_waitcnt vmcnt(13)\n\t" "v_smfmac_f32_32x32x32_bf16 v[8:23], v[112:115], v[40:47], %[ix]\n\t"
      "buffer_load_dwordx4 v[108:111], %[l16], %[rk], %[so] offen offset:3072\n\t"
      "v_exp_f32 v52, v52\n\t" "v_exp_f32 v53, v53\n\t" "v_exp_f32 v54, v54\n\t" "v_exp_f32 v55, v55\n\t"
      "s_add_u32 %[so], %[so], 0x800\n\t" "s_waitcnt vmcnt(12)\n\t" "v_smfmac_f32_32x32x32_bf16 v[8:23], v[116:119], v[48:55], %[ix]\n\t"
      "v_exp_f32 v56, v56\n\t" "v_exp_f32 v57, v57\n\t" "v_exp_f32 v58, v58\n\t" "v_exp_f32 v59, v59\n\t"
      "v_mfma_f32_32x32x16_bf16 v[40:55], v[120:123], v[72:75], 0\n\t"
      "v_exp_f32 v60, v60\n\t" "v_exp_f32 v61, v61\n\t" "v_exp_f32 v62, v62\n\t" "v_exp_f32 v63, v63\n\t"
      "v_mfma_f32_32x32x16_bf16 v[40:55], v[124:127], v[76:79], v[40:55]\n\t"
      "v_exp_f32 v64, v64\n\t" "v_exp_f32 v65, v65\n\t" "v_exp_f32 v66, v66\n\t" "v_exp_f32 v67, v67\n\t"
      "v_smfmac_f32_32x32x32_bf16 v[24:39], v[112:115], v[56:63], %[ix]\n\t"
      "v_exp_f32 v68, v68\n\t" "v_exp_f32 v69, v69\n\t" "v_exp_f32 v70, v70\n\t" "v_exp_f32 v71, v71\n\t"
      "s_nop 1\n\t" "v_smfmac_f32_32x32x32_bf16 v[24:39], v[116:119], v[64:71], %[ix]\n\t"
      "buffer_load_dwordx4 v[112:115], %[l16], %[rv], %[so] offen offset:0\n\t"
      "v_exp_f32 v40, v40\n\t" "v_exp_f32 v41, v41\n\t" "v_exp_f32 v42, v42\n\t" "v_exp_f32 v43, v43\n\t"
      "v_mfma_f32_32x32x16_bf16 v[56:71], v[120:123], v[80:83], 0\n\t"
      "buffer_load_dwordx4 v[116:119], %[l16], %[rv], %[so] offen offset:1024\n\t"
      "v_exp_f32 v44, v44\n\t" "v_exp_f32 v45, v45\n\t" "v_exp_f32 v46, v46\n\t" "v_exp_f32 v47, v47\n\t"
      "v_mfma_f32_32x32x16_bf16 v[56:71], v[124:127], v[84:87], v[56:71]\n\t"
      "buffer_load_dwordx4 v[120:123], %[l16], %[rk], %[so] offen offset:2048\n\t"
      "v_exp_f32 v48, v48\n\t" "v_exp_f32 v49, v49\n\t" "v_exp_f32 v50, v50\n\t" "v_exp_f32 v51, v51\n\t"
      "s_waitcnt vmcnt(13)\n\t" "v_smfmac_f32_32x32x32_bf16 v[8:23], v[128:131], v[40:47], %[ix]\n\t"
      "buffer_load_dwordx4 v[124:127], %[l16], %[rk], %[so] offen offset:3072\n\t"
      "v_exp_f32 v52, v52\n\t" "v_exp_f32 v53, v53\n\t" "v_exp_f32 v54, v54\n\t" "v_exp_f32 v55, v55\n\t"
      "s_add_u32 %[so], %[so], 0x800\n\t" "s_waitcnt vmcnt(12)\n\t" "v_smfmac_f32_32x32x32_bf16 v[8:23], v[132:135], v[48:55], %[ix]\n\t"
      "v_exp_f32 v56, v56\n\t" "v_exp_f32 v57, v57\n\t" "v_exp_f32 v58, v58\n\t" "v_exp_f32 v59, v59\n\t"
      "v_mfma_f32_32x32x16_bf16 v[40:55], v[136:139], v[72:75], 0\n\t"
      "v_exp_f32 v60, v60\n\t" "v_exp_f32 v61, v61\n\t" "v_exp_f32 v62, v62\n\t" "v_exp_f32 v63, v63\n\t"
      "v_mfma_f32_32x32x16_bf16 v[40:55], v[140:143], v[76:79], v[40:55]\n\t"
      "v_exp_f32 v64, v64\n\t" "v_exp_f32 v65, v65\n\t" "v_exp_f32 v66, v66\n\t" "v_exp_f32 v67, v67\n\t"
      "v_smfmac_f32_32x32x32_bf16 v[24:39], v[128:131], v[56:63], %[ix]\n\t"
      "v_exp_f32 v68, v68\n\t" "v_exp_f32 v69, v69\n\t" "v_exp_f32 v70, v70\n\t" "v_exp_f32 v71, v71\n\t"
      "s_nop 1\n\t" "v_smfmac_f32_32x32x32_bf16 v[24:39], v[132:135], v[64:71], %[ix]\n\t"
      "buffer_load_dwordx4 v[128:131], %[l16], %[rv], %[so] offen offset:0\n\t"
      "v_exp_f32 v40, v40\n\t" "v_exp_f32 v41, v41\n\t" "v_exp_f32 v42, v42\n\t" "v_exp_f32 v43, v43\n\t"
      "v_mfma_f32_32x32x16_bf16 v[56:71], v[136:139], v[80:83], 0\n\t"
      "buffer_load_dwordx4 v[132:135], %[l16], %[rv], %[so] offen offset:1024\n\t"
      "v_exp_f32 v44, v44\n\t" "v_exp_f32 v45, v45\n\t" "v_exp_f32 v46, v46\n\t" "v_exp_f32 v47, v47\n\t"
      "v_mfma_f32_32x32x16_bf16 v[56:71], v[140:143], v[84:87], v[56:71]\n\t"
      "buffer_load_dwordx4 v[136:139], %[l16], %[rk], %[so] offen offset:2048\n\t"
      "v_exp_f32 v48, v48\n\t" "v_exp_f32 v49, v49\n\t" "v_exp_f32 v50, v50\n\t" "v_exp_f32 v51, v51\n\t"
      "s_waitcnt vmcnt(13)\n\t" "v_smfmac_f32_32x32x32_bf16 v[8:23], v[144:147], v[40:47], %[ix]\n\t"
      "buffer_load_dwordx4 v[140:143], %[l16], %[rk], %[so] offen offset:3072\n\t"
      "v_exp_f32 v52, v52\n\t" "v_exp_f32 v53, v53\n\t" "v_exp_f32 v54, v54\n\t" "v_exp_f32 v55, v55\n\t"
      "s_add_u32 %[so], %[so], 0x800\n\t" "s_waitcnt vmcnt(12)\n\t" "v_smfmac_f32_32x32x32_bf16 v[8:23], v[148:151], v[48:55], %[ix]\n\t"
      "v_exp_f32 v56, v56\n\t" "v_exp_f32 v57, v57\n\t" "v_exp_f32 v58, v58\n\t" "v_exp_f32 v59, v59\n\t"
      "v_mfma_f32_32x32x16_bf16 v[40:55], v[88:91], v[72:75], 0\n\t"
      "v_exp_f32 v60, v60\n\t" "v_exp_f32 v61, v61\n\t" "v_exp_f32 v62, v62\n\t" "v_exp_f32 v63, v63\n\t"
      "v_mfma_f32_32x32x16_bf16 v[40:55], v[92:95], v[76:79], v[40:55]\n\t"
      "v_exp_f32 v64, v64\n\t" "v_exp_f32 v65, v65\n\t" "v_exp_f32 v66, v66\n\t" "v_exp_f32 v67, v67\n\t"
      "v_smfmac_f32_32x32x32_bf16 v[24:39], v[144:147], v[56:63], %[ix]\n\t"
      "v_exp_f32 v68, v68\n\t" "v_exp_f32 v69, v69\n\t" "v_exp_f32 v70, v70\n\t" "v_exp_f32 v71, v71\n\t"
      "s_cmp_lt_u32 %[so], %[se]\n\ts_cbranch_scc1 1b\n\ts_waitcnt vmcnt(0)\n\t" "v_smfmac_f32_32x32x32_bf16 v[24:39], v[148:151], v[64:71], %[ix]\n\t" "s_nop 11"
      : "+{v[8:23]}"(o[0]), "+{v[24:39]}"(o[1]), "+{v[40:55]}"(sa[0]), "+{v[56:71]}"(sa[1]),
        "+{v[88:91]}"(kf[0][0]), "+{v[96:99]}"(vf[0][0]), "+{v[92:95]}"(kf[0][1]), "+{v[100:103]}"(vf[0][1]),
        "+{v[104:107]}"(kf[1][0]), "+{v[112:115]}"(vf[1][0]), "+{v[108:111]}"(kf[1][1]), "+{v[116:119]}"(vf[1][1]),
        "+{v[120:123]}"(kf[2][0]), "+{v[128:131]}"(vf[2][0]), "+{v[124:127]}"(kf[2][1]), "+{v[132:135]}"(vf[2][1]),
        "+{v[136:139]}"(kf[3][0]), "+{v[144:147]}"(vf[3][0]), "+{v[140:143]}"(kf[3][1]), "+{v[148:151]}"(vf[3][1]), [so] "+s"(so)
      : "{v[72:75]}"(qf[0][0]), "{v[76:79]}"(qf[0][1]), "{v[80:83]}"(qf[1][0]), "{v[84:87]}"(qf[1][1]), [l16] "v"(l16), [rk] "s"(rk), [rv] "s"(rv), [se] "s"(so0 + KQ * 64u + 6144u), [ix] "v"(0xDDDD) : "scc", "memory");
  }
  float big = 0.f;
#pragma unroll
  for (int i = 0; i < 12; ++i) big = fmx(fmx(big, __builtin_fabsf(o[0][i])), __builtin_fabsf(o[1][i]));
  if (__builtin_expect(__any(!(big < 1.0e37f) || (hh == 1 && !(o[0][8] < 9.0e18f && o[1][8] < 9.0e18f))), 0)) {
#pragma unroll
    for (int qt = 0; qt < 2; ++qt) {
      o[qt] = Z;
#pragma unroll 1
      for (unsigned so = so0; so < so0 + KQ * 64u; so += 2048u) {
        f32x16 x = qk32(LDP16(rk, l16 + 1024, so), qf[qt][1], qk32(LDP16(rk, l16, so), qf[qt][0], Z));
        const float mn = m[qt] + fmx(max16(x), 0.f), hi = rbf(mn), lo = rbf(mn - hi), me = hi + lo;
        const float delta = me - m[qt], alpha = __builtin_amdgcn_exp2f(-delta);
        m[qt] = me;
        if (hh == 0) qf[qt][1][2] = pack2(-hi, -lo);
#pragma unroll
        for (int i = 0; i < 16; ++i) { x[i] = __builtin_amdgcn_exp2f(x[i] - delta); o[qt][i] *= alpha; }
        o[qt] = pv32<1>(LDP16(rv, l16 + 1024, so), x, pv32<0>(LDP16(rv, l16, so), x, o[qt]));
      }
    }
  }
#pragma unroll
  for (int qt = 0; qt < 2; ++qt)
    if (kh != qt) {
#pragma unroll
      for (int i = 0; i < 12; ++i) mrg[kh][qt][i][lane] = o[qt][i];
      mrg[kh][qt][12][lane] = m[qt];
    }
  __syncthreads();
  if (kh < 2) {
    float mo = kh ? m[1] : m[0], mk[NKS - 1], mn = mo, v[12];
#pragma unroll
    for (int k = 0; k < NKS - 1; ++k) { mk[k] = mrg[k + (k >= kh)][kh][12][lane]; mn = fmaxf(mn, mk[k]); }
    const float a0 = __builtin_amdgcn_exp2f(mo - mn);
#pragma unroll
    for (int i = 0; i < 12; ++i) v[i] = (kh ? o[1][i] : o[0][i]) * a0;
#pragma unroll
    for (int k = 0; k < NKS - 1; ++k) {
      const float ak = __builtin_amdgcn_exp2f(mk[k] - mn);
#pragma unroll
      for (int i = 0; i < 12; ++i) v[i] += mrg[k + (k >= kh)][kh][i][lane] * ak;
    }
    auto lr = __builtin_amdgcn_permlane32_swap(f2u(v[8]), f2u(v[8]), false, false);
    const float inv = __builtin_amdgcn_rcpf(u2f(lr[1]));
    u16* op = O + (size_t)(q0 + kh * 32 + r) * DM + hd * BD + 4 * hh;
    *(u32x2*)(op) = (u32x2){pack2(v[0] * inv, v[1] * inv), pack2(v[2] * inv, v[3] * inv)};
    *(u32x2*)(op + 8) = (u32x2){pack2(v[4] * inv, v[5] * inv), pack2(v[6] * inv, v[7] * inv)};
    if (hh == 0) *(u32x2*)(op + 16) = (u32x2){pack2(v[8] * inv, v[9] * inv), pack2(v[10] * inv, v[11] * inv)};
  }
}

#define ATTB_LAUNCH(q, k, v, o, st) attb_kernel<<<BH * 8, 256 * NKS, 0, st>>>(q, k, v, o)
#define OUT_IDX(t, n) ((size_t)(t) * DM + (n))

#define P_TAIL 540
#define P_D0 100
#define P_G2C 700
#define P_G2T 240
#define P_DUR0 970
#define P_TOL 60
#define P_SLOW 900
extern "C" void kernel_launch(void* const* d_in, const int* in_sizes, int n_in, void* d_out, int out_size, void* d_ws,
                              size_t ws_size, hipStream_t stream) {
  const u16* A_x = (const u16*)d_in[0];
  const u16* A_mem0 = (const u16*)d_in[1];
  const u16* A_Wq = (const u16*)d_in[2];
  const u16* A_Wk = (const u16*)d_in[3];
  const u16* A_Wv = (const u16*)d_in[4];
  const u16* A_Wo = (const u16*)d_in[5];
  const u16* B_wqkv = (const u16*)d_in[8];
  const u16* B_wo = (const u16*)d_in[9];
  char* ws = (char*)(((uintptr_t)d_ws + ((size_t)2 << 20) - 1) & ~(uintptr_t)(((size_t)2 << 20) - 1));
  u16* WF = (u16*)(ws + OFF_WF); u16* UF = (u16*)(ws + OFF_UF);
  u16* WqkvBT = (u16*)(ws + OFF_WQKVBT); u16* WoBT = (u16*)(ws + OFF_WOBT);
  u16* xf = (u16*)(ws + OFF_XF); u16* xn = (u16*)(ws + OFF_XN); u16* xa = (u16*)(ws + OFF_XA); u16* xb = (u16*)(ws + OFF_XB);
  float* part = (float*)(ws + OFF_PART);
  u16* Qb = (u16*)(ws + OFF_QB); u16* Kb = (u16*)(ws + OFF_KB); u16* VbT = (u16*)(ws + OFF_VBT); u16* Ob = (u16*)(ws + OFF_OB);
  {
    ProArgs pa;
    TrJobs& jb = pa.jobs;
    int t0 = 0;
    auto add = [&](int i, const u16* s, u16* d, int R, int C, int frag) { jb.j[i].src = s; jb.j[i].dst = d; jb.j[i].R = R; jb.j[i].C = C; jb.j[i].tile0 = t0; jb.j[i].frag = frag; t0 += (R / 64) * (C / 64); };
    add(0, A_Wq, WF, DM, HD, 1);
    add(1, A_Wk, WF + (size_t)HD * DM, DM, HD, 1);
    add(2, B_wqkv, WqkvBT, DM, 3 * DM, 1);
    add(3, B_wo, WoBT, DM, DM, 0);
    pa.x = A_x; pa.xf = xf; pa.qb = Qb; pa.sync = ws; pa.wo = A_Wo; pa.wv = A_Wv; pa.uf = UF; pa.mem0 = A_mem0;
    pa.pad = 0; pa.nu = NH * 8;
    pa.ntr = t0; pa.npad = (BH * 64 * 2 * 24 + 255) / 256; pa.nsync = (int)(2 * SYNC_LAYER / 16384); pa.nln = S_ / 4;
    prologue_kernel<<<32 + pa.nu + pa.ntr + pa.npad + pa.nsync + pa.nln, 256, 0, stream>>>(pa);
  }
  const u16* xcur = A_x;
  for (int layer = 0; layer < 2; ++layer) {
    {
      RecArgs r{WF, UF, xf, ws + OFF_QK, ws + OFF_Y, ws + OFF_Z, ws + OFF_PT, ws + (size_t)layer * SYNC_LAYER, ws + OFS_CAL + ROW, xcur, xa, xn, WqkvBT, Qb, nullptr,
                layer, P_TAIL, P_D0, P_G2C, P_G2T, P_DUR0, P_TOL, P_SLOW};
      rec_kernel<<<256, 256, 0, stream>>>(r);
    }
    ATTB_LAUNCH(Qb, Kb, VbT, Ob, stream);
    {
      GemmArgs g{Ob, WoBT, part, nullptr, nullptr, DM, DM, DM / 2, DM / 64, S_ / 128, (S_ / 128) * (DM / 64) * 2 / 8};
      gemm_kernel<EPI_PART, 64><<<(S_ / 128) * (DM / 64) * 2, 256, 0, stream>>>(g);
    }
    if (layer == 0) {
      resln_kernel<<<S_ / 4, 256, 0, stream>>>(xa, part, 2, xb, xf, 1);
      xcur = xb;
    } else {
      resln_kernel<<<S_ / 4, 256, 0, stream>>>(xa, part, 2, (u16*)d_out, nullptr, 0);
    }
  }
}
```

```cpp
#include <hip/hip_runtime.h>
#include <hip/hip_bf16.h>
#include <stdint.h>
#include <type_traits>
#include <utility>

typedef unsigned short u16;
typedef unsigned long long u64;
typedef __attribute__((ext_vector_type(8))) short bf16x8;
typedef __attribute__((ext_vector_type(4))) short bf16x4;
typedef __attribute__((ext_vector_type(4))) float f32x4;
typedef __attribute__((ext_vector_type(2))) float f32x2;
typedef __attribute__((ext_vector_type(16))) float f32x16;
typedef __attribute__((ext_vector_type(4))) unsigned u32x4;
typedef __attribute__((ext_vector_type(2))) unsigned u32x2;
#define GAS __attribute__((address_space(1)))
#define LAS __attribute__((address_space(3)))

constexpr int S_ = 2048, DM = 640;
constexpr int NH = 32, DH = 192, HD = NH * DH;
constexpr int MEM = 8, SEG = 32, NSEG = S_ / SEG;
constexpr int BH = 32, BD = 20;

constexpr size_t al256(size_t x) { return (x + 255) & ~(size_t)255; }
constexpr unsigned ROW = 32768;
constexpr unsigned M_SLOT = 4 * ROW;
constexpr unsigned OFS_P = 16 * ROW;
constexpr unsigned P_SLOT = 5 * ROW;
constexpr unsigned OFS_CAL = 26 * ROW;
constexpr unsigned OFS_CW = 28 * ROW;
constexpr unsigned CW_JP = 0;
constexpr unsigned CW_SS = CW_JP + 104 * 128;
constexpr unsigned CW_SO = CW_SS + 64 * 128;
constexpr unsigned CW_TQ = CW_SO + 512 * 128;
constexpr unsigned CW_TC = CW_TQ + 8 * 128;
constexpr unsigned CW_END = CW_TC + 64 * 128;
static_assert(CW_END <= 4 * ROW, "control words");
constexpr unsigned SYNC_LAYER = 32 * ROW;
constexpr size_t OFF_WF = 2 * (size_t)SYNC_LAYER;
constexpr size_t OFF_UF = OFF_WF + (size_t)768 * 20480;
constexpr size_t OFF_WQKVBT = OFF_UF + (size_t)80 * 16 * 20480;
constexpr size_t OFF_WOBT = OFF_WQKVBT + al256((size_t)3 * DM * DM * 2);
constexpr size_t OFF_XF = OFF_WOBT + al256((size_t)DM * DM * 2);
constexpr size_t OFF_XN = OFF_XF + al256((size_t)S_ * DM * 2);
constexpr size_t OFF_XA = OFF_XN + al256((size_t)S_ * DM * 2);
constexpr size_t OFF_XB = OFF_XA + al256((size_t)S_ * DM * 2);
constexpr unsigned QK_IMG = NSEG * NH * 12 * 1024;
constexpr size_t OFF_QK = OFF_XB + al256((size_t)S_ * DM * 2);
constexpr unsigned Y_IMG = NSEG * NH * 40 * 1024;
constexpr size_t OFF_Y = OFF_QK + 2 * (size_t)QK_IMG;
constexpr unsigned Z_IMG = NSEG * 40 * NH * 256;
constexpr size_t OFF_Z = OFF_Y + Y_IMG;
constexpr unsigned PT_M = NH * 2048, PT_SEG = PT_M + 2 * NH * 256, PT_IMG = NSEG * PT_SEG;
constexpr size_t OFF_PT = OFF_Z + Z_IMG;
constexpr size_t OFF_QB = OFF_PT + PT_IMG;
constexpr size_t OFF_KB = OFF_QB + al256((size_t)BH * S_ * 32 * 2);
constexpr size_t OFF_VBT = OFF_KB + al256((size_t)BH * S_ * 32 * 2);
constexpr size_t WS_TOTAL = OFF_VBT + al256((size_t)BH * S_ * 32 * 2);
constexpr size_t OFF_PART = OFF_Y;
constexpr size_t OFF_OB = OFF_QK;
static_assert(WS_TOTAL + ((size_t)2 << 20) <= ((size_t)256 << 20), "workspace budget");

__device__ __forceinline__ float u2f(unsigned v) { return __builtin_bit_cast(float, v); }
__device__ __forceinline__ unsigned f2u(float v) { return __builtin_bit_cast(unsigned, v); }
__device__ __forceinline__ float bf2f(unsigned v) { return __builtin_bit_cast(float, v << 16); }
__device__ __forceinline__ unsigned f2bf(float f) { __bf16 b = (__bf16)f; return (unsigned)__builtin_bit_cast(u16, b); }
__device__ __forceinline__ float rbf(float f) { return bf2f(f2bf(f)); }
typedef __attribute__((ext_vector_type(2))) __bf16 hbf16x2;
__device__ __forceinline__ unsigned pack2(float lo, float hi) { f32x2 v = {lo, hi}; hbf16x2 b = __builtin_convertvector(v, hbf16x2); return __builtin_bit_cast(unsigned, b); }
__device__ __forceinline__ f32x4 mfma32(bf16x8 a, bf16x8 b, f32x4 c) { return __builtin_amdgcn_mfma_f32_16x16x32_bf16(a, b, c, 0, 0, 0); }
__device__ __forceinline__ f32x4 mfma16(bf16x4 a, bf16x4 b, f32x4 c) { return __builtin_amdgcn_mfma_f32_16x16x16bf16_1k(a, b, c, 0, 0, 0); }
typedef __attribute__((ext_vector_type(8))) unsigned u32x8;
typedef __attribute__((ext_vector_type(8))) __bf16 hbf16x8;
typedef __attribute__((ext_vector_type(16))) __bf16 hbf16x16;
template <typename R>
__device__ __forceinline__ void store16_(u32x4 v, R r, unsigned vo, unsigned so, bool sc1) {
  if (sc1) __builtin_amdgcn_raw_buffer_store_b128(v, r, vo, so, 16); else __builtin_amdgcn_raw_buffer_store_b128(v, r, vo, so, 0);
  asm volatile("s_nop 1" :: "v"(v));
}
#define store16(v, r, vo, so, aux) store16_((v), (r), (vo), (so), (aux) != 0)
__device__ __forceinline__ bf16x4 mk4(unsigned lo, unsigned hi) { u32x2 v = {lo, hi}; return __builtin_bit_cast(bf16x4, v); }
__device__ __forceinline__ bf16x8 mk8(unsigned a, unsigned b, unsigned c, unsigned d) { u32x4 v = {a, b, c, d}; return __builtin_bit_cast(bf16x8, v); }
__device__ __forceinline__ unsigned dpp_ror8(unsigned v) { return (unsigned)__builtin_amdgcn_update_dpp(0, (int)v, 0x128, 0xf, 0xf, false); }

__device__ __forceinline__ bf16x4 lds_tr(const u16* p) { return __builtin_amdgcn_ds_read_tr16_b64_v4i16((LAS bf16x4*)p); }
__device__ __forceinline__ bf16x8 cat8(bf16x4 a, bf16x4 b) { return __builtin_shufflevector(a, b, 0, 1, 2, 3, 4, 5, 6, 7); }
__device__ __forceinline__ unsigned rtc() { return (unsigned)__builtin_amdgcn_s_memrealtime(); }
__device__ __forceinline__ unsigned mtc() { return (unsigned)__builtin_amdgcn_s_memtime(); }
constexpr unsigned OOB = 0x80000000u;
constexpr unsigned FAR = 0x40000000u;
constexpr unsigned SENT = 0xffffffffu;
#define WG_BARRIER() do { asm volatile("s_waitcnt lgkmcnt(0)" ::: "memory"); __builtin_amdgcn_s_barrier(); asm volatile("" ::: "memory"); } while (0)
struct ProArgs { const u16* x; const u16* mem0; const u16* wq; const u16* wk; const u16* wv; const u16* wo; const u16* wb; const u16* wob;
                 u16* wf; u16* uf; u16* wbf; u16* wobf; u16* xf; u16* qb; char* sync; };
#define CAL_N 4
#define PRO_NT 1
#define PRO_ROT 0
#define PRO_LD(m, p) (((m) & PRO_NT) ? __builtin_nontemporal_load((const u32x4*)(p)) : *(const u32x4*)(p))
constexpr int PA_LD = 200, PO_LD = 168, PT_LD = 136;
__global__ __launch_bounds__(704, 3) void prologue_kernel(ProArgs a) {
  __shared__ __attribute__((aligned(16))) u16 smem[192 * PO_LD + 320 * PT_LD];
  static_assert(10 * 16 * PA_LD <= 320 * PT_LD, "staging area");
  const int b = blockIdx.x, tid = threadIdx.x, lane = tid & 63, w = __builtin_amdgcn_readfirstlane(tid >> 6), c = lane & 15, g = lane >> 4;
  const __amdgpu_buffer_rsrc_t rs = __builtin_amdgcn_make_buffer_rsrc(a.sync, 0, (int)(2 * SYNC_LAYER), 0x00020000);
  if (w == 10) {
    const unsigned ln = OFS_CAL + ((b >> 3) & 7) * 4096u + ((b >> 6) & 1) * 2048u + ((b & 7) * 2 + (b >> 7)) * 128u;
    const u32x4 z = {0u, 0u, 0u, 0u};
    store16(z, rs, lane < 8 ? ln + lane * 16u : OOB, 0, 16);
    WG_BARRIER(); WG_BARRIER();
    unsigned best = 65535u;
#pragma unroll 1
    for (int r = 0; r < CAL_N; ++r) {
      unsigned t0, t1, x;
      asm volatile("s_waitcnt vmcnt(0)\n\ts_sleep 8\n\ts_memtime vcc\n\ts_waitcnt lgkmcnt(0)\n\ts_mov_b32 %0, vcc_lo\n\tbuffer_load_dword %2, %3, %4, 0 offen sc1\n\ts_waitcnt vmcnt(0)\n\t"
                   "s_memtime vcc\n\ts_waitcnt lgkmcnt(0)\n\ts_mov_b32 %1, vcc_lo" : "=&s"(t0), "=&s"(t1), "=&v"(x) : "v"(ln), "s"(rs) : "vcc", "memory");
      best = min(best, t1 - t0);
      if (r + 1 < CAL_N) store16(z, rs, lane < 8 ? ln + lane * 16u : OOB, 0, 16);
    }
    unsigned* tab = (unsigned*)(a.sync + OFS_CAL + ROW);
    if (lane == 0) { tab[b] = best; if (b < 8) tab[256 + b] = (unsigned)__builtin_amdgcn_s_getreg(20 | (3 << 11)) & 7u; }
    return;
  }
#define PT_(k)
  PT_(0);
  const int h = (b & 7) * 4 + (b >> 6), dh = (b >> 5) & 1, cg = (b >> 3) & 3;
  const int wk = PRO_ROT ? (w + (5 * cg >> 1)) % 10 : w, to = PRO_ROT ? tid + 320 * dh - (tid + 320 * dh >= 640 ? 640 : 0) : tid;
  u32x4 la[12], lo[6], lt[8], lx[2];
  const int tb = b < 96 ? b : b < 192 ? b - 96 : b < 222 ? b - 192 : b - 222, tld = b < 192 ? HD : b < 222 ? 3 * DM : DM;
  const u16* tsrc = (b < 96 ? a.wq : b < 192 ? a.wk : b < 222 ? a.wb : a.wob) + (size_t)(tb & 1) * 320 * tld + (tb >> 1) * 128;
  u16* tdst = (b < 96 ? a.wf : b < 192 ? a.wf + (size_t)HD * DM : b < 222 ? a.wbf : a.wobf) + (size_t)(tb >> 1) * 81920 + (tb & 1) * 5120;
  if (b < 232) {
#pragma unroll
    for (int i = 0; i < 8; ++i) lt[i] = PRO_LD(1, tsrc + (size_t)(i * 40 + (tid >> 4)) * tld + (tid & 15) * 8);
  }
#pragma unroll
  for (int i = 0; i < 6; ++i) { const int q = (to >> 6) * 384 + i * 64 + lane, r = q / 20; lo[i] = PRO_LD(2, a.wo + (size_t)(h * DH + r) * DM + 160 * cg + (q - r * 20) * 8); }
  const int row = b * 8 + (w & 7);
  if (w < 8) { lx[0] = *(const u32x4*)(a.x + (size_t)row * DM + lane * 8); lx[1] = *(const u32x4*)(a.x + (size_t)row * DM + 512 + (lane & 15) * 8); }
#pragma unroll
  for (int i = 0; i < 12; ++i) {
    const int q = (i % 6) * 64 + lane, r = q / 24;
    la[i] = PRO_LD(2, a.wv + (size_t)(320 * dh + 32 * wk + 8 * (r >> 2) + (r & 3) + 4 * (i / 6)) * HD + h * DH + (q - r * 24) * 8);
  }
  __builtin_amdgcn_sched_barrier(0);
  if (tid < 512) {
    const unsigned off = (unsigned)b * 8192u + tid * 16u, o = off & (SYNC_LAYER - 1), rr = o >> 15, in = o & 4095u;
    u32x4 z = {SENT, SENT, SENT, SENT};
    if (o >= OFS_CW) z = (u32x4){0u, 0u, 0u, 0u};
    else if (rr < 4 && (!(rr & 1) || in < 1024u)) {
      const int L = 5 * (rr >> 1) + 4 * (rr & 1) + (in >> 10), ln = (in & 1023u) >> 4;
      const unsigned* mr = (const unsigned*)(a.mem0 + (ln & 7) * DM + 64 * L + 16 * (ln >> 4) + 2 * ((ln >> 3) & 1));
      z = (u32x4){min(mr[0], SENT - 1), min(mr[2], SENT - 1), min(mr[4], SENT - 1), min(mr[6], SENT - 1)};
    }
    store16(z, rs, (off >= OFS_CAL && off < OFS_CW) ? OOB : off, 0, 16);
  }
  {
    u16* kb = a.qb + (size_t)BH * S_ * 32; u16* vb = kb + (size_t)BH * S_ * 32;
    if (tid < 256) {
      const size_t o = (size_t)(b * 256 + (tid & ~31)) * 32 + 512 + (tid & 31) * 8;
      *(u32x2*)(a.qb + o + 4) = (u32x2){0u, 0u}; *(u32x4*)(a.qb + o + 256) = (u32x4){0u, 0u, 0u, 0u};
      *(u32x2*)(kb + o + 4) = (u32x2){0x3f803f80u, 0u}; *(u32x4*)(kb + o + 256) = (u32x4){0u, 0u, 0u, 0u};
    } else {
      const int q = tid - 256, hb = q / 12, e = 20 + q - hb * 12;
      const unsigned f = e == 20 ? 0x3f803f80u : 0u;
      *(u32x4*)(vb + ((size_t)(b * 32 + hb) * 32 + e) * 8) = (u32x4){f, f, f, f};
    }
  }
  u16* st = smem + 192 * PO_LD;
  if (b < 232) {
#pragma unroll
    for (int i = 0; i < 8; ++i) *(u32x4*)(st + (i * 40 + (tid >> 4)) * PT_LD + (tid & 15) * 8) = lt[i];
  }
  PT_(1);
  WG_BARRIER();
  if (b < 232) {
#pragma unroll
    for (int t = 0; t < 8; ++t) {
      const u16* p = st + (32 * w + 8 * g + (c >> 2)) * PT_LD + 16 * t + 4 * (c & 3);
      *(bf16x8*)(tdst + (t * 20 + w) * 512 + lane * 8) = cat8(lds_tr(p), lds_tr(p + 4 * PT_LD));
    }
  }
#pragma unroll
  for (int i = 0; i < 6; ++i) {
    const int q = (to >> 6) * 384 + i * 64 + lane, r = q / 20, ch = q - r * 20;
    u16* p = smem + r * PO_LD + (ch >> 1) * 16 + (ch & 1) * 4;
    *(u32x2*)p = (u32x2){lo[i][0], lo[i][2]}; *(u32x2*)(p + 8) = (u32x2){lo[i][1], lo[i][3]};
  }
  if (w < 8) {
    const bool two = lane < 16;
    float v[16], sm = 0.f, q = 0.f;
#pragma unroll
    for (int i = 0; i < 8; ++i) { const unsigned u = (i < 4 || two) ? lx[i >> 2][i & 3] : 0u; v[2 * i] = bf2f(u & 0xffff); v[2 * i + 1] = bf2f(u >> 16); sm += v[2 * i] + v[2 * i + 1]; }
#pragma unroll
    for (int o = 32; o >= 1; o >>= 1) sm += __shfl_xor(sm, o);
    const float mean = sm * (1.0f / DM);
#pragma unroll
    for (int i = 0; i < 16; ++i) { const float d = (i < 8 || two) ? v[i] - mean : 0.f; q += d * d; }
#pragma unroll
    for (int o = 32; o >= 1; o >>= 1) q += __shfl_xor(q, o);
    const float mb = rbf(mean), den = rbf(sqrtf(rbf(rbf(q * (1.0f / DM)) + 1e-5f)));
#pragma unroll
    for (int hf = 0; hf < 2; ++hf)
      if (hf == 0 || two) {
        u32x4 o;
#pragma unroll
        for (int i = 0; i < 4; ++i) o[i] = pack2(rbf(v[hf * 8 + 2 * i] - mb) / den, rbf(v[hf * 8 + 2 * i + 1] - mb) / den);
        *(u32x4*)(a.xf + (size_t)(row >> 4) * 10240 + (hf * 64 + lane) * 128 + (row & 15) * 8) = o;
      }
  }
  bf16x8 af[2][6];
#pragma unroll
  for (int hf = 0; hf < 2; ++hf) {
    u16* pa = st + 32 * w * PT_LD;
    asm volatile("s_waitcnt lgkmcnt(0)" ::: "memory");
#pragma unroll
    for (int i = 0; i < 6; ++i) { const int q = i * 64 + lane, r = q / 24; *(u32x4*)(pa + r * PA_LD + (q - r * 24) * 8) = la[6 * hf + i]; }
    asm volatile("s_waitcnt lgkmcnt(0)" ::: "memory");
#pragma unroll
    for (int k = 0; k < 6; ++k) af[hf][k] = *(const bf16x8*)(pa + c * PA_LD + k * 32 + g * 8);
  }
  WG_BARRIER();
  PT_(2);
  u16* dst = a.uf + ((size_t)((20 * cg + (c >> 3)) * 16 + (h >> 1)) * 20 + 10 * dh + wk) * 512 + (g * 16 + (h & 1) * 8 + (c & 7)) * 8;
  const u16* bp = smem + (8 * g + (c >> 2)) * PO_LD + 4 * (c & 3);
#pragma unroll 2
  for (int t = 0; t < 10; ++t) {
    f32x4 d0 = {0.f, 0.f, 0.f, 0.f}, d1 = {0.f, 0.f, 0.f, 0.f};
#pragma unroll
    for (int kp = 0; kp < 6; ++kp) {
      const bf16x8 bw = cat8(lds_tr(bp + 32 * kp * PO_LD + 16 * t), lds_tr(bp + (32 * kp + 4) * PO_LD + 16 * t));
      d0 = mfma32(af[0][kp], bw, d0); d1 = mfma32(af[1][kp], bw, d1);
    }
    *(u32x4*)(dst + (size_t)t * 2 * 16 * 20 * 512) = (u32x4){pack2(d0[0], d0[1]), pack2(d0[2], d0[3]), pack2(d1[0], d1[1]), pack2(d1[2], d1[3])};
  }
  PT_(3);
  PT_(4);
}

struct RecArgs {
  const u16* wf; const u16* uf; const u16* xf; char* qk; char* y; char* z; char* pt; char* sync; const char* cal;
  const u16* xold; u16* xr; const u16* wb; u16* qkvb; unsigned long long* dbg;
  int layer, tail, d0, g2c, g2t, dur0, tol, slow;
};
template <typename F, int... I> __device__ __forceinline__ void static_for_(F&& f, std::integer_sequence<int, I...>) { (f(std::integral_constant<int, I>()), ...); }
template <int N, typename F> __device__ __forceinline__ void static_for(F&& f) { static_for_(f, std::make_integer_sequence<int, N>()); }
#define RLX_AGENT __ATOMIC_RELAXED, __HIP_MEMORY_SCOPE_AGENT
#define SPIN_LOG 20
constexpr unsigned SPIN_CAP = 1u << SPIN_LOG;
#define PIN_A(x) asm volatile("" : "+a"(x))
#define PIN_V(x) asm volatile("" : "+v"(x))
#define PIN_S(x) do { (x) = __builtin_amdgcn_readfirstlane(x); } while (0)
#define SB() __builtin_amdgcn_sched_barrier(0)
#define NOPS(n) do { SB(); asm volatile("s_nop %0" :: "n"(n)); SB(); } while (0)
#define RFL(x) ((unsigned)__builtin_amdgcn_readfirstlane(x))
template <int CTRL> __device__ __forceinline__ unsigned dppu(unsigned v) { return (unsigned)__builtin_amdgcn_update_dpp(0, (int)v, CTRL, 0xf, 0xf, true); }
template <int CTRL> __device__ __forceinline__ float dppf(float v) { return u2f(dppu<CTRL>(f2u(v))); }
template <int BANKS> __device__ __forceinline__ unsigned ror8_banks(unsigned v) { return (unsigned)__builtin_amdgcn_update_dpp((int)v, (int)v, 0x128, 0xf, BANKS, false); }
constexpr int ROR1 = 0x121, ROR2 = 0x122, ROR4 = 0x124, ROR8 = 0x128, SHR8 = 0x118;
__device__ __forceinline__ float fmx(float a, float b) { return __builtin_elementwise_maximum(a, b); }
__device__ __forceinline__ unsigned mx3(unsigned a, unsigned b, unsigned c) { return max(max(a, b), c); }
__device__ __forceinline__ unsigned mx4(u32x4 v) { return max(mx3(v[0], v[1], v[2]), v[3]); }
__device__ __forceinline__ u32x8 cat2(u32x4 lo, u32x4 hi) { return __builtin_shufflevector(lo, hi, 0, 1, 2, 3, 4, 5, 6, 7); }
__device__ __forceinline__ u32x4 lo4(u32x8 v) { return __builtin_shufflevector(v, v, 0, 1, 2, 3); }
__device__ __forceinline__ u32x4 hi4(u32x8 v) { return __builtin_shufflevector(v, v, 4, 5, 6, 7); }
__device__ __forceinline__ u32x2 lo2(u32x4 v) { return __builtin_shufflevector(v, v, 0, 1); }
__device__ __forceinline__ u32x2 hi2(u32x4 v) { return __builtin_shufflevector(v, v, 2, 3); }
__device__ __forceinline__ u32x4 pack8(f32x4 a, f32x4 b) { return (u32x4){pack2(a[0], a[1]), pack2(a[2], a[3]), pack2(b[0], b[1]), pack2(b[2], b[3])}; }
#define DEV __device__ __forceinline__
DEV void smf_v(f32x4& acc, u32x4 a, u32x8 b, int idx) { asm volatile("v_smfmac_f32_16x16x64_bf16 %0, %1, %2, %3" : "+v"(acc) : "v"(a), "v"(b), "v"(idx)); }
template <int R> DEV void smf_a(f32x4& acc, u32x4 a, int idx) { asm volatile("v_smfmac_f32_16x16x64_bf16 %0, %1, a[%c3:%c4], %2" : "+v"(acc) : "v"(a), "v"(idx), "n"(R), "n"(R + 7)); }
template <int R> DEV void mf32_ba(f32x4& acc, u32x4 a) { asm volatile("v_mfma_f32_16x16x32_bf16 %0, %1, a[%c2:%c3], %0" : "+v"(acc) : "v"(a), "n"(R), "n"(R + 3)); }
template <int R> DEV void mf32z_ba(f32x4& d, u32x4 a) { asm volatile("v_mfma_f32_16x16x32_bf16 %0, %1, a[%c2:%c3], 0" : "=&v"(d) : "v"(a), "n"(R), "n"(R + 3)); }
template <int R> DEV void mf32_aa(f32x4& acc, u32x4 b) { asm volatile("v_mfma_f32_16x16x32_bf16 %0, a[%c2:%c3], %1, %0" : "+v"(acc) : "v"(b), "n"(R), "n"(R + 3)); }
template <int R> DEV void mf32z_aa(f32x4& d, u32x4 b) { asm volatile("v_mfma_f32_16x16x32_bf16 %0, a[%c2:%c3], %1, 0" : "=&v"(d) : "v"(b), "n"(R), "n"(R + 3)); }
template <int R> DEV void ld_a(__amdgpu_buffer_rsrc_t r, unsigned vo, unsigned so) { asm volatile("buffer_load_dwordx4 a[%c3:%c4], %0, %1, %2 offen" :: "v"(vo), "s"(r), "s"(so), "n"(R), "n"(R + 3) : "memory"); }
template <int R> DEV void ld_a1(__amdgpu_buffer_rsrc_t r, unsigned vo, unsigned so) { asm volatile("buffer_load_dwordx4 a[%c3:%c4], %0, %1, %2 offen sc1" :: "v"(vo), "s"(r), "s"(so), "n"(R), "n"(R + 3) : "memory"); }
DEV void mf32(f32x4& acc, u32x4 a, u32x4 b) { asm volatile("v_mfma_f32_16x16x32_bf16 %0, %1, %2, %0" : "+v"(acc) : "v"(a), "v"(b)); }
DEV void mf32z(f32x4& d, u32x4 a, u32x4 b) { asm volatile("v_mfma_f32_16x16x32_bf16 %0, %1, %2, 0" : "=&v"(d) : "v"(a), "v"(b)); }
DEV void mf32_n(f32x4& acc, u32x4 a, u32x4 b) { asm volatile("s_nop 1\n\tv_mfma_f32_16x16x32_bf16 %0, %1, %2, %0" : "+v"(acc) : "v"(a), "v"(b)); }
DEV void mf32z_n(f32x4& d, u32x4 a, u32x4 b) { asm volatile("s_nop 1\n\tv_mfma_f32_16x16x32_bf16 %0, %1, %2, 0" : "=&v"(d) : "v"(a), "v"(b)); }
DEV void mf16(f32x4& acc, u32x2 a, u32x2 b) { asm volatile("v_mfma_f32_16x16x16_bf16 %0, %1, %2, %0" : "+v"(acc) : "v"(a), "v"(b)); }
DEV void mf16z(f32x4& d, u32x2 a, u32x2 b) { asm volatile("v_mfma_f32_16x16x16_bf16 %0, %1, %2, 0" : "=&v"(d) : "v"(a), "v"(b)); }
DEV void zero4(f32x4& d) { f32x2 x, y; asm volatile("v_mov_b64 %0, 0\n\tv_mov_b64 %1, 0" : "=v"(x), "=v"(y)); d = __builtin_shufflevector(x, y, 0, 1, 2, 3); asm volatile("" : "+v"(d)); }
DEV void clock_now(u64& t) { asm volatile("s_memtime %0" : "=s"(t)); }
DEV void zero2(f32x2& x) { asm volatile("v_mov_b64 %0, 0" : "=v"(x)); }
DEV int fresh_lane() { int l; asm volatile("v_mbcnt_lo_u32_b32 %0, -1, 0\n\tv_mbcnt_hi_u32_b32 %0, -1, %0" : "=v"(l)); __builtin_assume(l >= 0 && l < 64); return l; }
DEV void keep_v(unsigned x) { asm volatile("" :: "v"(x)); }
DEV void pin_s(unsigned& x) { asm volatile("" : "+s"(x)); }
template <typename T> DEV void pin_v(T& x) { asm volatile("" : "+v"(x)); }
#define AGPR_FENCE() asm volatile("" ::: \
    "a0", "a1", "a2", "a3", "a4", "a5", "a6", "a7", "a8", "a9", "a10", "a11", "a12", "a13", "a14", "a15", "a16", "a17", "a18", "a19", "a20", "a21", "a22", \
    "a23", "a24", "a25", "a26", "a27", "a28", "a29", "a30", "a31", "a32", "a33", "a34", "a35", "a36", "a37", "a38", "a39", "a40", "a41", "a42", "a43", \
    "a44", "a45", "a46", "a47", "a48", "a49", "a50", "a51", "a52", "a53", "a54", "a55", "a56", "a57", "a58", "a59", "a60", "a61", "a62", "a63", "a64", \
    "a65", "a66", "a67", "a68", "a69", "a70", "a71", "a72", "a73", "a74", "a75", "a76", "a77", "a78", "a79", "a80", "a81", "a82", "a83", "a84", "a85", \
    "a86", "a87", "a88", "a89", "a90", "a91", "a92", "a93", "a94", "a95", "a96", "a97", "a98", "a99", "a100", "a101", "a102", "a103", "a104", "a105", \
    "a106", "a107", "a108", "a109", "a110", "a111", "a112", "a113", "a114", "a115", "a116", "a117", "a118", "a119", "a120", "a121", "a122", "a123", \
    "a124", "a125", "a126", "a127", "a128", "a129", "a130", "a131", "a132", "a133", "a134", "a135", "a136", "a137", "a138", "a139", "a140", "a141", \
    "a142", "a143", "a144", "a145", "a146", "a147", "a148", "a149", "a150", "a151", "a152", "a153", "a154", "a155", "a156", "a157", "a158", "a159", \
    "a160", "a161", "a162", "a163", "a164", "a165", "a166", "a167", "a168", "a169", "a170", "a171", "a172", "a173", "a174", "a175", "a176", "a177", \
    "a178", "a179", "a180", "a181", "a182", "a183", "a184", "a185", "a186", "a187", "a188", "a189", "a190", "a191", "a192", "a193", "a194", "a195", \
    "a196", "a197", "a198", "a199", "a200", "a201", "a202", "a203", "a204", "a205", "a206", "a207", "a208", "a209", "a210", "a211", "a212", "a213", \
    "a214", "a215", "a216", "a217", "a218", "a219", "a220", "a221", "a222", "a223", "a224", "a225", "a226", "a227", "a228", "a229", "a230", "a231", \
    "a232", "a233", "a234", "a235", "a236", "a237", "a238", "a239", "a240", "a241", "a242", "a243", "a244", "a245", "a246", "a247", "a248", "a249", \
    "a250", "a251", "a252", "a253", "a254", "a255")
#define LDP16(r, off, so) __builtin_amdgcn_raw_buffer_load_b128((r), (off), (so), 0)
#define VM_WAIT(n) asm volatile("s_waitcnt vmcnt(%0)" :: "n"(n) : "memory")
#define LD16(r, off, so) __builtin_amdgcn_raw_buffer_load_b128((r), (off), (so), 16)
#define LD8(r, off, so) __builtin_amdgcn_raw_buffer_load_b64((r), (off), (so), 16)
#define LD4(r, off, so) __builtin_amdgcn_raw_buffer_load_b32((r), (off), (so), 16)
#define ST16(v, r, off, so) store16((v), (r), (off), (so), 16)
#define RSRC(p, n) __builtin_amdgcn_make_buffer_rsrc((void*)(p), 0, (int)(n), 0x00020000)
#define LDS16(off) (*(LAS u32x4*)(lds + (off)))
template <int IMM> DEV void dma1k(__amdgpu_buffer_rsrc_t r, unsigned vo, unsigned so, unsigned ldsoff) {
  asm volatile("s_mov_b32 m0, %0\n\ts_nop 0\n\tbuffer_load_dwordx4 %1, %2, %3 offen offset:%c4 lds" :: "s"(ldsoff), "v"(vo), "s"(r), "s"(so), "n"(IMM) : "memory");
}

DEV int s_clamp(int v, int lim) { int r; asm("s_min_i32 %0, %1, %2\n\ts_max_i32 %0, %0, %3" : "=&s"(r) : "s"(v), "n"(lim), "n"(-lim) : "scc"); return r; }
DEV unsigned s_mulshr16(unsigned x, unsigned y) { unsigned r; asm("s_mul_i32 %0, %1, %2\n\ts_lshr_b32 %0, %0, 16" : "=s"(r) : "s"(x), "s"(y) : "scc"); return r; }
struct Clock {
  unsigned me, re, dm, dt, rho, sig;
  float fm, ft, fx, fy;
  DEV unsigned edge() {
    u64 m, r0, r1, r2, r3, r4, r5, r6, r7; unsigned n;
#define CNT_(k) "s_cmp_eq_u64 %" #k ", %1\n\ts_addc_u32 %9, %9, 0\n\t"
    asm volatile("s_memtime %0\n\ts_memrealtime %1\n\ts_nop 2\n\ts_memrealtime %2\n\ts_nop 2\n\ts_memrealtime %3\n\ts_nop 2\n\ts_memrealtime %4\n\ts_nop 2\n\t"
                 "s_memrealtime %5\n\ts_nop 2\n\ts_memrealtime %6\n\ts_nop 2\n\ts_memrealtime %7\n\ts_nop 2\n\ts_memrealtime %8\n\t"
                 "s_mov_b32 %9, 0\n\ts_waitcnt lgkmcnt(0)\n\t" CNT_(2) CNT_(3) CNT_(4) CNT_(5) CNT_(6) CNT_(7) CNT_(8)
                 : "=&s"(m), "=&s"(r0), "=&s"(r1), "=&s"(r2), "=&s"(r3), "=&s"(r4), "=&s"(r5), "=&s"(r6), "=&s"(r7), "=&s"(n) :: "scc", "memory");
    take(m, r0, n);
    return (unsigned)m;
  }
  DEV void take(u64 m, u64 r0, unsigned n) {
    const unsigned m1 = (unsigned)m + 16u * n + 12u, t1 = ((unsigned)r0 + 4u) * 8u;
    dm = m1 - me; dt = t1 - re; me = m1; re = t1;
  }
  DEV void edge_z(f32x4 (&z)[4], u32x4 x0, u32x4 x1, u32x8 u0, u32x8 u1, int idx) {
    u64 m, r0, r1, r2, r3, r4, r5, r6, r7; unsigned n;
#define EZ_(acc, x, b) "v_smfmac_f32_16x16x64_bf16 %" #acc ", %" #x ", " b ", %18\n\t"
    asm volatile(EZ_(10, 14, "a[64:71]") "s_memtime %0\n\ts_memrealtime %1\n\t" EZ_(11, 14, "a[144:151]") "s_nop 0\n\ts_memrealtime %2\n\t" EZ_(12, 14, "a[224:231]") "s_nop 0\n\ts_memrealtime %3\n\t"
                 EZ_(13, 14, "%16") "s_nop 0\n\ts_memrealtime %4\n\t" EZ_(10, 15, "a[72:79]") "s_nop 0\n\ts_memrealtime %5\n\t" EZ_(11, 15, "a[152:159]") "s_nop 0\n\ts_memrealtime %6\n\t"
                 EZ_(12, 15, "a[232:239]") "s_nop 0\n\ts_memrealtime %7\n\t" EZ_(13, 15, "%17") "s_nop 0\n\ts_memrealtime %8\n\t"
                 "s_mov_b32 %9, 0\n\ts_waitcnt lgkmcnt(0)\n\t" CNT_(2) CNT_(3) CNT_(4) CNT_(5) CNT_(6) CNT_(7) CNT_(8)
                 : "=&s"(m), "=&s"(r0), "=&s"(r1), "=&s"(r2), "=&s"(r3), "=&s"(r4), "=&s"(r5), "=&s"(r6), "=&s"(r7), "=&s"(n), "+v"(z[0]), "+v"(z[1]), "+v"(z[2]), "+v"(z[3])
                 : "v"(x0), "v"(x1), "v"(u0), "v"(u1), "v"(idx) : "scc", "memory");
    take(m, r0, n);
  }
  template <int K> DEV void rate_step() {
    if (K == 0) { fm = (float)dm; pin_v(fm); }
    if (K == 1) { ft = (float)dt; pin_v(ft); }
    if (K == 2) { fy = __builtin_amdgcn_rcpf(fm); pin_v(fy); }
    if (K == 3) { fx = __builtin_amdgcn_rcpf(ft); pin_v(fx); }
    if (K == 4) { fm *= 2048.0f; pin_v(fm); }
    if (K == 5) { ft *= 65536.0f; pin_v(ft); }
    if (K == 6) { fm *= fx; pin_v(fm); }
    if (K == 7) { ft *= fy; pin_v(ft); }
    if (K == 8) { fm = u2f((unsigned)fm); pin_v(fm); }
    if (K == 9) { ft = u2f((unsigned)ft); pin_v(ft); }
    if (K == 10) { dm = RFL(f2u(fm)); asm volatile("" : "+s"(dm)); }
    if (K == 11) { dt = RFL(f2u(ft)); asm volatile("" : "+s"(dt)); }
    if (K == 12) { const bool ok = dm - 12u * 256u < 14u * 256u; rho = ok ? dm : rho; sig = ok ? dt : sig; asm volatile("" : "+s"(rho), "+s"(sig)); }
  }
  template <int K> DEV void rho_step() {
    if (K < 2) rate_step<K>(); else if (K < 4) rate_step<K + 1>(); else if (K < 7) rate_step<2 * K - 2>();
    else { rho = dm - 12u * 256u < 14u * 256u ? dm : rho; asm volatile("" : "+s"(rho)); }
  }
  DEV void rate() { static_for<13>([&](auto K) { rate_step<decltype(K)::value>(); }); }
  DEV void init() { rho = 22u * 256u; sig = 65536u * 8u / 22u; me = 0u; re = 0u; edge(); }
  DEV unsigned local(unsigned t) const { unsigned r = me + (unsigned)((s_clamp((int)(t - re), 100000) * (int)rho) >> 11); PIN_S(r); return r; }
  DEV unsigned global(unsigned m) const { int p = s_clamp((int)(m - me), 40000) * (int)sig; PIN_S(p); p >>= 16; PIN_S(p); return re + (unsigned)p; }
};
__device__ __forceinline__ void wait_until(unsigned tgt) {
  unsigned t;
  asm volatile("1:\n\ts_memtime vcc\n\ts_waitcnt lgkmcnt(0)\n\ts_sub_i32 %0, %1, vcc_lo\n\ts_sub_u32 vcc_hi, %0, 209\n\ts_cmp_lt_u32 vcc_hi, 60000\n\ts_cbranch_scc1 1b\n\t"
               "s_sub_i32 %0, %0, 112\n\ts_max_i32 %0, %0, 0\n\ts_min_i32 %0, %0, 96\n\ts_and_b32 %0, %0, 0x7c\n\t"
               "s_getpc_b64 vcc\n2:\n\ts_sub_u32 %0, 3f-2b, %0\n\ts_add_u32 vcc_lo, vcc_lo, %0\n\ts_addc_u32 vcc_hi, vcc_hi, 0\n\ts_setpc_b64 vcc\n\t"
               ".rept 24\n\ts_nop 0\n\t.endr\n3:" : "=&s"(t) : "s"(tgt) : "vcc", "scc", "memory");
}
__device__ __forceinline__ void coarse_until(unsigned tgt) {
  for (;;) { const unsigned rem = tgt - mtc(); if (rem - 1u >= 60000u) break; }
}
#define DUR_UP 6
#define DUR_DN 2
DEV int max32(int d) {
  d = max(d, (int)dppu<ROR8>(d)); d = max(d, (int)dppu<ROR4>(d)); d = max(d, (int)dppu<ROR2>(d)); d = max(d, (int)dppu<ROR1>(d));
  return max(__builtin_amdgcn_readlane(d, 0), __builtin_amdgcn_readlane(d, 16));
}
struct Sched {
  unsigned T, tP, dur, gap, more;
  DEV void init(unsigned t, const RecArgs& a) { T = t; tP = t; dur = a.dur0; gap = 0u; more = 0u; }
  DEV unsigned on_time() const { return tP + gap; }
  DEV unsigned limit(const RecArgs& a) const { return T + dur + a.tol; }
  DEV void next(u32x2 an, const RecArgs& a, bool start) {
    const int dm = max32((int)(an[0] - T)), gm = max32((int)an[1]), over = dm - (int)dur;
    const bool late = start || over > a.tol;
    more = late ? more + 150u : 0u;
    T = late ? T + (unsigned)dm + a.slow + more : tP + gap;
    int dd = late ? (start ? 0 : DUR_UP) : max(over, over >> DUR_DN);
    PIN_S(dd);
    dur += (unsigned)dd; gap = (unsigned)gm;
    tP = T + dur + a.d0;
    PIN_S(T); PIN_S(tP); PIN_S(dur); PIN_S(more); PIN_S(gap);
  }
};
template <typename R>
__device__ __forceinline__ u32x2 ann_valid(u32x2 an, R rs, unsigned vo, unsigned so, bool& dead) {
  if (__builtin_expect(__any(max(an[0], an[1]) == SENT), 0)) {
    for (unsigned spins = 0; !dead;) {
      __builtin_amdgcn_s_sleep(3);
      const bool need = max(an[0], an[1]) == SENT;
      const u32x2 n = LD8(rs, need ? vo : OOB, so);
      if (need) an = n;
      if (!__any(max(an[0], an[1]) == SENT)) break;
      if (++spins > SPIN_CAP) dead = true;
      asm volatile("" ::: "memory");
    }
  }
  return an;
}
template <typename R>
__device__ __forceinline__ void need_progress(R rs, unsigned vo, unsigned want, unsigned& have) {
  for (unsigned spins = 0; have < want && spins < SPIN_CAP; ++spins) {
    unsigned v = LD4(rs, vo, 0);
    v = min(v, dppu<0x4E>(v)); v = min(v, dppu<0xB1>(v));
    have = RFL(v);
    if (have < want) __builtin_amdgcn_s_sleep(8);
    asm volatile("" ::: "memory");
  }
}
struct Softmax {
  f32x4 s; float m, x;
  DEV void a() {
    float t;
    x = fmx(fmx(s[0], s[1]), s[2]); pin_v(x); x = fmx(fmx(x, s[3]), m);
    asm volatile("s_nop 1\n\tv_max_f32_dpp %0, %0, %0 row_ror:8 row_mask:0xf bank_mask:0xf\n\ts_nop 1\n\tv_max_f32_dpp %0, %0, %0 row_ror:4 row_mask:0xf bank_mask:0xf\n\t"
                 "v_mov_b32 %1, %0\n\ts_nop 1\n\tv_permlane32_swap_b32 %0, %1\n\tv_max_f32 %0, %0, %1\n\tv_mul_f32 %0, 0xbdd53b94, %0" : "+v"(x), "=&v"(t));
  }
  DEV void b() {
    const float C = 0.07216878364870322f * 1.4426950408889634f;
    s = __builtin_elementwise_fma(s, (f32x4){C, C, C, C}, (f32x4){x, x, x, x}); m = __builtin_fmaf(m, C, x);
#pragma unroll
    for (int i = 0; i < 4; ++i) s[i] = __builtin_amdgcn_exp2f(s[i]);
    m = __builtin_amdgcn_exp2f(m);
    pin_v(s); pin_v(m);
  }
  DEV void c() {
    float t;
    const f32x2 u = (f32x2){s[0], s[1]} + (f32x2){s[2], s[3]};
    x = (u[0] + m) + u[1];
    asm volatile("s_nop 1\n\tv_add_f32_dpp %0, %0, %0 row_ror:8 row_mask:0xf bank_mask:0xf\n\ts_nop 1\n\tv_add_f32_dpp %0, %0, %0 row_ror:4 row_mask:0xf bank_mask:0xf\n\t"
                 "v_mov_b32 %1, %0\n\ts_nop 1\n\tv_permlane32_swap_b32 %0, %1\n\tv_add_f32 %0, %0, %1\n\tv_rcp_f32 %0, %0" : "+v"(x), "=&v"(t));
  }
  DEV u32x4 d() {
    unsigned o0, o1, o2, o3; float t, u;
    asm volatile("s_nop 0\n\tv_mul_f32 %4, %10, %11\n\tv_mul_f32 %5, %10, %11\n\tv_mul_f32 %0, %6, %11\n\tv_mul_f32 %2, %7, %11\n\tv_permlane32_swap_b32 %4, %5\n\t"
                 "v_mul_f32 %1, %8, %11\n\tv_mul_f32 %3, %9, %11\n\tv_cvt_pk_bf16_f32 %0, %0, |%2|\n\tv_cvt_pk_bf16_f32 %1, %1, |%3|\n\tv_cvt_pk_bf16_f32 %4, %4, |%5|\n\t"
                 "v_mov_b32_dpp %2, %0 row_ror:8 row_mask:0xf bank_mask:0xf\n\tv_mov_b32_dpp %3, %1 row_ror:8 row_mask:0xf bank_mask:0xf\n\t"
                 "v_mov_b32_dpp %0, %4 row_shr:8 row_mask:0xf bank_mask:0x4\n\tv_mov_b32_dpp %1, %4 row_shr:4 row_mask:0xf bank_mask:0x4\n\t"
                 "v_mov_b32_dpp %2, %4 quad_perm:[0,1,2,3] row_mask:0xf bank_mask:0x4\n\tv_mov_b32_dpp %3, %4 row_shl:4 row_mask:0xf bank_mask:0x4"
                 : "=&v"(o0), "=&v"(o1), "=&v"(o2), "=&v"(o3), "=&v"(t), "=&v"(u) : "v"(s[0]), "v"(s[1]), "v"(s[2]), "v"(s[3]), "v"(m), "v"(x));
    return (u32x4){o0, o1, o2, o3};
  }
};
#define TMARK(k)
#define STAMP(k)
#define STAMP_DECL(sh)
#define STAMP_FLUSH(s, ref, x0, x1)
#define STAMP_PUT(s)
#define STAMP_DUMP()
#define DBG_LDS 0

#define AN_AT 980u
#define LATE_PAD 480u
#define HEAD 1u
#define S_FINE 0
#define PFQK 0
#define EX_UP 16u
#define SM_W 2
constexpr int S_EX = 0, S_ET = S_EX + 4 * 2048, S_CT = S_ET + 4 * 5120, S_LW = S_CT + 48, S_NL = 13, S_END = S_LW + 4 * S_NL * 2048;
__host__ __device__ constexpr int s_q(int n) { return n < 30 ? n / 6 : n < 50 ? 5 + (n - 30) / 4 : 5 + (n - 50) / 2; }
__host__ __device__ constexpr int s_t(int n) { return n < 30 ? n % 6 : n < 50 ? (n - 30) % 4 : 4 + (n & 1); }
__host__ __device__ constexpr bool s_lds(int n) { return n % 4 == 3 && n / 4 < S_NL; }
__host__ __device__ constexpr int s_ri(int n) { return n - ((n + 1) / 4 < S_NL ? (n + 1) / 4 : S_NL); }
template <int kind>
__device__ __forceinline__ void score_role(const RecArgs& a, LAS char* lds, int rep, int il, unsigned posM, unsigned posP) {
  const int lane = fresh_lane(), w = __builtin_amdgcn_readfirstlane(threadIdx.x >> 6), c = lane & 15, g = lane >> 4, hf = c >> 3, i8 = c & 7;
  const int h = rep * 4 + il, iod = rep >> 2;
  const __amdgpu_buffer_rsrc_t rs = RSRC(a.sync, SYNC_LAYER), rq = RSRC(a.qk, 2 * QK_IMG), rp = RSRC(a.pt, PT_IMG);
  Clock ck; ck.init();
  u32x8 WV[28 - S_NL];
  {
    const __amdgpu_buffer_rsrc_t rw = RSRC(a.wf, 768 * 20480);
    const unsigned vo = lane * 16, wb = (unsigned)(h * 12 + 3 * w) * 20480u;
    static_for<60>([&](auto N) {
      constexpr int n = decltype(N)::value, q = s_q(n), t = s_t(n), ri = s_ri(n);
      const unsigned so = wb + (unsigned)((t & 1) * 384 + (t >> 1)) * 20480u + (unsigned)(q < 5 ? q + 5 * iod : q - 5 * iod) * 2048u;
      if (s_lds(n)) { LDS16(S_LW + (w * S_NL + n / 4) * 2048 + lane * 16) = LDP16(rw, vo, so); LDS16(S_LW + (w * S_NL + n / 4) * 2048 + 1024 + lane * 16) = LDP16(rw, vo + 1024, so); }
      else if (ri >= 32) WV[ri < 32 ? 0 : ri - 32] = cat2(LDP16(rw, vo, so), LDP16(rw, vo + 1024, so));
    });
    SB();
    static_for<60>([&](auto N) {
      constexpr int n = decltype(N)::value, q = s_q(n), t = s_t(n), ri = s_ri(n);
      const unsigned so = wb + (unsigned)((t & 1) * 384 + (t >> 1)) * 20480u + (unsigned)(q < 5 ? q + 5 * iod : q - 5 * iod) * 2048u;
      if (!s_lds(n) && ri < 32) { ld_a<8 * (ri & 31)>(rw, vo, so); ld_a<8 * (ri & 31) + 4>(rw, vo, so + 1024u); VM_WAIT(40); }
    });
    VM_WAIT(0);
#pragma unroll
    for (int i = 0; i < 28 - S_NL; ++i) PIN_V(WV[i]);
  }
  const int sidx = hf ? 0xEEEE : 0x4444;
  u32x4 idn;
#pragma unroll
  for (int jj = 0; jj < 4; ++jj) idn[jj] = (((c >> 2) & 1) == (g & 1) && jj == ((c & 3) >> 1) + 2 * hf) ? ((c & 1) ? 0x3f800000u : 0x3f80u) : 0u;
  const unsigned lomask = hf ? 0u : ~0u;
  unsigned l16 = lane * 16u;
  const unsigned an_ld = lane < 32 && lane != h ? ((lane >> 4) * 2 + 1) * ROW + posM * 4096u + 1024u + c * 128u : OOB;
  const unsigned an_st = ((h >> 4) * 2 + 1) * ROW + (lane >> 3) * 4096u + 1024u + (h & 15) * 128u + i8 * 16u;
  const int sr = (lane & 3) | ((lane >> 2) & 4), sg = ((lane >> 2) & 1) | ((lane >> 4) & 2), sk = ((lane >> 1) & 6) | (lane >> 5), cq = 8 * (w & 1) + sr;
  const bool smem_lane = (lane & 44) == 8;
  const unsigned p_st = !hf ? OFS_P + (h >> 3) * ROW + (8 / SM_W) * w * 4096u + ((h >> 1) & 3) * 1024u + sg * 256u + (h & 1) * 128u + sr * 16u
                            : smem_lane ? OFS_P + 4 * ROW + (8 / SM_W) * w * 4096u + h * 128u + sr * 16u : OOB;
  const unsigned pt_st = !hf ? (unsigned)(h * 2048 + (w >> 1) * 1024 + (sg * 16 + cq) * 16) : smem_lane ? PT_M + ((w >> 1) * NH + h) * 256u + cq * 16u : OOB;
  const unsigned lw = S_LW + w * S_NL * 2048 + lane * 16;
  unsigned ex_w = S_EX + w * 2048 + lane * 16; PIN_V(ex_w);
  const unsigned et_w = S_ET + w * 5120 + lane * 16, et_r = S_ET + ((w >> 1) ? 1 + hf : 0) * 1024 + (sg * 16 + cq) * 16,
                 et_m = S_ET + (3 + (w >> 1)) * 1024 + ((2 + (sk >> 2)) * 16 + cq) * 16 + (sk & 3) * 4;
  const unsigned ex_r = S_EX + (sg * 16 + hf * 8 + sr) * 16, ex_m = S_EX + 1024 + ((2 + (sk >> 2)) * 16 + sr) * 16 + (sk & 3) * 4;
  const unsigned jp_vo = lane < 4 ? OFS_CW + CW_JP + (unsigned)((((lane & 1) * 384 + h * 12 + ((lane >> 1) & 1) * 11) / 20) * 128) : OOB;
  const unsigned pf_vo = lane < 48 ? (lane >= 24 ? QK_IMG + (lane - 24) * 128u : lane * 128u) : OOB;
  unsigned jhave = 0;
  u32x4 kf[3], qf[3];
  auto load_qk = [&](unsigned so) {
#pragma unroll
    for (int p = 0; p < 3; ++p) { kf[p] = LD16(rq, l16 + p * 1024, so + QK_IMG); qf[p] = LD16(rq, l16 + p * 1024, so); }
  };
  TMARK(1);
  need_progress(rs, jp_vo, HEAD, jhave);
  TMARK(2);
  unsigned qk_so = (unsigned)(h * 12 + 3 * w) * 1024u;
  load_qk(qk_so);
  bool dead = false;
  unsigned extra = 0;
  Softmax tk; tk.s = (f32x4){0.f, 0.f, 0.f, 0.f}; tk.m = 0.f;
  ck.edge(); ck.rate();
  Sched sd; sd.init(ck.global(mtc()), a);
  if (kind == 2) LDS16(S_CT) = (u32x4){0u, 0u, 1u, 0u};
  unsigned mT = mtc();
  u32x2 an = {0u, 0u}; bool owed = false;
  const unsigned kA = posM * 4096u + iod * (2 * ROW), kB = posM * 4096u + (1 - iod) * (2 * ROW);
  unsigned sl = 0u, soA = kA, soB = kB, soA1 = kA + ROW, soB1 = kB + ROW, sp = 0u, jp_so = 0u, pt_so = FAR, pf_so = FAR;
  f32x4 acc[6];
  u32x4 tl[2], th[2];
  auto prime = [&]() {
    tl[0] = LDS16(lw); th[0] = LDS16(lw + 1024); tl[1] = LDS16(lw + 2048); th[1] = LDS16(lw + 3072);
#pragma unroll
    for (int t = 0; t < 6; ++t) zero4(acc[t]);
    SB();
  };
  prime();
  auto token_parts = [&](f32x4 (&e)[4], float (&em)[4]) {
#pragma unroll
    for (int k = 0; k < 4; ++k) e[k] = __builtin_bit_cast(f32x4, LDS16(et_r + k * 5120));
#pragma unroll
    for (int k = 0; k < 4; ++k) em[k] = *(LAS float*)(lds + et_m + k * 5120);
  };
  auto token_sums = [&](f32x4 (&e)[4], float (&em)[4]) {
    f32x4 es = (e[0] + e[1]) + (e[2] + e[3]);
    PIN_V(es);
#pragma unroll
    for (int r = 0; r < 4; ++r) tk.s[r] = 16 * hf + 4 * sg + r <= 8 * w + sr ? es[r] : -INFINITY;
    const f32x2 u = (f32x2){em[0], em[1]} + (f32x2){em[2], em[3]};
    tk.m = u[0] + u[1];
    pin_v(tk.s); pin_v(tk.m);
  };
  unsigned budget = SPIN_CAP;
  STAMP_DECL(0);
#pragma unroll 1
  for (int s = 0; s < NSEG; ++s) {
    PIN_V(l16);
    f32x4 dx, dm, tr, ts;
    u32x4 cm[10], ta, tc, bq; u32x2 X[3];
#define LDM(q) LD16(rs, l16 + ((q) % 5 < 4 ? ((q) % 5) * 1024 : 0), (q) < 4 ? soA : (q) == 4 ? soA1 : (q) < 9 ? soB : soB1)
#define POS(n) do { constexpr int q_ = s_q(n), t_ = s_t(n), ri_ = s_ri(n); \
      if (s_lds(n)) smf_v(acc[t_], cm[q_], cat2(tl[((n) / 4) & 1], th[((n) / 4) & 1]), sidx); \
      else if (ri_ < 32) smf_a<8 * (ri_ & 31)>(acc[t_], cm[q_], sidx); else smf_v(acc[t_], cm[q_], WV[ri_ < 32 ? 0 : ri_ - 32], sidx); \
      SB(); \
      if ((n) % 4 == 3 && (n) / 4 + 2 < S_NL) tl[((n) / 4) & 1] = LDS16(lw + ((n) / 4 + 2) * 2048); \
      if ((n) % 4 == 0 && (n) > 0 && (n) / 4 + 1 < S_NL) th[((n) / 4 + 1) & 1] = LDS16(lw + ((n) / 4 + 1) * 2048 + 1024); \
      SB(); } while (0)
#define CVT(dst, src, k) do { unsigned v_ = pack2((src)[2 * (k)], (src)[2 * (k) + 1]); PIN_V(v_); dst = v_; SB(); } while (0)
#define B_LO(p, k) do { unsigned v_ = X[p][k] & lomask; PIN_V(v_); bq[k] = v_; SB(); } while (0)
#define B_HI(p, k) do { unsigned v_ = dppu<SHR8>(X[p][k]); PIN_V(v_); bq[2 + (k)] = v_; SB(); } while (0)
    unsigned jpend; u64 tb1 = 0;
    SB();
    wait_until(mT);
    SB();
    STAMP(0);
    cm[0] = LDM(0); SB(); tk.a(); SB();
    cm[1] = LDM(1); SB(); tk.b(); SB();
    cm[2] = LDM(2); SB(); tk.c(); SB();
    cm[3] = LDM(3); SB();
    u32x4 ptk = tk.d(); PIN_V(ptk); SB();
    cm[4] = LDM(4); SB();
    unsigned chk = 0;
    static_for<48>([&](auto N) {
      constexpr int n = decltype(N)::value, q = s_q(n);
      POS(n);
      if (n == 0 && !S_FINE) STAMP(1);
      if constexpr (n % 4 == 2 && 5 + n / 4 < 10) cm[5 + n / 4] = LDM(5 + n / 4);
      SB();
      if (n == 1 || n == 9 || n == 13 || n == 21 || n == 25 || n == 32 || n == 36 || n == 40 || n == 44 || n == 47) { chk = mx3(chk, cm[q][0], cm[q][2]); pin_v(chk); }
      SB();
    });
    POS(48);
    u64 bad = __builtin_amdgcn_ballot_w64(chk == SENT); asm volatile("" : "+s"(bad)); SB();
    POS(49);
    if (__builtin_expect(bad != 0, 0)) {
      *(LAS unsigned*)(lds + S_CT + 8) = (unsigned)s + 1u;
      u64 miss;
      do {
        __builtin_amdgcn_s_sleep(1);
        unsigned m = 0;
#pragma unroll
        for (int q = 5; q < 10; ++q) cm[q] = LDM(q);
#pragma unroll
        for (int q = 5; q < 10; ++q) m = max(m, mx4(cm[q]));
        SB();
#pragma unroll
        for (int q = 0; q < 5; ++q) cm[q] = LDM(q);
#pragma unroll
        for (int q = 0; q < 5; ++q) m = max(m, mx4(cm[q]));
        miss = __builtin_amdgcn_ballot_w64(m == SENT);
        asm volatile("s_sub_u32 %1, %1, 1\n\ts_cselect_b32 %1, 0, %1\n\ts_cselect_b64 %0, 0, %0" : "+s"(miss), "+s"(budget) :: "scc", "memory");
      } while (miss != 0);
      prime();
      NOPS(1);
      static_for<50>([&](auto N) {
        constexpr int n = decltype(N)::value;
        POS(n);
        if constexpr (n % 6 == 5 && n < 30) cm[5 + n / 6] = LDM(5 + n / 6);
        SB();
      });
    }
    if (!S_FINE) STAMP(2);
    CVT(ta[0], acc[0], 0);
    POS(50); asm volatile("" : "+v"(tl[0]), "+v"(th[0])); SB(); CVT(ta[1], acc[0], 1);
    POS(51); __builtin_amdgcn_raw_buffer_store_b128(ptk, rp, pt_st, pt_so, 16); SB(); CVT(ta[2], acc[1], 0);
    POS(52); jpend = LD4(rs, jp_vo, jp_so); SB(); CVT(ta[3], acc[1], 1);
    asm volatile("" :: "v"(ptk));
    POS(53); CVT(tc[0], acc[2], 0);
    mf32z(tr, ta, idn); SB(); CVT(tc[1], acc[2], 1);
    POS(54);
    unsigned junk = 0u;
    if (PFQK) { junk = LD4(rq, pf_vo, pf_so); SB(); }
    CVT(tc[2], acc[3], 0);
    POS(55); CVT(tc[3], acc[3], 1);
    POS(56); CVT(X[0][0], tr, 0);
    mf32z(ts, tc, idn); SB(); CVT(X[0][1], tr, 1);
    POS(57); B_LO(0, 0);
    POS(58); B_LO(0, 1);
    POS(59); B_HI(0, 0);
    mf16z(dm, X[0], X[0]); SB(); B_HI(0, 1); CVT(X[1][0], ts, 0); CVT(X[1][1], ts, 1);
    CVT(ta[0], acc[4], 0); CVT(ta[1], acc[4], 1);
    mf32z(dx, kf[0], bq); SB(); CVT(ta[2], acc[5], 0); CVT(ta[3], acc[5], 1);
    B_LO(1, 0); B_LO(1, 1);
    mf32z(tr, ta, idn); SB(); B_HI(1, 0); B_HI(1, 1);
    mf16(dm, X[1], X[1]); SB();
    mf32(dx, kf[1], bq); SB();
    CVT(X[2][0], tr, 0); CVT(X[2][1], tr, 1); B_LO(2, 0); B_LO(2, 1);
    mf16(dm, X[2], X[2]); SB(); B_HI(2, 0); B_HI(2, 1);
    mf32_n(dx, kf[2], bq); SB();
    if (!S_FINE) STAMP(3);
    NOPS(2);
    LDS16(ex_w + 1024) = __builtin_bit_cast(u32x4, dm);
    NOPS(3);
    LDS16(ex_w) = __builtin_bit_cast(u32x4, dx);
    if constexpr (kind == 0) {
      asm volatile("s_waitcnt lgkmcnt(0)\n\ts_barrier ; softmax waves" ::: "memory");
      if (!S_FINE) STAMP(4);
      f32x4 e[4]; float em[4];
#pragma unroll
      for (int k = 0; k < 4; ++k) e[k] = __builtin_bit_cast(f32x4, LDS16(ex_r + k * 2048));
#pragma unroll
      for (int k = 0; k < 4; ++k) em[k] = *(LAS float*)(lds + ex_m + k * 2048);
      asm volatile("" :: "s"(sp + 4096u), "s"(sp + 8192u), "s"(sp + 12288u));
      SB();
      Softmax sm;
      sm.s = (e[0] + e[1]) + (e[2] + e[3]);
      const f32x2 u = (f32x2){em[0], em[1]} + (f32x2){em[2], em[3]};
      sm.m = u[0] + u[1];
      sm.a(); sm.b(); sm.c();
      const u32x4 v = sm.d();
#pragma unroll
      for (int k = 0; k < 8 / SM_W - 1; ++k) __builtin_amdgcn_raw_buffer_store_b128(v, rs, p_st, sp + k * 4096u, 16);
      ST16(v, rs, p_st, sp + (8 / SM_W - 1) * 4096u);
    } else if constexpr (kind == 1) {
      unsigned fl;
      const unsigned a_gp = a.g2t + s_mulshr16(a.g2c, ck.sig) + extra;
      asm volatile("s_waitcnt lgkmcnt(0)\n\ts_barrier ; clock\n\tds_read_b32 %0, %1" : "=&v"(fl) : "v"((unsigned)(uintptr_t)lds + S_CT + 8u), "s"(a_gp) : "memory");
      const unsigned m0 = ck.edge();
      PIN_V(fl);
      const bool failed = RFL(fl) == (unsigned)s + 1u;
      bq[0] = (ck.re + (unsigned)(((int)(m0 + a.tail - ck.me) * (int)ck.sig) >> 16) + (failed ? LATE_PAD : 0u)) & ~1u;
      bq[1] = a_gp;
      ST16(bq, rs, an_st, sl);
      *(LAS u32x2*)(lds + S_CT + 32) = lo2(bq);
      extra += failed && s > 0 && extra < 240u ? EX_UP + 1u : 0u; PIN_S(extra);
    } else {
      asm volatile("s_waitcnt lgkmcnt(0)\n\ts_barrier\n\ts_memtime %0" : "=s"(tb1) :: "memory");
      __builtin_amdgcn_raw_ptr_buffer_atomic_add_i32(1, rs, l16 << 20, s > 1 ? OFS_CW + CW_SS + (unsigned)(s - 2) * 128u : FAR, 0);
      asm volatile("s_waitcnt lgkmcnt(0)" : "+s"(tb1));
      if (owed) sd.next(an, a, false);
    }
    SB();
    STAMP(5);
    const unsigned sl0 = sl, mT0 = mT;
    {
      f32x4 d[5]; f32x2 zz[12];
      static_for<15>([&](auto N) {
        constexpr int n = decltype(N)::value, t = n / 3, p = n % 3;
        const u32x2 fa = t < 2 ? lo2(kf[p]) : t == 2 ? hi2(kf[p]) : X[p], fb = (t == 0 || t == 3) ? lo2(qf[p]) : hi2(qf[p]);
        if (p == 0) mf16z(d[t], fa, fb); else mf16(d[t], fa, fb);
        SB();
        if (n < 4) { const u32x4 v = LDS16(lw + n * 1024); if (n & 1) th[n >> 1] = v; else tl[n >> 1] = v; }
        if (n >= 5 && p == 2) LDS16(et_w + (t - 1) * 1024) = __builtin_bit_cast(u32x4, d[t > 0 ? t - 1 : 0]);
        if (n == 4) { sl = (sl + M_SLOT) & (4 * M_SLOT - 1); pin_s(sl); }
        if (n == 6) { soA = sl + kA; soB = sl + kB; pin_s(soA); pin_s(soB); }
        if (n == 7) { soA1 = soA + ROW; soB1 = soB + ROW; pin_s(soA1); pin_s(soB1); }
        if (n == 9) { sp = P_SLOT - sp; pt_so = (unsigned)s * PT_SEG; pin_s(sp); pin_s(pt_so); }
        if (n == 10) { qk_so += s + 1 < NSEG ? NH * 12 * 1024u : 0u; pin_s(qk_so); }
        SB();
        if (n < 12) zero2(zz[n < 12 ? n : 0]);
        if (n == 12) keep_v(junk);
        if (n == 12) { jpend = min(jpend, dppu<0x4E>(jpend)); pin_v(jpend); }
        if (n == 13) { jpend = min(jpend, dppu<0xB1>(jpend)); pin_v(jpend); }
        SB();
      });
#pragma unroll
      for (int t = 0; t < 6; ++t) { acc[t] = __builtin_shufflevector(zz[2 * t], zz[2 * t + 1], 0, 1, 2, 3); pin_v(acc[t]); }
      jhave = max(jhave, RFL(jpend));
      jp_so = jhave < min((unsigned)NSEG, (unsigned)s + 6u) ? 0u : FAR; pin_s(jp_so);
      pf_so = jhave > (unsigned)s + 2u ? qk_so + NH * 12 * 1024u : FAR; pin_s(pf_so);
      NOPS(1);
      LDS16(et_w + 4096) = __builtin_bit_cast(u32x4, d[4]);
      SB();
    }
    if (S_FINE) STAMP(1);
    if constexpr (kind == 2) {
      coarse_until((unsigned)tb1 + AN_AT);
      an = LD8(rs, an_ld, sl0);
      SB();
    }
    if constexpr (kind == 1) {
      ck.rate(); extra -= extra != 0u; PIN_S(extra);
      LDS16(S_CT + 16) = (u32x4){ck.me, ck.re, ck.rho, 0u};
    }
    if (__builtin_expect(jhave < min((unsigned)s + 2u, (unsigned)NSEG), 0)) need_progress(rs, jp_vo, min((unsigned)s + 2u, (unsigned)NSEG), jhave);
    STAMP(6);
    WG_BARRIER();
    if (S_FINE) STAMP(2);
    {
      f32x4 e[4]; float em[4]; u32x4 cc; u32x2 own;
      if constexpr (kind == 2) { cc = LDS16(S_CT + 16); own = *(LAS u32x2*)(lds + S_CT + 32); }
      token_parts(e, em);
      SB();
      PIN_V(l16);
      if constexpr (kind < 2) load_qk(qk_so);
      else {
        ck.me = RFL(cc[0]); ck.re = RFL(cc[1]); ck.rho = RFL(cc[2]);
        mT = ck.local(sd.on_time());
        u32x2 pub = {(unsigned)s + 1u, mT}; PIN_V(pub);
        const unsigned lim = sd.limit(a);
        SB();
        if (S_FINE) STAMP(3);
        if (lane == h) an = own;
        const u64 mis = __builtin_amdgcn_ballot_w64(max(an[0], an[1]) == SENT) | (__builtin_amdgcn_ballot_w64((int)(an[0] - lim) > 0) & 0xffffffffull);
        owed = s > 0 && mis == 0;
        if (__builtin_expect(!owed, 0)) {
          an = ann_valid(an, rs, an_ld, sl0, dead);
          sd.next(an, a, s == 0);
          mT = ck.local(sd.T); pub[1] = mT;
        }
        *(LAS u32x2*)(lds + S_CT) = pub;
        SB();
        load_qk(qk_so);
      }
      SB();
      token_sums(e, em);
    }
    SB();
    if (S_FINE) STAMP(4);
    if constexpr (kind < 2) {
      u32x2 v;
      do { v = *(volatile LAS u32x2*)(lds + S_CT); } while (RFL(v[0]) != (unsigned)s + 1u);
      mT = RFL(v[1]);
    }
    STAMP(7);
    STAMP_FLUSH(s, mT0, owed ? sd.on_time() : sd.T, bad != 0);
  }
  tk.a(); tk.b(); tk.c();
  ST16(tk.d(), rp, pt_st, (unsigned)(NSEG - 1) * PT_SEG);
  asm volatile("s_waitcnt vmcnt(0)" ::: "memory");
  WG_BARRIER();
  __builtin_amdgcn_raw_ptr_buffer_atomic_add_i32(1, rs, l16 << 20, w < 2 ? OFS_CW + CW_SS + (unsigned)(NSEG - 2 + w) * 128u : FAR, 0);
  TMARK(3);
  STAMP_DUMP();
  __builtin_amdgcn_endpgm();
}

constexpr int O_M = 0, O_RED = O_M + 10240, O_CT = O_RED + 3072;
#define P_GAP 3
#define B_GAP 15
#define OM_LAG 80u
__device__ __forceinline__ void out_role(const RecArgs& a, LAS char* lds, int rep, int k, unsigned posM, unsigned posP) {
  const int lane = fresh_lane(), w = __builtin_amdgcn_readfirstlane(threadIdx.x >> 6), c = lane & 15, g = lane >> 4, hf = c >> 3, i8 = c & 7;
  const int i = rep * 10 + k, iod = rep >> 2, ct = i >> 1, o = i & 1;
  const __amdgpu_buffer_rsrc_t rs = RSRC(a.sync, SYNC_LAYER), ry = RSRC(a.y, Y_IMG), rz = RSRC(a.z, Z_IMG);
  const unsigned l16 = lane * 16, l20 = l16 << 20;
  Clock ck; ck.init();
  const int t0 = 8 * iod + 2 * w, t2 = 8 * (1 - iod) + 2 * w;
  u32x8 UV[8];
  {
    const __amdgpu_buffer_rsrc_t rw = RSRC(a.uf, 80 * 16 * 20480);
    static_for<40>([&](auto F) {
      constexpr int f = decltype(F)::value, sl = f / 10, L = f % 10;
      const unsigned so = (unsigned)(i * 16 + (sl < 2 ? t0 : t2) + (sl & 1)) * 20480u + L * 2048u;
      if (f < 32) { ld_a<8 * (f & 31)>(rw, l16, so); ld_a<8 * (f & 31) + 4>(rw, l16, so + 1024u); VM_WAIT(40); }
      else UV[f & 7] = cat2(LDP16(rw, l16, so), LDP16(rw, l16 + 1024, so));
    });
    VM_WAIT(0);
#pragma unroll
    for (int f = 0; f < 8; ++f) PIN_V(UV[f]);
  }
  const int sidx = hf ? 0xEEEE : 0x4444;
  const int hq = 2 * ((g < 2 ? t0 : t2) + (g & 1)) + hf;
  const unsigned y_vo = hf * 40960u + g * 256u + o * 128u + i8 * 16u;
  const unsigned z_vo = hq * 256u + o * 128u + i8 * 16u;
  const unsigned p_vo = OFS_P + posP * 4096u + l16, pm_vo = OFS_P + 4 * ROW + posP * 4096u + hq * 128u + i8 * 16u;
  const unsigned pA = (unsigned)(t0 >> 2) * ROW + (t0 & 3) * 1024u, pB = (unsigned)(t2 >> 2) * ROW + (t2 & 3) * 1024u;
  const unsigned m_vo = posM * 4096u + l16;
  const unsigned an_ld = lane < 32 ? ((lane >> 4) * 2 + 1) * ROW + posM * 4096u + 1024u + c * 128u : OOB;
  const int Lm = i >> 3;
  const unsigned m_st = ((Lm / 5) * 2 + (Lm % 5 == 4)) * ROW + (lane >> 3) * 4096u + (Lm % 5 & 3) * 1024u + (i & 7) * 128u + i8 * 16u;
  unsigned mo[4], ml[4], mc[4];
#pragma unroll
  for (int q = 0; q < 4; ++q) {
    const int L = w == 1 ? q : 3 * w - 2 + q;
    const bool skip = (w != 1 && q == 3) || (L == Lm && (lane >> 3) == (i & 7));
    mo[q] = skip ? OOB : m_vo; ml[q] = skip ? O_RED + 2304 + (lane & 31) * 16 : O_M + L * 1024 + lane * 16;
    mc[q] = ((L / 5) * 2 + (L % 5 == 4)) * ROW + (L % 5 & 3) * 1024u;
  }
  const unsigned own_l = lane < 8 ? O_M + i * 128 + lane * 16 : O_RED + 2304 + (lane & 31) * 16;
  if (w == 0) { LDS16(own_l) = LD16(rs, m_st, 0); LDS16(O_CT) = (u32x4){0u, 0u, 1u, 0u}; *(LAS unsigned*)(lds + O_CT + 16) = 0u; }
  const unsigned rz_vo = w < 2 ? OFS_P + ((2 * k + w) >> 2) * ROW + posP * 4096u + ((2 * k + w) & 3) * 1024u + l16
                       : w == 2 ? ((k / 5) * 2 + (k % 5 == 4)) * ROW + (k % 5 & 3) * 1024u + m_vo
                       : k < 4 ? ((k >> 1) * 2 + 1) * ROW + 1024u + (k & 1) * 1024u + m_vo : OOB;
  int rz_b = w == 3 ? 2 : 1; unsigned rz_m = w < 2 ? 1u : 3u, rz_k = w < 2 ? P_SLOT : M_SLOT;
  asm volatile("" : "+s"(rz_b), "+s"(rz_m), "+s"(rz_k));
  const unsigned red_w = (g >> 1) == hf ? O_RED + i8 * 272 + (g & 1) * 128 + (w * 2 + hf) * 8 : O_RED + 2304 + lane * 8;
  const unsigned red_r = O_RED + i8 * 272 + g * 64 + hf * 32;
  const unsigned jp_vo = lane < 4 ? OFS_CW + CW_JP + (unsigned)(40 + rep * 8 + (16 * k + (lane & 1) * 15) / 20) * 128u : OOB;
  unsigned jhave = 0;
  bool dead = false;
  ck.edge(); ck.rate();
  Sched sd; sd.init(ck.global(mtc()), a);
  unsigned mM = mtc();
  TMARK(1);
  STAMP_DECL(8);
  u32x4 zf = {0u, 0u, 0u, 0u};
  auto step = [&](int s, auto LAST, auto KIND) {
    constexpr bool last = decltype(LAST)::value, kp = decltype(KIND)::value == 2, w0 = decltype(KIND)::value == 0;
    constexpr int NC = w0 ? 0 : kp ? 3 : 4;
    const unsigned sl = (unsigned)(s & 3) * M_SLOT, mT = mM;
    need_progress(rs, jp_vo, (unsigned)s + 1u, jhave);
    u32x4 yf[4];
    const unsigned ys = (unsigned)(s * NH * 40 + ct) * 1024u, z_so = s < 1 ? FAR : (unsigned)((s - 1) * 40 + ct) * 8192u;
    if constexpr (kp) static_for<8>([&](auto K) { ck.rho_step<decltype(K)::value>(); });
    f32x4 za[4];
#pragma unroll
    for (int t = 0; t < 4; ++t) zero4(za[t]);
    unsigned sv = SENT; PIN_V(sv);
    u32x4 pz = {sv, sv, sv, sv}; PIN_V(pz);
    const unsigned rz_so = s < rz_b ? FAR : ((unsigned)(s - rz_b) & rz_m) * rz_k, fl_so = s > 1 ? OFS_CW + CW_SO + rep * 128u + (unsigned)(s - 2) * 1024u : FAR;
    const unsigned sp = (unsigned)(s & 1) * P_SLOT, sn = (unsigned)((s + 1) & 3) * M_SLOT;
    asm volatile("" :: "s"(rz_so), "s"(fl_so), "s"(z_so), "s"(ys + t0 * 81920u), "s"(ys + t2 * 81920u), "s"(sl + mc[0]), "s"(sl + mc[1]), "s"(sl + mc[2]), "s"(sl + mc[3]),
                 "s"(sp + pA), "s"(sp + pB), "s"(sp), "s"(sn));
    STAMP(0);
    wait_until(mM);
    SB();
    u32x4 cm[4];
#pragma unroll
    for (int q = 0; q < NC; ++q) cm[q] = LD16(rs, mo[q], sl + mc[q]);
    SB();
    auto bulk = [&](auto K) {
      constexpr int k = decltype(K)::value;
      if constexpr (k < 4) { if constexpr (!last) yf[k] = LD16(ry, y_vo, ys + (k < 2 ? t0 : t2) * 81920u + (k & 1) * 81920u); }
      else __builtin_amdgcn_raw_buffer_store_b128(zf, rz, z_vo, z_so, 16);
    };
    if (w0) NOPS(15);
    NOPS(15); NOPS(B_GAP); static_for<5>(bulk); SB();
    unsigned jv;
    if constexpr (kp) { jv = LD4(rs, jp_vo, jhave < min((unsigned)NSEG, (unsigned)s + 6u) ? 0u : FAR); SB(); }
    unsigned fails = 0;
    if constexpr (!w0) {
      unsigned chk = 0u;
#pragma unroll
      for (int q = 0; q < NC; ++q) chk = mx3(chk, cm[q][0], cm[q][2]);
      if (__builtin_expect(__any(chk == SENT), 0)) {
        fails = 1;
        *(LAS unsigned*)(lds + O_CT + 8) = (unsigned)s + 1u;
        for (unsigned spins = 0; !dead;) {
          __builtin_amdgcn_s_sleep(4);
          bool nd[NC]; u32x4 n[NC]; unsigned m = 0;
#pragma unroll
          for (int q = 0; q < NC; ++q) { nd[q] = mx4(cm[q]) == SENT; n[q] = LD16(rs, nd[q] ? m_vo : OOB, sl + mc[q]); }
#pragma unroll
          for (int q = 0; q < NC; ++q) { if (nd[q]) cm[q] = n[q]; m = max(m, mx4(cm[q])); }
          if (!__any(m == SENT)) break;
          if (++spins > SPIN_CAP) dead = true;
          asm volatile("" ::: "memory");
        }
      }
#pragma unroll
      for (int q = 0; q < NC; ++q) LDS16(ml[q]) = cm[q];
    }
    WG_BARRIER();
    STAMP(1);
    unsigned lt = 0u;
    {
      u32x4 mf[4];
      mf[0] = LDS16(O_M + l16); mf[1] = LDS16(O_M + 1024 + l16); mf[2] = LDS16(O_M + 2048 + l16);
      if constexpr (kp) lt = *(LAS unsigned*)(lds + O_CT + 8);
      SB();
      static_for<kp ? 8 : 10>([&](auto LL) {
        constexpr int L = decltype(LL)::value;
        smf_a<8 * L>(za[0], mf[L % 4], sidx); SB();
        if (L + 3 < 10) mf[(L + 3) % 4] = LDS16(O_M + (L + 3) * 1024 + l16);
        SB();
        smf_a<80 + 8 * L>(za[1], mf[L % 4], sidx); SB();
        if (L == 0) __builtin_amdgcn_raw_buffer_store_b128(pz, rs, rz_vo, rz_so, 16);
        if (L == 1 && w0) __builtin_amdgcn_raw_ptr_buffer_atomic_add_i32(1, rs, l20, fl_so, 0);
        SB();
        smf_a<160 + 8 * L>(za[2], mf[L % 4], sidx);
        if (L < 2) smf_a<240 + 8 * (L & 1)>(za[3], mf[L % 4], sidx); else smf_v(za[3], mf[L % 4], UV[L < 2 ? 0 : L - 2], sidx);
        SB();
      });
      asm volatile("" :: "v"(pz), "v"(zf));
      if constexpr (kp) ck.edge_z(za, mf[0], mf[1], UV[6], UV[7], sidx); else NOPS(7);
    }
    STAMP(2);
    unsigned zp[4];
    auto zpost = [&](int q) {
      auto x = __builtin_amdgcn_permlane32_swap(f2u(za[q >> 1][2 * (q & 1)]), f2u(za[2 + (q >> 1)][2 * (q & 1)]), false, false);
      auto y = __builtin_amdgcn_permlane32_swap(f2u(za[q >> 1][2 * (q & 1) + 1]), f2u(za[2 + (q >> 1)][2 * (q & 1) + 1]), false, false);
      zp[q] = pack2(u2f(x[0]) + u2f(x[1]), u2f(y[0]) + u2f(y[1])); pin_v(zp[q]);
      if (q == 3) {
        auto u = __builtin_amdgcn_permlane16_swap(zp[0], zp[2], false, false);
        auto v = __builtin_amdgcn_permlane16_swap(zp[1], zp[3], false, false);
        zf = (u32x4){u[0], v[0], u[1], v[1]}; pin_v(zf);
      }
    };
    if constexpr (last) { zpost(0); zpost(1); zpost(2); zpost(3); ST16(zf, rz, z_vo, (unsigned)(s * 40 + ct) * 8192u); return; }
    SB();
    unsigned mP;
    if constexpr (kp) {
      mP = ck.local(sd.tP);
      if (__builtin_expect(RFL(lt) == (unsigned)s + 1u, 0)) {
        fails |= 4;
        const u32x2 r = ann_valid((u32x2){SENT, SENT}, rs, an_ld, sl, dead);
        ck.edge();
        mP = ck.local(sd.T + (unsigned)max32((int)(r[0] - sd.T)) + a.d0);
      }
      *(LAS u32x2*)(lds + O_CT) = (u32x2){(unsigned)s + 1u, mP};
    } else {
      u32x2 v;
      do { v = *(volatile LAS u32x2*)(lds + O_CT); } while (RFL(v[0]) != (unsigned)s + 1u);
      mP = RFL(v[1]);
    }
    STAMP(3);
    SB();
    wait_until(mP);
    SB();
    u32x4 pf[5];
    pf[0] = LD16(rs, p_vo, sp + pA); SB(); zpost(0); NOPS(P_GAP);
    pf[1] = LD16(rs, p_vo, sp + pA + 1024u); SB(); zpost(1); NOPS(P_GAP);
    pf[2] = LD16(rs, p_vo, sp + pB); SB(); zpost(2); NOPS(P_GAP);
    pf[3] = LD16(rs, p_vo, sp + pB + 1024u); SB(); zpost(3); SB();
    pf[4] = LD16(rs, pm_vo, sp); SB();
    u32x2 an;
    if constexpr (kp) { NOPS(15); NOPS(7); an = LD8(rs, an_ld, sl); }
    STAMP(4);
    SB();
    f32x4 d;
    unsigned chk;
    mf32z(d, yf[0], pf[0]); SB(); chk = max(pf[0][0], pf[0][2]); PIN_V(chk); SB();
    mf32(d, yf[1], pf[1]); SB(); chk = mx3(chk, pf[1][0], pf[1][2]); PIN_V(chk); SB();
    mf32(d, yf[2], pf[2]); SB(); chk = mx3(chk, pf[2][0], pf[2][2]); PIN_V(chk); SB();
    mf32(d, yf[3], pf[3]); SB(); chk = mx3(chk, pf[3][0], pf[3][2]); PIN_V(chk); SB();
    mf32(d, zf, pf[4]); SB(); chk = mx3(chk, pf[4][0], pf[4][2]);
    u64 bad = __builtin_amdgcn_ballot_w64(chk == SENT); asm volatile("" : "+s"(bad));
    NOPS(5);
    *(LAS f32x2*)(lds + red_w) = (f32x2){d[0], d[1]}; *(LAS f32x2*)(lds + red_w + 64) = (f32x2){d[2], d[3]};
    SB();
    if (__builtin_expect(bad != 0, 0)) {
      fails |= 2;
      for (unsigned spins = 0; !dead;) {
        __builtin_amdgcn_s_sleep(2);
        bool nd[5]; u32x4 n[5]; unsigned m = 0;
#pragma unroll
        for (int q = 0; q < 5; ++q) { nd[q] = mx4(pf[q]) == SENT; n[q] = LD16(rs, nd[q] ? (q < 4 ? p_vo : pm_vo) : OOB, sp + (q < 2 ? pA : q < 4 ? pB : 0u) + (q & 1) * 1024u); }
#pragma unroll
        for (int q = 0; q < 5; ++q) { if (nd[q]) pf[q] = n[q]; m = max(m, mx4(pf[q])); }
        if (!__any(m == SENT)) break;
        if (++spins > SPIN_CAP) dead = true;
        asm volatile("" ::: "memory");
      }
#pragma unroll
      for (int q = 0; q < 5; ++q) PIN_V(pf[q]);
      NOPS(1);
      mf32z(d, yf[0], pf[0]); mf32(d, yf[1], pf[1]); mf32(d, yf[2], pf[2]); mf32(d, yf[3], pf[3]); mf32(d, zf, pf[4]);
      NOPS(7);
      *(LAS f32x2*)(lds + red_w) = (f32x2){d[0], d[1]}; *(LAS f32x2*)(lds + red_w + 64) = (f32x2){d[2], d[3]};
    }
    STAMP(5);
    if constexpr (w0) {
      asm volatile("s_waitcnt lgkmcnt(0)\n\ts_barrier ; first wave" ::: "memory");
      const f32x4 e = __builtin_bit_cast(f32x4, LDS16(red_r)) + __builtin_bit_cast(f32x4, LDS16(red_r + 16));
      const f32x2 u = (f32x2){e[0], e[1]} + (f32x2){e[2], e[3]};
      unsigned o0, o1, o2, o3;
      asm volatile("s_nop 1\n\tv_add_f32_dpp %0, %4, %4 row_ror:8 row_mask:0xf bank_mask:0xf\n\tv_add_f32_dpp %1, %5, %5 row_ror:8 row_mask:0xf bank_mask:0xf\n\t"
                   "v_cvt_pk_bf16_f32 %2, %0, %1\n\tv_min_u32 %0, -2, %2\n\tv_min_u32 %1, -2, %2\n\ts_nop 1\n\tv_permlane16_swap_b32 %0, %1\n\t"
                   "v_mov_b32 %2, %0\n\tv_mov_b32 %3, %1\n\ts_nop 0\n\tv_permlane32_swap_b32 %0, %2\n\tv_permlane32_swap_b32 %1, %3"
                   : "=&v"(o0), "=&v"(o1), "=&v"(o2), "=&v"(o3) : "v"(u[0]), "v"(u[1]));
      ST16(((u32x4){o0, o1, o2, o3}), rs, m_st, sn);
      LDS16(own_l) = (u32x4){o0, o1, o2, o3};
    } else if (kp) asm volatile("s_waitcnt lgkmcnt(0)\n\ts_barrier" ::: "memory");
    else asm volatile("s_waitcnt lgkmcnt(0)\n\ts_barrier\n\ts_sleep 3" ::: "memory");
    SB();
    STAMP(6);
    if constexpr (kp) {
      sd.next(ann_valid(an, rs, an_ld, sl, dead), a, s == 0);
      mM = ck.local(sd.T + OM_LAG);
      jv = min(jv, dppu<0xB1>(jv)); jhave = max(jhave, RFL(jv));
      LDS16(O_CT + 16) = (u32x4){(unsigned)s + 1u, mM, jhave, 0u};
    } else {
      u32x4 v;
      do { v = *(volatile LAS u32x4*)(lds + O_CT + 16); } while (RFL(v[0]) != (unsigned)s + 1u);
      mM = RFL(v[1]); jhave = max(jhave, RFL(v[2]));
    }
    STAMP(7);
    STAMP_FLUSH(s, mT - OM_LAG * 21u / 8u, mP, fails);
  };
  auto run = [&](auto KIND) {
#pragma unroll 1
    for (int s = 0; s < NSEG - 1; ++s) step(s, std::false_type(), KIND);
    step(NSEG - 1, std::true_type(), KIND);
  };
  if (w == 0) run(std::integral_constant<int, 0>()); else if (w == 3) run(std::integral_constant<int, 2>()); else run(std::integral_constant<int, 1>());
  VM_WAIT(0);
  WG_BARRIER();
  __builtin_amdgcn_raw_ptr_buffer_atomic_add_i32(1, rs, l20, w < 2 ? OFS_CW + CW_SO + rep * 128u + (unsigned)(NSEG - 2 + w) * 1024u : FAR, 0);
  TMARK(3);
  STAMP_DUMP();
}

#define J_FLAG 11
#define J_ST0 12
#define J_STAG 2
__host__ __device__ constexpr int j_slot(int kb) { return (kb + 1) % 3; }
template <bool YO>
__device__ __forceinline__ void proj_role(const RecArgs& a, LAS char* lds, int jn) {
  const int lane = fresh_lane(), w = __builtin_amdgcn_readfirstlane(threadIdx.x >> 6), c = lane & 15, g = lane >> 4;
  const __amdgpu_buffer_rsrc_t rs = RSRC(a.sync, SYNC_LAYER), rx = RSRC(a.xf, S_ * DM * 2), ro = YO ? RSRC(a.y, Y_IMG) : RSRC(a.qk, 2 * QK_IMG);
  const unsigned l16 = lane * 16;
#define DMA_P(so, lo, p) dma1k<((p) & 3) * 1024>(rx, l16, (so) + ((p) >> 2) * 4096u, (lo) + ((p) >> 2) * 4096u)
  const unsigned lb = (unsigned)(uintptr_t)lds + w * 10240u;
  static_for<20>([&](auto P_) { constexpr int p = decltype(P_)::value; DMA_P(w * 10240u + (p / 10) * 40960u, lb + (p / 10) * 40960u, p % 10); });
  const int T0 = 20 * (YO ? jn - 40 : jn) + 5 * w;
  u32x4 WV[36];
  {
    const __amdgpu_buffer_rsrc_t rw = YO ? RSRC(a.uf, 80 * 16 * 20480) : RSRC(a.wf, 768 * 20480);
    static_for<100>([&](auto F) {
      constexpr int f = decltype(F)::value;
      const unsigned so = (unsigned)T0 * 20480u + f * 1024u;
      if (f < 64) { ld_a<4 * (f & 63)>(rw, l16, so); VM_WAIT(24); } else WV[f < 64 ? 0 : f - 64] = LDP16(rw, l16, so);
    });
#pragma unroll
    for (int f = 0; f < 36; ++f) PIN_V(WV[f]);
  }
  const unsigned o_vo = YO ? (c >> 3) * 40960u + g * 256u + (c & 7) * 16u : l16;
  const unsigned l20 = l16 << 20, f_so = w == 0 ? OFS_CW + CW_JP + jn * 128u : FAR;
  unsigned o_so[5];
#pragma unroll
  for (int t = 0; t < 5; ++t) {
    const unsigned T = T0 + t;
    o_so[t] = YO ? (T & 15u) * 81920u + (T >> 5) * 1024u + ((T >> 4) & 1u) * 128u : T < 384u ? T * 1024u : T < 768u ? QK_IMG + (T - 384u) * 1024u : FAR;
  }
  TMARK(1);
  constexpr unsigned OSEG = YO ? NH * 40960u : 384u * 1024u;
  u32x4 pend[5], xr[3][2];
  f32x4 acc[10];
#pragma unroll
  for (int q = 0; q < 10; ++q) zero4(acc[q]);
#pragma unroll
  for (int t = 0; t < 5; ++t) pend[t] = (u32x4){0u, 0u, 0u, 0u};
  auto pack = [&](int q) { unsigned u = pack2(acc[q][0], acc[q][1]), v = pack2(acc[q][2], acc[q][3]); pin_v(u); pin_v(v); pend[q >> 1][2 * (q & 1)] = u; pend[q >> 1][2 * (q & 1) + 1] = v; };
  VM_WAIT(0);
  WG_BARRIER();
  unsigned bo = 0u;
  xr[1][0] = LDS16(l16); xr[1][1] = LDS16(l16 + 20480);
#pragma unroll 1
  for (int s = 0; s < NSEG; ++s) {
    if (J_STAG && s >= J_ST0 && s < J_ST0 + 8) { for (int d = ((int)(blockIdx.x >> 3) * 5 + (int)(blockIdx.x & 7) * 2) % 13; d > 0; --d) NOPS(J_STAG); }
    const unsigned bn = bo == 81920u ? 0u : bo + 40960u, bf = bn == 81920u ? 0u : bn + 40960u;
    const unsigned pso = s > 0 ? (unsigned)(s - 1) * OSEG : FAR, dso = (unsigned)min(s + 2, NSEG - 1) * 40960u + w * 10240u, dlo = lb + bf;
    unsigned xb = bo + l16, xn = bn + l16, sv = (unsigned)s; PIN_V(xb); PIN_V(xn); PIN_V(sv);
    asm volatile("" :: "s"(pso), "s"(dso), "s"(dlo));
    SB();
    static_for<200>([&](auto N) {
      constexpr int n = decltype(N)::value, kb = n / 10, q = n % 10, f = (q >> 1) * 20 + kb;
      constexpr int sl = j_slot(kb);
      if (f < 64) {
        if (kb == 0) { if (YO) mf32z_ba<4 * (f & 63)>(acc[q], xr[sl][q & 1]); else mf32z_aa<4 * (f & 63)>(acc[q], xr[sl][q & 1]); }
        else { if (YO) mf32_ba<4 * (f & 63)>(acc[q], xr[sl][q & 1]); else mf32_aa<4 * (f & 63)>(acc[q], xr[sl][q & 1]); }
      } else {
        const u32x4 wv = WV[f < 64 ? 0 : f - 64], xv = xr[sl][q & 1];
        if (kb == 0) { if (YO) mf32z(acc[q], xv, wv); else mf32z(acc[q], wv, xv); }
        else { if (YO) mf32(acc[q], xv, wv); else mf32(acc[q], wv, xv); }
      }
      SB();
      if constexpr (kb < 18) { if (q == 3 || q == 4) xr[j_slot(kb + 2)][q - 3] = LDS16(xb + (q - 3) * 20480 + (kb + 2) * 1024); }
      else if constexpr (kb == 19) { if (q < 2) xr[1][q] = LDS16(xn + q * 20480); }
      if constexpr (kb == 0) { if (q < 2) xr[2][q] = LDS16(xb + q * 20480 + 1024); }
      if constexpr (q == 7 && kb >= 1 && kb < 6) __builtin_amdgcn_raw_buffer_store_b128(pend[kb - 1], ro, o_vo, o_so[kb - 1] + pso, 16);
      if constexpr (q == 7 && kb == J_FLAG) {
        asm volatile("s_waitcnt vmcnt(0)\n\ts_barrier" ::: "memory");
        __builtin_amdgcn_raw_buffer_store_b32(sv, rs, l20, f_so, 16);
      }
      if constexpr ((q == 6 || q == 8) && kb > J_FLAG && 2 * (kb - J_FLAG - 1) < 10) DMA_P(dso, dlo, 2 * (kb - J_FLAG - 1) + (q == 8));
      SB();
      if constexpr (kb == 19 && q >= 3) pack(q - 3);
      if constexpr (kb == 0 && q < 3) pack(7 + q);
      SB();
    });
    bo = bn;
  }
  NOPS(7);
  pack(7); pack(8); pack(9);
#pragma unroll
  for (int t = 0; t < 5; ++t) ST16(pend[t], ro, o_vo, o_so[t] + (NSEG - 1) * OSEG);
  VM_WAIT(0);
  WG_BARRIER();
  __builtin_amdgcn_raw_buffer_store_b32((unsigned)NSEG, rs, l16 << 20, w == 0 ? OFS_CW + CW_JP + jn * 128u : FAR, 16);
  TMARK(2);
}

constexpr int T_LD = 84;
#define T_SLP 60
#define T_GRP 5
#define T_FLY 0
__device__ __forceinline__ void token_role(const RecArgs& a, LAS char* lds, int rep) {
  const int tid = threadIdx.x, w = __builtin_amdgcn_readfirstlane(tid >> 6);
  const __amdgpu_buffer_rsrc_t rs = RSRC(a.sync, SYNC_LAYER), ry = RSRC(a.y, Y_IMG), rz = RSRC(a.z, Z_IMG), rp = RSRC(a.pt, PT_IMG), rx = RSRC(a.xr, S_ * DM * 2);
  LAS float* part = (LAS float*)lds;
  LAS unsigned* tick = (LAS unsigned*)(lds + 4 * 32 * T_LD * 4);
  auto wait_count = [&](unsigned off, unsigned want, bool calm) {
    if (w == 0) {
      for (unsigned spins = 0; spins < SPIN_CAP; ++spins) {
        if (RFL(LD4(rs, off, 0)) >= want) break;
        if (calm) __builtin_amdgcn_s_sleep(T_SLP); else __builtin_amdgcn_s_sleep(6);
        asm volatile("" ::: "memory");
      }
    }
    WG_BARRIER();
  };
#pragma unroll 1
  for (;;) {
    WG_BARRIER();
    if (tid == 0) *tick = __hip_atomic_fetch_add((GAS unsigned*)(a.sync + OFS_CW + CW_TQ + rep * 128), 1u, RLX_AGENT);
    WG_BARRIER();
    const int n = __builtin_amdgcn_readfirstlane(*(volatile LAS unsigned*)tick);
    if (n >= 64) { TMARK(3); return; }
    const int s = rep + 8 * (n >> 3), cg = n & 7;
    const bool calm = s < NSEG - 3;
    int lane = tid & 63; PIN_V(lane);
    const int c = lane & 15, g = lane >> 4;
    u32x4 xo[2];
#pragma unroll
    for (int r = 0; r < 2; ++r) { const int p = r * 256 + tid; if (p < 320) xo[r] = *(const u32x4*)(a.xold + (a.layer ? (size_t)s * 20480 + ((p >> 4) & 1) * 10240 + (10 * cg + (p >> 5)) * 128 + (p & 15) * 8 : (size_t)(s * 32 + (p & 31)) * DM + 80 * cg + (p >> 5) * 8)); }
    if (!calm) wait_count(OFS_CW + CW_SS + (s - 4) * 128, 32u, true);
    wait_count(OFS_CW + CW_SO + (s * 8 + cg) * 128, 10u, calm);
    const unsigned l16 = lane * 16;
    static_for<50>([&](auto F) {
      constexpr int f = decltype(F)::value, kk = f / 5, t = f % 5;
      if (kk < 8) ld_a1<4 * f>(ry, l16, (unsigned)((s * NH + 8 * w + kk) * 40 + 5 * cg + t) * 1024u);
      else ld_a1<4 * f>(rz, l16, (unsigned)((s * 40 + 5 * cg + t) * NH + 8 * w + 4 * (kk - 8)) * 256u);
      if (f % T_GRP == T_GRP - 1 && calm) VM_WAIT(T_FLY);
    });
    VM_WAIT(40);
    wait_count(OFS_CW + CW_SS + s * 128, 32u, calm);
    u32x4 bf[10][2];
#pragma unroll
    for (int kk = 0; kk < 10; ++kk)
#pragma unroll
      for (int b = 0; b < 2; ++b) {
        bf[kk][b] = LD16(rp, l16, (unsigned)s * PT_SEG + (kk < 8 ? (unsigned)((8 * w + kk) * 2 + b) * 1024u : PT_M + (unsigned)(b * NH + 8 * w + 4 * (kk - 8)) * 256u));
        if (b == 1 && (kk & 1) && calm) VM_WAIT(T_FLY);
      }
    f32x4 acc[5][2];
    static_for<100>([&](auto N) {
      constexpr int n = decltype(N)::value, kk = n / 10, t = (n % 10) >> 1, b = n & 1;
      if (kk == 0) mf32z_aa<4 * t>(acc[t][b], bf[0][b]); else mf32_aa<4 * (5 * kk + t)>(acc[t][b], bf[kk][b]);
    });
    NOPS(7);
#pragma unroll
    for (int t = 0; t < 5; ++t)
#pragma unroll
      for (int b = 0; b < 2; ++b) {
        LAS float* p = part + (w * 32 + 16 * b + c) * T_LD + 16 * t + 8 * (g & 1) + 2 * (g >> 1);
        *(LAS f32x2*)p = (f32x2){acc[t][b][0], acc[t][b][1]}; *(LAS f32x2*)(p + 4) = (f32x2){acc[t][b][2], acc[t][b][3]};
      }
    WG_BARRIER();
#pragma unroll
    for (int r = 0; r < 2; ++r) {
      const int p = r * 256 + tid;
      if (p < 320) {
        const LAS float* q = part + (p & 31) * T_LD + (p >> 5) * 8;
        f32x4 s0 = *(const LAS f32x4*)q, s1 = *(const LAS f32x4*)(q + 4);
#pragma unroll
        for (int k = 1; k < 4; ++k) { s0 += *(const LAS f32x4*)(q + k * 32 * T_LD); s1 += *(const LAS f32x4*)(q + k * 32 * T_LD + 4); }
        u32x4 o;
#pragma unroll
        for (int k = 0; k < 2; ++k) {
          o[k] = pack2(bf2f(xo[r][k] & 0xffff) + rbf(s0[2 * k]), bf2f(xo[r][k] >> 16) + rbf(s0[2 * k + 1]));
          o[2 + k] = pack2(bf2f(xo[r][2 + k] & 0xffff) + rbf(s1[2 * k]), bf2f(xo[r][2 + k] >> 16) + rbf(s1[2 * k + 1]));
        }
        ST16(o, rx, (unsigned)(((p >> 4) & 1) * 20480 + (10 * cg + (p >> 5)) * 256 + (p & 15) * 16), (unsigned)s * 40960u);
      }
    }
    VM_WAIT(0);
    WG_BARRIER();
    __builtin_amdgcn_raw_ptr_buffer_atomic_add_i32(1, rs, l16 << 20, w == 0 ? OFS_CW + CW_TC + (unsigned)s * 128u : FAR, 0);
  }
}

#define B_SETS 6
#define B_CUT 46
constexpr int B_ONE = 2 * 40960, B_ST = B_ONE + 20480;
template <bool VO>
__device__ __forceinline__ void bproj_role(const RecArgs& a, LAS char* lds, int rep, int s0, int s1, int ds) {
  const int lane = fresh_lane(), w = __builtin_amdgcn_readfirstlane(threadIdx.x >> 6), c = lane & 15, g = lane >> 4;
  const __amdgpu_buffer_rsrc_t rs = RSRC(a.sync, SYNC_LAYER), rx = RSRC(a.xr, S_ * DM * 2), ro = RSRC(a.qkvb, 3 * BH * S_ * 64);
  const unsigned l16 = lane * 16;
  const int T0 = 16 * rep + 4 * w;
  u32x4 WV[16];
  {
    const __amdgpu_buffer_rsrc_t rw = RSRC(a.wb, 120 * 20480);
    static_for<80>([&](auto F) {
      constexpr int f = decltype(F)::value;
      const unsigned so = (unsigned)T0 * 20480u + f * 1024u;
      if (f < 64) { ld_a<4 * (f & 63)>(rw, l16, so); VM_WAIT(40); } else WV[f & 15] = LDP16(rw, l16, so);
    });
    VM_WAIT(0);
#pragma unroll
    for (int f = 0; f < 16; ++f) PIN_V(WV[f]);
  }
  auto mm = [&](auto F, auto Z, f32x4& acc, u32x4 x) {
    constexpr int f = decltype(F)::value; constexpr bool z = decltype(Z)::value;
    if constexpr (f < 64) { if (z) { if (VO) mf32z_ba<4 * f>(acc, x); else mf32z_aa<4 * f>(acc, x); } else { if (VO) mf32_ba<4 * f>(acc, x); else mf32_aa<4 * f>(acc, x); } }
    else { if (VO) mf32(acc, x, WV[f - 64]); else mf32(acc, WV[f - 64], x); }
  };
  u32x4 one = {0x3f803f80u, 0x3f803f80u, 0x3f803f80u, 0x3f803f80u}; PIN_V(one);
#pragma unroll
  for (int k = 0; k < 5; ++k) LDS16(B_ONE + (w * 5 + k) * 1024 + l16) = one;
  f32x4 cs[4];
  NOPS(1);
  static_for<80>([&](auto N) { constexpr int n = decltype(N)::value; mm(std::integral_constant<int, (n % 4) * 20 + n / 4>(), std::bool_constant<(n < 4)>(), cs[n % 4], one); });
  unsigned o_vo[4];
#pragma unroll
  for (int t = 0; t < 4; ++t) {
    const int n = 16 * (T0 + t) + (VO ? c : 4 * g), im = n / DM, hd = (n - im * DM) / BD, e = n - im * DM - hd * BD;
    o_vo[t] = n >= 3 * DM ? OOB : (unsigned)(im * BH + hd) * (S_ * 64u) + (VO ? ((g >> 1) * 32 + e) * 16 + (g & 1) * 8 : (e >> 3) * 512 + c * 16 + (e & 4) * 2);
  }
  const float qs = T0 < 40 ? 0.22360679774997896f * 1.4426950408889634f : 1.0f;
  const unsigned sa_ = (w & 1) * 20480 + l16, st_w = (c >> 2) == g ? B_ST + (w * 16 + c) * 4 : B_ST + 256 + lane * 4;
  u32x4 xq[10];
  auto ready = [&](int s) {
    for (unsigned spins = 0; spins < SPIN_CAP; ++spins) {
      if (RFL(LD4(rs, l16 << 20, OFS_CW + CW_TC + (unsigned)s * 128u)) >= 8u) break;
      if (s < NSEG - 4) __builtin_amdgcn_s_sleep(40); else __builtin_amdgcn_s_sleep(6);
      asm volatile("" ::: "memory");
    }
  };
  bool have = false;
  unsigned xb = 40960;
#pragma unroll 1
  for (int s = s0; s < s1; s += ds) {
    xb = 40960 - xb;
    const unsigned xn = 40960 - xb + w * 10240 + l16;
    if (!have) {
      ready(s);
#pragma unroll
      for (int k = 0; k < 10; ++k) xq[k] = LD16(rx, l16, (unsigned)s * 40960u + w * 10240u + k * 1024u);
#pragma unroll
      for (int k = 0; k < 10; ++k) LDS16(xb + w * 10240 + k * 1024 + l16) = xq[k];
    }
    const unsigned nx = LD4(rs, l16 << 20, s + ds < s1 ? OFS_CW + CW_TC + (unsigned)(s + ds) * 128u : FAR);
    WG_BARRIER();
    f32x4 acc[8], as; u32x4 xr[3][4];
    const unsigned xa = xb + l16, sa = xb + sa_, sb = w < 2 ? sa : B_ONE + l16;
    unsigned nso = FAR;
#define B_GET(kb, q) (xr[(kb) % 3][q] = LDS16(((q) < 2 ? xa + (q) * 20480 : (q) == 2 ? sa : sb) + (kb) * 1024))
#pragma unroll
    for (int q = 0; q < 8; ++q) B_GET(q >> 2, q & 3);
    SB();
    static_for<180>([&](auto N) {
      constexpr int n = decltype(N)::value, kb = n / 9, q = n % 9;
      if constexpr (q < 8) mm(std::integral_constant<int, (q >> 1) * 20 + kb>(), std::bool_constant<kb == 0>(), acc[q], xr[kb % 3][q & 1]);
      else { if (kb == 0) mf32z(as, xr[0][2], xr[0][3]); else mf32(as, xr[kb % 3][2], xr[kb % 3][3]); }
      SB();
      if (q >= 1 && q < 5 && kb + 2 < 20) B_GET(kb + 2, q - 1);
      if (n == 5 * 9 + 5) { have = RFL(nx) >= 8u; nso = have ? (unsigned)(s + ds) * 40960u + w * 10240u : FAR; pin_s(nso); }
      if constexpr (kb >= 5 && kb < 10 && (q == 6 || q == 7)) xq[2 * (kb - 5) + q - 6] = LD16(rx, l16, nso + (2 * (kb - 5) + q - 6) * 1024u);
      if constexpr (kb >= 15 && (q == 6 || q == 7)) LDS16(xn + (2 * (kb - 15) + q - 6) * 1024) = xq[2 * (kb - 15) + q - 6];
      SB();
    });
    NOPS(7);
    *(LAS float*)(lds + st_w) = (c & 2) ? ((c & 1) ? as[3] : as[2]) : ((c & 1) ? as[1] : as[0]);
    WG_BARRIER();
#pragma unroll
    for (int tt = 0; tt < 2; ++tt) {
      f32x4 mu, rr;
      if (VO) {
        const f32x4 sq = *(LAS f32x4*)(lds + B_ST + tt * 64 + g * 16), sm = *(LAS f32x4*)(lds + B_ST + 128 + tt * 64 + g * 16);
        mu = sm * (1.0f / DM);
#pragma unroll
        for (int r = 0; r < 4; ++r) rr[r] = __builtin_amdgcn_rsqf(fmaxf(sq[r] * (1.0f / DM) - mu[r] * mu[r], 0.f) + 1e-5f);
      } else {
        const float sq = *(LAS float*)(lds + B_ST + tt * 64 + c * 4), m1 = *(LAS float*)(lds + B_ST + 128 + tt * 64 + c * 4) * (1.0f / DM);
        const float r1 = __builtin_amdgcn_rsqf(fmaxf(sq * (1.0f / DM) - m1 * m1, 0.f) + 1e-5f) * qs;
        mu = (f32x4){m1, m1, m1, m1}; rr = (f32x4){r1, r1, r1, r1};
      }
#pragma unroll
      for (int t = 0; t < 4; ++t) {
        const f32x4 y = (acc[2 * t + tt] - mu * cs[t]) * rr;
        __builtin_amdgcn_raw_buffer_store_b64((u32x2){pack2(y[0], y[1]), pack2(y[2], y[3])}, ro, o_vo[t], (unsigned)s * 2048u + tt * (VO ? 1024u : 256u), 0);
      }
    }
  }
  TMARK(3);
  __builtin_amdgcn_endpgm();
}

__global__ __launch_bounds__(256, 1) void rec_kernel(RecArgs a) {
  __shared__ __attribute__((aligned(16))) char lds_[S_END + DBG_LDS];
  LAS char* lds = (LAS char*)lds_;
  const int b = blockIdx.x, rep = b & 7, il = b >> 3;
  AGPR_FENCE();
  TMARK(0);
  if (il < 14) {
    const int lane = threadIdx.x & 63;
    const unsigned* tab = (const unsigned*)a.cal;
    const unsigned x = tab[256 + (lane >> 3)] & 7u, xr = tab[256 + rep] & 7u;
    const unsigned* tp = tab + (((((x & 1u) << 2) | (x >> 1)) ^ lane) & 7u) * 8u + (lane >> 3);
    unsigned v = (min(tp[0], tp[128]) + min(tp[64], tp[192])) & 0x1ffffu;
    v += __shfl_xor(v, 8); v += __shfl_xor(v, 16); v += __shfl_xor(v, 32);
    v = v * 8u + (lane & 7);
    v = min(v, (unsigned)__shfl_xor(v, 1)); v = min(v, (unsigned)__shfl_xor(v, 2)); v = min(v, (unsigned)__shfl_xor(v, 4));
    const unsigned posP = RFL(((((xr & 1u) << 2) | (xr >> 1)) ^ v ^ (unsigned)a.layer) & 7u), posM = posP ^ 4u;
    const int w = __builtin_amdgcn_readfirstlane(threadIdx.x >> 6);
    if (il < 4) {
      if (w < 2) score_role<0>(a, lds, rep, il, posM, posP); else if (w == 2) score_role<1>(a, lds, rep, il, posM, posP); else score_role<2>(a, lds, rep, il, posM, posP);
    } else out_role(a, lds, rep, il - 4, posM, posP);
    return;
  }
  if (il < 19) { if (b != 19 * 8 - 1) proj_role<false>(a, lds, b - 14 * 8); }
  else if (il < 27) proj_role<true>(a, lds, 40 + rep * 8 + il - 19);
  AGPR_FENCE();
  if (il > 27 - B_SETS - 1 && il <= 27) {
    const int s0 = il == 27 ? 0 : B_CUT + 26 - il, s1 = il == 27 ? B_CUT : NSEG, ds = il == 27 ? 1 : B_SETS;
    if (rep < 5) bproj_role<false>(a, lds, rep, s0, s1, ds); else bproj_role<true>(a, lds, rep, s0, s1, ds);
  }
  AGPR_FENCE();
  token_role(a, lds, rep);
}

#define NKS 2
constexpr int KQ = S_ / NKS;
#define OUT_IDX(t, n) ((size_t)((t) >> 4) * 10240 + ((n) >> 3) * 128 + ((t) & 15) * 8 + ((n) & 7))
DEV f32x16 qk32(u32x4 k, u32x4 q, f32x16 c) { return __builtin_amdgcn_mfma_f32_32x32x16_bf16(__builtin_bit_cast(bf16x8, k), __builtin_bit_cast(bf16x8, q), c, 0, 0, 0); }
template <int H> DEV f32x16 pv32(u32x4 v, f32x16 p, f32x16 c) {
  typedef __attribute__((ext_vector_type(8))) float f32x8;
  const f32x8 h = __builtin_shufflevector(p, p, 8 * H, 8 * H + 1, 8 * H + 2, 8 * H + 3, 8 * H + 4, 8 * H + 5, 8 * H + 6, 8 * H + 7);
  return __builtin_amdgcn_smfmac_f32_32x32x32_bf16(__builtin_bit_cast(hbf16x8, v), __builtin_bit_cast(hbf16x16, h), c, 0xDDDD, 0, 0);
}
DEV float max16(f32x16 s) {
  float mx = fmx(fmx(s[0], s[1]), s[2]);
#pragma unroll
  for (int i = 3; i < 15; i += 2) mx = fmx(fmx(mx, s[i]), s[i + 1]);
  mx = fmx(mx, s[15]);
  auto rr = __builtin_amdgcn_permlane32_swap(f2u(mx), f2u(mx), false, false);
  return fmx(u2f(rr[0]), u2f(rr[1]));
}
__global__ __launch_bounds__(256 * NKS, NKS) void attb_kernel(const u16* __restrict__ Qp, const u16* __restrict__ Kp, const u16* __restrict__ VbT,
                                                       u16* __restrict__ O) {
  __shared__ float mrg_[4][NKS][2][13][64];
  const int lane = threadIdx.x & 63, wv = __builtin_amdgcn_readfirstlane(threadIdx.x >> 6), kh = wv >> 2, r = lane & 31, hh = lane >> 5;
  float (*mrg)[2][13][64] = mrg_[wv & 3];
  const int hd = (blockIdx.x & 7) * 4 + ((blockIdx.x >> 3) & 3), qb = blockIdx.x >> 5;
  const int q0 = qb * 256 + (wv & 3) * 64;
  const __amdgpu_buffer_rsrc_t rk = RSRC(Kp, BH * S_ * 64), rv = RSRC(VbT, BH * S_ * 64);
  unsigned l16 = lane * 16;
  const unsigned so0 = (unsigned)(hd * S_ + kh * KQ) * 64u;
  u32x4 kf[4][2], vf[4][2], qf[2][2];
  kf[0][0] = LDP16(rk, l16, so0); kf[0][1] = LDP16(rk, l16 + 1024, so0);
#pragma unroll
  for (int qt = 0; qt < 2; ++qt)
#pragma unroll
    for (int ks = 0; ks < 2; ++ks) qf[qt][ks] = *(const u32x4*)(Qp + ((size_t)hd * S_ + q0 + qt * 32) * 32 + ks * 512 + lane * 8);
#pragma unroll
  for (int u = 0; u < 3; ++u) {
    vf[u][0] = LDP16(rv, l16, so0 + u * 2048u); vf[u][1] = LDP16(rv, l16 + 1024, so0 + u * 2048u);
    kf[u + 1][0] = LDP16(rk, l16, so0 + (u + 1) * 2048u); kf[u + 1][1] = LDP16(rk, l16 + 1024, so0 + (u + 1) * 2048u);
  }
  f32x16 Z;
#pragma unroll
  for (int i = 0; i < 16; ++i) Z[i] = 0.f;
  f32x16 o[2] = {Z, Z}, sa[2];
  float m[2];
#pragma unroll
  for (int qt = 0; qt < 2; ++qt) {
    const float mn = max16(qk32(kf[0][1], qf[qt][1], qk32(kf[0][0], qf[qt][0], Z))), hi = rbf(mn), lo = rbf(mn - hi);
    m[qt] = hi + lo;
    if (hh == 0) qf[qt][1][2] = pack2(-hi, -lo);
  }
  sa[0] = qk32(kf[0][1], qf[0][1], qk32(kf[0][0], qf[0][0], Z));
  sa[1] = Z; vf[3][1] = (u32x4){0u, 0u, 0u, 0u};
  {
    unsigned so = so0 + 6144u;
    vf[3][0] = vf[3][1];
    asm volatile("s_nop 11\n1:\n\t"
      "s_nop 1\n\t" "v_smfmac_f32_32x32x32_bf16 v[24:39], v[148:151], v[64:71], %[ix]\n\t"
      "buffer_load_dwordx4 v[144:147], %[l16], %[rv], %[so] offen offset:0\n\t"
      "v_exp_f32 v40, v40\n\t" "v_exp_f32 v41, v41\n\t" "v_exp_f32 v42, v42\n\t" "v_exp_f32 v43, v43\n\t"
      "v_mfma_f32_32x32x16_bf16 v[56:71], v[88:91], v[80:83], 0\n\t"
      "buffer_load_dwordx4 v[148:151], %[l16], %[rv], %[so] offen offset:1024\n\t"
      "v_exp_f32 v44, v44\n\t" "v_exp_f32 v45, v45\n\t" "v_exp_f32 v46, v46\n\t" "v_exp_f32 v47, v47\n\t"
      "v_mfma_f32_32x32x16_bf16 v[56:71], v[92:95], v[84:87], v[56:71]\n\t"
      "buffer_load_dwordx4 v[88:91], %[l16], %[rk], %[so] offen offset:2048\n\t"
      "v_exp_f32 v48, v48\n\t" "v_exp_f32 v49, v49\n\t" "v_exp_f32 v50, v50\n\t" "v_exp_f32 v51, v51\n\t"
      "s_waitcnt vmcnt(13)\n\t" "v_smfmac_f32_32x32x32_bf16 v[8:23], v[96:99], v[40:47], %[ix]\n\t"
      "buffer_load_dwordx4 v[92:95], %[l16], %[rk], %[so] offen offset:3072\n\t"
      "v_exp_f32 v52, v52\n\t" "v_exp_f32 v53, v53\n\t" "v_exp_f32 v54, v54\n\t" "v_exp_f32 v55, v55\n\t"
      "s_add_u32 %[so], %[so], 0x800\n\t" "s_waitcnt vmcnt(12)\n\t" "v_smfmac_f32_32x32x32_bf16 v[8:23], v[100:103], v[48:55], %[ix]\n\t"
      "v_exp_f32 v56, v56\n\t" "v_exp_f32 v57, v57\n\t" "v_exp_f32 v58, v58\n\t" "v_exp_f32 v59, v59\n\t"
      "v_mfma_f32_32x32x16_bf16 v[40:55], v[104:107], v[72:75], 0\n\t"
      "v_exp_f32 v60, v60\n\t" "v_exp_f32 v61, v61\n\t" "v_exp_f32 v62, v62\n\t" "v_exp_f32 v63, v63\n\t"
      "v_mfma_f32_32x32x16_bf16 v[40:55], v[108:111], v[76:79], v[40:55]\n\t"
      "v_exp_f32 v64, v64\n\t" "v_exp_f32 v65, v65\n\t" "v_exp_f32 v66, v66\n\t" "v_exp_f32 v67, v67\n\t"
      "v_smfmac_f32_32x32x32_bf16 v[24:39], v[96:99], v[56:63], %[ix]\n\t"
      "v_exp_f32 v68, v68\n\t" "v_exp_f32 v69, v69\n\t" "v_exp_f32 v70, v70\n\t" "v_exp_f32 v71, v71\n\t"
      "s_nop 1\n\t" "v_smfmac_f32_32x32x32_bf16 v[24:39], v[100:103], v[64:71], %[ix]\n\t"
      "buffer_load_dwordx4 v[96:99], %[l16], %[rv], %[so] offen offset:0\n\t"
      "v_exp_f32 v40, v40\n\t" "v_exp_f32 v41, v41\n\t" "v_exp_f32 v42, v42\n\t" "v_exp_f32 v43, v43\n\t"
      "v_mfma_f32_32x32x16_bf16 v[56:71], v[104:107], v[80:83], 0\n\t"
      "buffer_load_dwordx4 v[100:103], %[l16], %[rv], %[so] offen offset:1024\n\t"
      "v_exp_f32 v44, v44\n\t" "v_exp_f32 v45, v45\n\t" "v_exp_f32 v46, v46\n\t" "v_exp_f32 v47, v47\n\t"
      "v_mfma_f32_32x32x16_bf16 v[56:71], v[108:111], v[84:87], v[56:71]\n\t"
      "buffer_load_dwordx4 v[104:107], %[l16], %[rk], %[so] offen offset:2048\n\t"
      "v_exp_f32 v48, v48\n\t" "v_exp_f32 v49, v49\n\t" "v_exp_f32 v50, v50\n\t" "v_exp_f32 v51, v51\n\t"
      "s_waitcnt vmcnt(13)\n\t" "v_smfmac_f32_32x32x32_bf16 v[8:23], v[112:115], v[40:47], %[ix]\n\t"
      "buffer_load_dwordx4 v[108:111], %[l16], %[rk], %[so] offen offset:3072\n\t"
      "v_exp_f32 v52, v52\n\t" "v_exp_f32 v53, v53\n\t" "v_exp_f32 v54, v54\n\t" "v_exp_f32 v55, v55\n\t"
      "s_add_u32 %[so], %[so], 0x800\n\t" "s_waitcnt vmcnt(12)\n\t" "v_smfmac_f32_32x32x32_bf16 v[8:23], v[116:119], v[48:55], %[ix]\n\t"
      "v_exp_f32 v56, v56\n\t" "v_exp_f32 v57, v57\n\t" "v_exp_f32 v58, v58\n\t" "v_exp_f32 v59, v59\n\t"
      "v_mfma_f32_32x32x16_bf16 v[40:55], v[120:123], v[72:75], 0\n\t"
      "v_exp_f32 v60, v60\n\t" "v_exp_f32 v61, v61\n\t" "v_exp_f32 v62, v62\n\t" "v_exp_f32 v63, v63\n\t"
      "v_mfma_f32_32x32x16_bf16 v[40:55], v[124:127], v[76:79], v[40:55]\n\t"
      "v_exp_f32 v64, v64\n\t" "v_exp_f32 v65, v65\n\t" "v_exp_f32 v66, v66\n\t" "v_exp_f32 v67, v67\n\t"
      "v_smfmac_f32_32x32x32_bf16 v[24:39], v[112:115], v[56:63], %[ix]\n\t"
      "v_exp_f32 v68, v68\n\t" "v_exp_f32 v69, v69\n\t" "v_exp_f32 v70, v70\n\t" "v_exp_f32 v71, v71\n\t"
      "s_nop 1\n\t" "v_smfmac_f32_32x32x32_bf16 v[24:39], v[116:119], v[64:71], %[ix]\n\t"
      "buffer_load_dwordx4 v[112:115], %[l16], %[rv], %[so] offen offset:0\n\t"
      "v_exp_f32 v40, v40\n\t" "v_exp_f32 v41, v41\n\t" "v_exp_f32 v42, v42\n\t" "v_exp_f32 v43, v43\n\t"
      "v_mfma_f32_32x32x16_bf16 v[56:71], v[120:123], v[80:83], 0\n\t"
      "buffer_load_dwordx4 v[116:119], %[l16], %[rv], %[so] offen offset:1024\n\t"
      "v_exp_f32 v44, v44\n\t" "v_exp_f32 v45, v45\n\t" "v_exp_f32 v46, v46\n\t" "v_exp_f32 v47, v47\n\t"
      "v_mfma_f32_32x32x16_bf16 v[56:71], v[124:127], v[84:87], v[56:71]\n\t"
      "buffer_load_dwordx4 v[120:123], %[l16], %[rk], %[so] offen offset:2048\n\t"
      "v_exp_f32 v48, v48\n\t" "v_exp_f32 v49, v49\n\t" "v_exp_f32 v50, v50\n\t" "v_exp_f32 v51, v51\n\t"
      "s_waitcnt vmcnt(13)\n\t" "v_smfmac_f32_32x32x32_bf16 v[8:23], v[128:131], v[40:47], %[ix]\n\t"
      "buffer_load_dwordx4 v[124:127], %[l16], %[rk], %[so] offen offset:3072\n\t"
      "v_exp_f32 v52, v52\n\t" "v_exp_f32 v53, v53\n\t" "v_exp_f32 v54, v54\n\t" "v_exp_f32 v55, v55\n\t"
      "s_add_u32 %[so], %[so], 0x800\n\t" "s_waitcnt vmcnt(12)\n\t" "v_smfmac_f32_32x32x32_bf16 v[8:23], v[132:135], v[48:55], %[ix]\n\t"
      "v_exp_f32 v56, v56\n\t" "v_exp_f32 v57, v57\n\t" "v_exp_f32 v58, v58\n\t" "v_exp_f32 v59, v59\n\t"
      "v_mfma_f32_32x32x16_bf16 v[40:55], v[136:139], v[72:75], 0\n\t"
      "v_exp_f32 v60, v60\n\t" "v_exp_f32 v61, v61\n\t" "v_exp_f32 v62, v62\n\t" "v_exp_f32 v63, v63\n\t"
      "v_mfma_f32_32x32x16_bf16 v[40:55], v[140:143], v[76:79], v[40:55]\n\t"
      "v_exp_f32 v64, v64\n\t" "v_exp_f32 v65, v65\n\t" "v_exp_f32 v66, v66\n\t" "v_exp_f32 v67, v67\n\t"
      "v_smfmac_f32_32x32x32_bf16 v[24:39], v[128:131], v[56:63], %[ix]\n\t"
      "v_exp_f32 v68, v68\n\t" "v_exp_f32 v69, v69\n\t" "v_exp_f32 v70, v70\n\t" "v_exp_f32 v71, v71\n\t"
      "s_nop 1\n\t" "v_smfmac_f32_32x32x32_bf16 v[24:39], v[132:135], v[64:71], %[ix]\n\t"
      "buffer_load_dwordx4 v[128:131], %[l16], %[rv], %[so] offen offset:0\n\t"
      "v_exp_f32 v40, v40\n\t" "v_exp_f32 v41, v41\n\t" "v_exp_f32 v42, v42\n\t" "v_exp_f32 v43, v43\n\t"
      "v_mfma_f32_32x32x16_bf16 v[56:71], v[136:139], v[80:83], 0\n\t"
      "buffer_load_dwordx4 v[132:135], %[l16], %[rv], %[so] offen offset:1024\n\t"
      "v_exp_f32 v44, v44\n\t" "v_exp_f32 v45, v45\n\t" "v_exp_f32 v46, v46\n\t" "v_exp_f32 v47, v47\n\t"
      "v_mfma_f32_32x32x16_bf16 v[56:71], v[140:143], v[84:87], v[56:71]\n\t"
      "buffer_load_dwordx4 v[136:139], %[l16], %[rk], %[so] offen offset:2048\n\t"
      "v_exp_f32 v48, v48\n\t" "v_exp_f32 v49, v49\n\t" "v_exp_f32 v50, v50\n\t" "v_exp_f32 v51, v51\n\t"
      "s_waitcnt vmcnt(13)\n\t" "v_smfmac_f32_32x32x32_bf16 v[8:23], v[144:147], v[40:47], %[ix]\n\t"
      "buffer_load_dwordx4 v[140:143], %[l16], %[rk], %[so] offen offset:3072\n\t"
      "v_exp_f32 v52, v52\n\t" "v_exp_f32 v53, v53\n\t" "v_exp_f32 v54, v54\n\t" "v_exp_f32 v55, v55\n\t"
      "s_add_u32 %[so], %[so], 0x800\n\t" "s_waitcnt vmcnt(12)\n\t" "v_smfmac_f32_32x32x32_bf16 v[8:23], v[148:151], v[48:55], %[ix]\n\t"
      "v_exp_f32 v56, v56\n\t" "v_exp_f32 v57, v57\n\t" "v_exp_f32 v58, v58\n\t" "v_exp_f32 v59, v59\n\t"
      "v_mfma_f32_32x32x16_bf16 v[40:55], v[88:91], v[72:75], 0\n\t"
      "v_exp_f32 v60, v60\n\t" "v_exp_f32 v61, v61\n\t" "v_exp_f32 v62, v62\n\t" "v_exp_f32 v63, v63\n\t"
      "v_mfma_f32_32x32x16_bf16 v[40:55], v[92:95], v[76:79], v[40:55]\n\t"
      "v_exp_f32 v64, v64\n\t" "v_exp_f32 v65, v65\n\t" "v_exp_f32 v66, v66\n\t" "v_exp_f32 v67, v67\n\t"
      "v_smfmac_f32_32x32x32_bf16 v[24:39], v[144:147], v[56:63], %[ix]\n\t"
      "v_exp_f32 v68, v68\n\t" "v_exp_f32 v69, v69\n\t" "v_exp_f32 v70, v70\n\t" "v_exp_f32 v71, v71\n\t"
      "s_cmp_lt_u32 %[so], %[se]\n\ts_cbranch_scc1 1b\n\ts_waitcnt vmcnt(0)\n\t" "v_smfmac_f32_32x32x32_bf16 v[24:39], v[148:151], v[64:71], %[ix]\n\t" "s_nop 11"
      : "+{v[8:23]}"(o[0]), "+{v[24:39]}"(o[1]), "+{v[40:55]}"(sa[0]), "+{v[56:71]}"(sa[1]),
        "+{v[88:91]}"(kf[0][0]), "+{v[96:99]}"(vf[0][0]), "+{v[92:95]}"(kf[0][1]), "+{v[100:103]}"(vf[0][1]),
        "+{v[104:107]}"(kf[1][0]), "+{v[112:115]}"(vf[1][0]), "+{v[108:111]}"(kf[1][1]), "+{v[116:119]}"(vf[1][1]),
        "+{v[120:123]}"(kf[2][0]), "+{v[128:131]}"(vf[2][0]), "+{v[124:127]}"(kf[2][1]), "+{v[132:135]}"(vf[2][1]),
        "+{v[136:139]}"(kf[3][0]), "+{v[144:147]}"(vf[3][0]), "+{v[140:143]}"(kf[3][1]), "+{v[148:151]}"(vf[3][1]), [so] "+s"(so)
      : "{v[72:75]}"(qf[0][0]), "{v[76:79]}"(qf[0][1]), "{v[80:83]}"(qf[1][0]), "{v[84:87]}"(qf[1][1]), [l16] "v"(l16), [rk] "s"(rk), [rv] "s"(rv), [se] "s"(so0 + KQ * 64u + 6144u), [ix] "v"(0xDDDD) : "scc", "memory");
  }
  float big = 0.f;
#pragma unroll
  for (int i = 0; i < 12; ++i) big = fmx(fmx(big, __builtin_fabsf(o[0][i])), __builtin_fabsf(o[1][i]));
  if (__builtin_expect(__any(!(big < 1.0e37f) || (hh == 1 && !(o[0][8] < 9.0e18f && o[1][8] < 9.0e18f))), 0)) {
#pragma unroll
    for (int qt = 0; qt < 2; ++qt) {
      o[qt] = Z;
#pragma unroll 1
      for (unsigned so = so0; so < so0 + KQ * 64u; so += 2048u) {
        f32x16 x = qk32(LDP16(rk, l16 + 1024, so), qf[qt][1], qk32(LDP16(rk, l16, so), qf[qt][0], Z));
        const float mn = m[qt] + fmx(max16(x), 0.f), hi = rbf(mn), lo = rbf(mn - hi), me = hi + lo;
        const float delta = me - m[qt], alpha = __builtin_amdgcn_exp2f(-delta);
        m[qt] = me;
        if (hh == 0) qf[qt][1][2] = pack2(-hi, -lo);
#pragma unroll
        for (int i = 0; i < 16; ++i) { x[i] = __builtin_amdgcn_exp2f(x[i] - delta); o[qt][i] *= alpha; }
        o[qt] = pv32<1>(LDP16(rv, l16 + 1024, so), x, pv32<0>(LDP16(rv, l16, so), x, o[qt]));
      }
    }
  }
#pragma unroll
  for (int qt = 0; qt < 2; ++qt)
    if (kh != qt) {
#pragma unroll
      for (int i = 0; i < 12; ++i) mrg[kh][qt][i][lane] = o[qt][i];
      mrg[kh][qt][12][lane] = m[qt];
    }
  __syncthreads();
  if (kh < 2) {
    float mo = kh ? m[1] : m[0], mk[NKS - 1], mn = mo, v[12];
#pragma unroll
    for (int k = 0; k < NKS - 1; ++k) { mk[k] = mrg[k + (k >= kh)][kh][12][lane]; mn = fmaxf(mn, mk[k]); }
    const float a0 = __builtin_amdgcn_exp2f(mo - mn);
#pragma unroll
    for (int i = 0; i < 12; ++i) v[i] = (kh ? o[1][i] : o[0][i]) * a0;
#pragma unroll
    for (int k = 0; k < NKS - 1; ++k) {
      const float ak = __builtin_amdgcn_exp2f(mk[k] - mn);
#pragma unroll
      for (int i = 0; i < 12; ++i) v[i] += mrg[k + (k >= kh)][kh][i][lane] * ak;
    }
    auto lr = __builtin_amdgcn_permlane32_swap(f2u(v[8]), f2u(v[8]), false, false);
    const float inv = __builtin_amdgcn_rcpf(u2f(lr[1]));
    const int t = q0 + kh * 32 + r, n = hd * BD + 4 * hh;
    *(u32x2*)(O + OUT_IDX(t, n)) = (u32x2){pack2(v[0] * inv, v[1] * inv), pack2(v[2] * inv, v[3] * inv)};
    *(u32x2*)(O + OUT_IDX(t, n + 8)) = (u32x2){pack2(v[4] * inv, v[5] * inv), pack2(v[6] * inv, v[7] * inv)};
    if (hh == 0) *(u32x2*)(O + OUT_IDX(t, n + 16)) = (u32x2){pack2(v[8] * inv, v[9] * inv), pack2(v[10] * inv, v[11] * inv)};
  }
}

#define ATTB_LAUNCH(q, k, v, o, st) attb_kernel<<<BH * 8, 256 * NKS, 0, st>>>(q, k, v, o)
#define OP_SLP 6
struct OutArgs { const u16* of; const u16* wo; const u16* xr; u16* xb; u16* xf; u16* out; char* ex; };
constexpr int P_LD = 164;
__global__ __launch_bounds__(256, 1) void oproj_kernel(OutArgs a) {
  __shared__ __attribute__((aligned(16))) float part[4 * 32 * P_LD];
  __shared__ f32x2 red[8][32];
  const int tid = threadIdx.x, lane = tid & 63, w = __builtin_amdgcn_readfirstlane(tid >> 6), c = lane & 15, g = lane >> 4;
  const int s = (blockIdx.x & 7) + 8 * (blockIdx.x >> 5), cg = (blockIdx.x >> 3) & 3;
  const __amdgpu_buffer_rsrc_t ra = RSRC(a.of, S_ * DM * 2), rw = RSRC(a.wo, DM * DM * 2);
  u32x4 af[5][2], wf[5][10], xo[3];
#pragma unroll
  for (int k = 0; k < 5; ++k) {
#pragma unroll
    for (int b = 0; b < 2; ++b) af[k][b] = LDP16(ra, lane * 16, (unsigned)((2 * s + b) * 20 + 5 * w + k) * 1024u);
#pragma unroll
    for (int t = 0; t < 10; ++t) wf[k][t] = LDP16(rw, lane * 16, (unsigned)((10 * cg + t) * 20 + 5 * w + k) * 1024u);
  }
  const int tok = tid & 31;
  const size_t fo = (size_t)(2 * s + (tok >> 4)) * 10240 + (20 * cg + (tid >> 5)) * 128 + (tok & 15) * 8;
#pragma unroll
  for (int r = 0; r < 3; ++r) if (r < 2 || tid < 128) xo[r] = *(const u32x4*)(a.xr + fo + r * 1024);
  SB();
  f32x4 acc[10][2];
#pragma unroll
  for (int k = 0; k < 5; ++k)
#pragma unroll
    for (int t = 0; t < 10; ++t)
#pragma unroll
      for (int b = 0; b < 2; ++b)
        acc[t][b] = mfma32(__builtin_bit_cast(bf16x8, wf[k][t]), __builtin_bit_cast(bf16x8, af[k][b]), k ? acc[t][b] : (f32x4){0.f, 0.f, 0.f, 0.f});
#pragma unroll
  for (int t = 0; t < 10; ++t)
#pragma unroll
    for (int b = 0; b < 2; ++b) *(f32x4*)&part[(w * 32 + 16 * b + c) * P_LD + 16 * t + 4 * g] = acc[t][b];
  __syncthreads();
  float v[3][8], sm = 0.f, sq = 0.f;
#pragma unroll
  for (int r = 0; r < 3; ++r)
    if (r < 2 || tid < 128) {
      const float* q = part + tok * P_LD + (r * 8 + (tid >> 5)) * 8;
      f32x4 s0 = *(const f32x4*)q, s1 = *(const f32x4*)(q + 4);
#pragma unroll
      for (int k = 1; k < 4; ++k) { s0 += *(const f32x4*)(q + k * 32 * P_LD); s1 += *(const f32x4*)(q + k * 32 * P_LD + 4); }
      u32x4 o;
#pragma unroll
      for (int k = 0; k < 2; ++k) {
        o[k] = pack2(bf2f(xo[r][k] & 0xffff) + rbf(s0[2 * k]), bf2f(xo[r][k] >> 16) + rbf(s0[2 * k + 1]));
        o[2 + k] = pack2(bf2f(xo[r][2 + k] & 0xffff) + rbf(s1[2 * k]), bf2f(xo[r][2 + k] >> 16) + rbf(s1[2 * k + 1]));
      }
      if (a.out) *(u32x4*)(a.out + (size_t)(32 * s + tok) * DM + 160 * cg + (r * 8 + (tid >> 5)) * 8) = o;
      else *(u32x4*)(a.xb + fo + r * 1024) = o;
#pragma unroll
      for (int k = 0; k < 4; ++k) { v[r][2 * k] = bf2f(o[k] & 0xffff); v[r][2 * k + 1] = u2f(o[k] & 0xffff0000u); }
#pragma unroll
      for (int k = 0; k < 8; ++k) { sm += v[r][k]; sq += v[r][k] * v[r][k]; }
    }
  if (a.out) return;
  red[tid >> 5][tok] = (f32x2){sm, sq};
  __syncthreads();
  if (w == 0) {
    const __amdgpu_buffer_rsrc_t re = RSRC(a.ex, 64 * 4 * 256);
    f32x2 p = red[4 * (lane >> 5)][tok];
#pragma unroll
    for (int k = 1; k < 4; ++k) p += red[4 * (lane >> 5) + k][tok];
    p[0] += __shfl_xor(p[0], 32); p[1] += __shfl_xor(p[1], 32);
    const u32x2 mine = {min(f2u(p[0]), SENT - 1), min(f2u(p[1]), SENT - 1)};
    __builtin_amdgcn_raw_buffer_store_b64(mine, re, lane < 32 ? (unsigned)(cg * 256 + tok * 8) : OOB, (unsigned)s * 1024u, 16);
    u32x2 x[2];
#pragma unroll
    for (int k = 0; k < 2; ++k) x[k] = 2 * k + (lane >> 5) == cg ? mine : (u32x2){SENT, SENT};
    __builtin_amdgcn_s_sleep(OP_SLP);
    for (unsigned spins = 0; spins < SPIN_CAP; ++spins) {
      u32x2 y[2]; bool nd[2];
#pragma unroll
      for (int k = 0; k < 2; ++k) { nd[k] = max(x[k][0], x[k][1]) == SENT; y[k] = LD8(re, nd[k] ? (unsigned)(k * 512 + lane * 8) : OOB, (unsigned)s * 1024u); }
#pragma unroll
      for (int k = 0; k < 2; ++k) if (nd[k]) x[k] = y[k];
      if (!__any(mx3(x[0][0], x[0][1], max(x[1][0], x[1][1])) == SENT)) break;
      __builtin_amdgcn_s_sleep(2);
      asm volatile("" ::: "memory");
    }
    float ts = u2f(x[0][0]) + u2f(x[1][0]), tq = u2f(x[0][1]) + u2f(x[1][1]);
    ts += __shfl_xor(ts, 32); tq += __shfl_xor(tq, 32);
    const float mean = ts * (1.0f / DM), var = fmaxf(tq * (1.0f / DM) - mean * mean, 0.f);
    if (lane < 32) red[0][tok] = (f32x2){rbf(mean), 1.0f / rbf(sqrtf(rbf(rbf(var) + 1e-5f)))};
  }
  __syncthreads();
  const f32x2 st = red[0][tok];
#pragma unroll
  for (int r = 0; r < 3; ++r)
    if (r < 2 || tid < 128) {
      u32x4 o;
#pragma unroll
      for (int k = 0; k < 4; ++k) o[k] = pack2(rbf(v[r][2 * k] - st[0]) * st[1], rbf(v[r][2 * k + 1] - st[0]) * st[1]);
      *(u32x4*)(a.xf + fo + r * 1024) = o;
    }
}

#define P_TAIL 540
#define P_D0 100
#define P_G2C 700
#define P_G2T 240
#define P_DUR0 970
#define P_TOL 60
#define P_SLOW 900
extern "C" void kernel_launch(void* const* d_in, const int* in_sizes, int n_in, void* d_out, int out_size, void* d_ws,
                              size_t ws_size, hipStream_t stream) {
  const u16* A_x = (const u16*)d_in[0];
  const u16* A_mem0 = (const u16*)d_in[1];
  const u16* A_Wq = (const u16*)d_in[2];
  const u16* A_Wk = (const u16*)d_in[3];
  const u16* A_Wv = (const u16*)d_in[4];
  const u16* A_Wo = (const u16*)d_in[5];
  const u16* B_wqkv = (const u16*)d_in[8];
  const u16* B_wo = (const u16*)d_in[9];
  char* ws = (char*)(((uintptr_t)d_ws + ((size_t)2 << 20) - 1) & ~(uintptr_t)(((size_t)2 << 20) - 1));
  u16* WF = (u16*)(ws + OFF_WF); u16* UF = (u16*)(ws + OFF_UF);
  u16* WqkvBT = (u16*)(ws + OFF_WQKVBT); u16* WoBT = (u16*)(ws + OFF_WOBT);
  u16* xf = (u16*)(ws + OFF_XF); u16* xn = (u16*)(ws + OFF_XN); u16* xb = (u16*)(ws + OFF_XB);
  u16* Qb = (u16*)(ws + OFF_QB); u16* Kb = (u16*)(ws + OFF_KB); u16* VbT = (u16*)(ws + OFF_VBT); u16* Ob = (u16*)(ws + OFF_OB);
  {
    ProArgs pa{A_x, A_mem0, A_Wq, A_Wk, A_Wv, A_Wo, B_wqkv, B_wo, WF, UF, WqkvBT, WoBT, xf, Qb, ws};
    prologue_kernel<<<256, 704, 0, stream>>>(pa);
  }
  const u16* xcur = A_x;
  for (int layer = 0; layer < 2; ++layer) {
    {
      RecArgs r{WF, UF, xf, ws + OFF_QK, ws + OFF_Y, ws + OFF_Z, ws + OFF_PT, ws + (size_t)layer * SYNC_LAYER, ws + OFS_CAL + ROW, xcur, xn, WqkvBT, Qb, nullptr,
                layer, P_TAIL, P_D0, P_G2C, P_G2T, P_DUR0, P_TOL, P_SLOW};
      rec_kernel<<<256, 256, 0, stream>>>(r);
    }
    ATTB_LAUNCH(Qb, Kb, VbT, Ob, stream);
    {
      OutArgs o{Ob, WoBT, xn, xb, xf, layer ? (u16*)d_out : nullptr, ws + SYNC_LAYER + OFS_CAL};
      oproj_kernel<<<256, 256, 0, stream>>>(o);
      xcur = xb;
    }
  }
}
```

```cpp
#include <hip/hip_runtime.h>
#include <hip/hip_bf16.h>
#include <stdint.h>
#include <type_traits>
#include <utility>

typedef unsigned short u16;
typedef unsigned long long u64;
typedef __attribute__((ext_vector_type(8))) short bf16x8;
typedef __attribute__((ext_vector_type(4))) short bf16x4;
typedef __attribute__((ext_vector_type(4))) float f32x4;
typedef __attribute__((ext_vector_type(2))) float f32x2;
typedef __attribute__((ext_vector_type(16))) float f32x16;
typedef __attribute__((ext_vector_type(4))) unsigned u32x4;
typedef __attribute__((ext_vector_type(2))) unsigned u32x2;
#define GAS __attribute__((address_space(1)))
#define LAS __attribute__((address_space(3)))

constexpr int S_ = 2048, DM = 640;
constexpr int NH = 32, DH = 192, HD = NH * DH;
constexpr int MEM = 8, SEG = 32, NSEG = S_ / SEG;
constexpr int BH = 32, BD = 20;

constexpr size_t al256(size_t x) { return (x + 255) & ~(size_t)255; }
constexpr unsigned ROW = 32768;
constexpr unsigned M_SLOT = 4 * ROW;
constexpr unsigned OFS_P = 16 * ROW;
constexpr unsigned P_SLOT = 5 * ROW;
constexpr unsigned OFS_CAL = 26 * ROW;
constexpr unsigned OFS_CW = 28 * ROW;
constexpr unsigned CW_JP = 0;
constexpr unsigned CW_YC = CW_JP + 48 * 128;
constexpr unsigned CW_SS = CW_YC + 68 * 128;
constexpr unsigned CW_SO = CW_SS + 64 * 128;
constexpr unsigned CW_TQ = CW_SO + 512 * 128;
constexpr unsigned CW_TC = CW_TQ + 8 * 128;
constexpr unsigned CW_END = CW_TC + 64 * 128;
static_assert(CW_END <= 4 * ROW, "control words");
constexpr unsigned SYNC_LAYER = 32 * ROW;
constexpr size_t OFF_WF = 2 * (size_t)SYNC_LAYER;
constexpr size_t OFF_WOF = OFF_WF + (size_t)1152 * 20480;
constexpr size_t OFF_UF = OFF_WOF + (size_t)NH * 240 * 1024;
constexpr size_t OFF_WQKVBT = OFF_UF + (size_t)80 * 16 * 20480;
constexpr size_t OFF_WOBT = OFF_WQKVBT + al256((size_t)3 * DM * DM * 2);
constexpr size_t OFF_XN = OFF_WOBT + al256((size_t)DM * DM * 2);
constexpr unsigned QK_IMG = NSEG * NH * 12 * 1024;
constexpr size_t OFF_QK = OFF_XN + al256((size_t)S_ * DM * 2);
constexpr unsigned Y_IMG = NSEG * NH * 40 * 1024;
constexpr size_t OFF_Y = OFF_QK + 2 * (size_t)QK_IMG;
constexpr unsigned Z_IMG = NSEG * 40 * NH * 256;
constexpr size_t OFF_Z = OFF_Y + Y_IMG;
constexpr unsigned PT_M = NH * 2048, PT_SEG = PT_M + 2 * NH * 256, PT_IMG = NSEG * PT_SEG;
constexpr size_t OFF_PT = OFF_Z + Z_IMG;
constexpr size_t OFF_QB = OFF_PT + PT_IMG;
constexpr size_t OFF_KB = OFF_QB + al256((size_t)BH * S_ * 32 * 2);
constexpr size_t OFF_VBT = OFF_KB + al256((size_t)BH * S_ * 32 * 2);
constexpr size_t WS_TOTAL = OFF_VBT + al256((size_t)BH * S_ * 32 * 2);
constexpr size_t OFF_OB = OFF_QK;
static_assert(WS_TOTAL + ((size_t)2 << 20) <= ((size_t)256 << 20), "workspace budget");

__device__ __forceinline__ float u2f(unsigned v) { return __builtin_bit_cast(float, v); }
__device__ __forceinline__ unsigned f2u(float v) { return __builtin_bit_cast(unsigned, v); }
__device__ __forceinline__ float bf2f(unsigned v) { return __builtin_bit_cast(float, v << 16); }
__device__ __forceinline__ unsigned f2bf(float f) { __bf16 b = (__bf16)f; return (unsigned)__builtin_bit_cast(u16, b); }
__device__ __forceinline__ float rbf(float f) { return bf2f(f2bf(f)); }
typedef __attribute__((ext_vector_type(2))) __bf16 hbf16x2;
__device__ __forceinline__ unsigned pack2(float lo, float hi) { f32x2 v = {lo, hi}; hbf16x2 b = __builtin_convertvector(v, hbf16x2); return __builtin_bit_cast(unsigned, b); }
__device__ __forceinline__ f32x4 mfma32(bf16x8 a, bf16x8 b, f32x4 c) { return __builtin_amdgcn_mfma_f32_16x16x32_bf16(a, b, c, 0, 0, 0); }
__device__ __forceinline__ f32x4 mfma16(bf16x4 a, bf16x4 b, f32x4 c) { return __builtin_amdgcn_mfma_f32_16x16x16bf16_1k(a, b, c, 0, 0, 0); }
typedef __attribute__((ext_vector_type(8))) unsigned u32x8;
typedef __attribute__((ext_vector_type(8))) __bf16 hbf16x8;
typedef __attribute__((ext_vector_type(16))) __bf16 hbf16x16;
template <typename R>
__device__ __forceinline__ void store16_(u32x4 v, R r, unsigned vo, unsigned so, bool sc1) {
  if (sc1) __builtin_amdgcn_raw_buffer_store_b128(v, r, vo, so, 16); else __builtin_amdgcn_raw_buffer_store_b128(v, r, vo, so, 0);
  asm volatile("s_nop 1" :: "v"(v));
}
#define store16(v, r, vo, so, aux) store16_((v), (r), (vo), (so), (aux) != 0)
__device__ __forceinline__ bf16x4 mk4(unsigned lo, unsigned hi) { u32x2 v = {lo, hi}; return __builtin_bit_cast(bf16x4, v); }
__device__ __forceinline__ bf16x8 mk8(unsigned a, unsigned b, unsigned c, unsigned d) { u32x4 v = {a, b, c, d}; return __builtin_bit_cast(bf16x8, v); }
__device__ __forceinline__ unsigned dpp_ror8(unsigned v) { return (unsigned)__builtin_amdgcn_update_dpp(0, (int)v, 0x128, 0xf, 0xf, false); }

__device__ __forceinline__ bf16x4 lds_tr(const u16* p) { return __builtin_amdgcn_ds_read_tr16_b64_v4i16((LAS bf16x4*)p); }
__device__ __forceinline__ bf16x8 cat8(bf16x4 a, bf16x4 b) { return __builtin_shufflevector(a, b, 0, 1, 2, 3, 4, 5, 6, 7); }
__device__ __forceinline__ unsigned rtc() { return (unsigned)__builtin_amdgcn_s_memrealtime(); }
__device__ __forceinline__ unsigned mtc() { return (unsigned)__builtin_amdgcn_s_memtime(); }
constexpr unsigned OOB = 0x80000000u;
constexpr unsigned FAR = 0x40000000u;
constexpr unsigned SENT = 0xffffffffu;
#define WG_BARRIER() do { asm volatile("s_waitcnt lgkmcnt(0)" ::: "memory"); __builtin_amdgcn_s_barrier(); asm volatile("" ::: "memory"); } while (0)
struct ProArgs { const u16* x; const u16* mem0; const u16* wq; const u16* wk; const u16* wv; const u16* wo; const u16* wb; const u16* wob;
                 u16* wf; u16* wof; u16* uf; u16* wbf; u16* wobf; u16* xf; u16* qb; char* sync; };
#define CAL_N 4
#define PRO_NT 1
#define PRO_ROT 0
#define PRO_LD(m, p) (((m) & PRO_NT) ? __builtin_nontemporal_load((const u32x4*)(p)) : *(const u32x4*)(p))
#define PRO_WT 4
template <int M> __device__ __forceinline__ void pro_st(u16* p, u32x4 v) {
  if (M == 0) *(u32x4*)p = v;
  else if (M == 1) asm volatile("global_store_dwordx4 %0, %1, off sc1\n\ts_nop 1" :: "v"(p), "v"(v) : "memory");
  else __builtin_nontemporal_store(v, (u32x4*)p);
}
constexpr int PA_LD = 200, PO_LD = 168, PT_LD = 136;
__global__ __launch_bounds__(704, 3) void prologue_kernel(ProArgs a) {
  __shared__ __attribute__((aligned(16))) u16 smem[192 * PO_LD + 320 * PT_LD];
  static_assert(10 * 16 * PA_LD <= 320 * PT_LD, "staging area");
  const int b = blockIdx.x, tid = threadIdx.x, lane = tid & 63, w = __builtin_amdgcn_readfirstlane(tid >> 6), c = lane & 15, g = lane >> 4;
  const __amdgpu_buffer_rsrc_t rs = __builtin_amdgcn_make_buffer_rsrc(a.sync, 0, (int)(2 * SYNC_LAYER), 0x00020000);
  if (w == 10) {
    const unsigned ln = OFS_CAL + ((b >> 3) & 7) * 4096u + ((b >> 6) & 1) * 2048u + ((b & 7) * 2 + (b >> 7)) * 128u;
    const u32x4 z = {0u, 0u, 0u, 0u};
    store16(z, rs, lane < 8 ? ln + lane * 16u : OOB, 0, 16);
    WG_BARRIER(); WG_BARRIER();
    unsigned best = 65535u;
#pragma unroll 1
    for (int r = 0; r < CAL_N; ++r) {
      unsigned t0, t1, x;
      asm volatile("s_waitcnt vmcnt(0)\n\ts_sleep 8\n\ts_memtime vcc\n\ts_waitcnt lgkmcnt(0)\n\ts_mov_b32 %0, vcc_lo\n\tbuffer_load_dword %2, %3, %4, 0 offen sc1\n\ts_waitcnt vmcnt(0)\n\t"
                   "s_memtime vcc\n\ts_waitcnt lgkmcnt(0)\n\ts_mov_b32 %1, vcc_lo" : "=&s"(t0), "=&s"(t1), "=&v"(x) : "v"(ln), "s"(rs) : "vcc", "memory");
      best = min(best, t1 - t0);
      if (r + 1 < CAL_N) store16(z, rs, lane < 8 ? ln + lane * 16u : OOB, 0, 16);
    }
    unsigned* tab = (unsigned*)(a.sync + OFS_CAL + ROW);
    if (lane == 0) { tab[b] = best; if (b < 8) tab[256 + b] = (unsigned)__builtin_amdgcn_s_getreg(20 | (3 << 11)) & 7u; }
    return;
  }
#define PT_(k)
  PT_(0);
  const int h = (b & 7) * 4 + (b >> 6), dh = (b >> 5) & 1, cg = (b >> 3) & 3;
  const int wk = PRO_ROT ? (w + (5 * cg >> 1)) % 10 : w, to = PRO_ROT ? tid + 320 * dh - (tid + 320 * dh >= 640 ? 640 : 0) : tid;
  u32x4 la[12], lo[6], lt[8], lx[2];
  const int tb = b < 96 ? b : b < 192 ? b - 96 : b < 222 ? b - 192 : b - 222, tld = b < 192 ? HD : b < 222 ? 3 * DM : DM;
  const u16* tsrc = (b < 96 ? a.wq : b < 192 ? a.wk : b < 222 ? a.wb : a.wob) + (size_t)(tb & 1) * 320 * tld + (tb >> 1) * 128;
  u16* tdst = (b < 96 ? a.wf : b < 192 ? a.wf + (size_t)HD * DM : b < 222 ? a.wbf : a.wobf) + (size_t)(tb >> 1) * 81920 + (tb & 1) * 5120;
  if (b < 232) {
#pragma unroll
    for (int i = 0; i < 8; ++i) lt[i] = PRO_LD(1, tsrc + (size_t)(i * 40 + (tid >> 4)) * tld + (tid & 15) * 8);
  }
#pragma unroll
  for (int i = 0; i < 6; ++i) { const int q = (to >> 6) * 384 + i * 64 + lane, r = q / 20; lo[i] = PRO_LD(2, a.wo + (size_t)(h * DH + r) * DM + 160 * cg + (q - r * 20) * 8); }
  const int row = b * 8 + (w & 7);
  if (w < 8) { lx[0] = *(const u32x4*)(a.x + (size_t)row * DM + lane * 8); lx[1] = *(const u32x4*)(a.x + (size_t)row * DM + 512 + (lane & 15) * 8); }
#pragma unroll
  for (int i = 0; i < 12; ++i) {
    const int q = (i % 6) * 64 + lane, r = q / 24;
    la[i] = PRO_LD(2, a.wv + (size_t)(320 * dh + 32 * wk + 8 * (r >> 2) + (r & 3) + 4 * (i / 6)) * HD + h * DH + (q - r * 24) * 8);
  }
  __builtin_amdgcn_sched_barrier(0);
  if (tid < 512) {
    const unsigned off = (unsigned)b * 8192u + tid * 16u, o = off & (SYNC_LAYER - 1), rr = o >> 15, in = o & 4095u;
    u32x4 z = {SENT, SENT, SENT, SENT};
    if (o >= OFS_CW) z = (u32x4){0u, 0u, 0u, 0u};
    else if (rr < 4 && (!(rr & 1) || in < 1024u)) {
      const int L = 5 * (rr >> 1) + 4 * (rr & 1) + (in >> 10), ln = (in & 1023u) >> 4;
      const unsigned* mr = (const unsigned*)(a.mem0 + (ln & 7) * DM + 64 * L + 16 * (ln >> 4) + 2 * ((ln >> 3) & 1));
      z = (u32x4){min(mr[0], SENT - 1), min(mr[2], SENT - 1), min(mr[4], SENT - 1), min(mr[6], SENT - 1)};
    }
    store16(z, rs, (off >= OFS_CAL && off < OFS_CW) ? OOB : off, 0, 16);
  }
  {
    u16* kb = a.qb + (size_t)BH * S_ * 32; u16* vb = kb + (size_t)BH * S_ * 32;
    if (tid < 256) {
      const size_t o = (size_t)(b * 256 + (tid & ~31)) * 32 + 512 + (tid & 31) * 8;
      *(u32x2*)(a.qb + o + 4) = (u32x2){0u, 0u}; *(u32x4*)(a.qb + o + 256) = (u32x4){0u, 0u, 0u, 0u};
      *(u32x2*)(kb + o + 4) = (u32x2){0x3f803f80u, 0u}; *(u32x4*)(kb + o + 256) = (u32x4){0u, 0u, 0u, 0u};
    } else {
      const int q = tid - 256, hb = q / 12, e = 20 + q - hb * 12;
      const unsigned f = e == 20 ? 0x3f803f80u : 0u;
      *(u32x4*)(vb + ((size_t)(b * 32 + hb) * 32 + e) * 8) = (u32x4){f, f, f, f};
    }
  }
  u16* st = smem + 192 * PO_LD;
  if (b < 232) {
#pragma unroll
    for (int i = 0; i < 8; ++i) *(u32x4*)(st + (i * 40 + (tid >> 4)) * PT_LD + (tid & 15) * 8) = lt[i];
  }
  PT_(1);
  WG_BARRIER();
  if (b < 232) {
#pragma unroll
    for (int t = 0; t < 8; ++t) {
      const u16* p = st + (32 * w + 8 * g + (c >> 2)) * PT_LD + 16 * t + 4 * (c & 3);
      pro_st<PRO_WT & 3>(tdst + (t * 20 + w) * 512 + lane * 8, __builtin_bit_cast(u32x4, cat8(lds_tr(p), lds_tr(p + 4 * PT_LD))));
    }
  }
#pragma unroll
  for (int i = 0; i < 6; ++i) {
    const int q = (to >> 6) * 384 + i * 64 + lane, r = q / 20, ch = q - r * 20;
    u16* p = smem + r * PO_LD + (ch >> 1) * 16 + (ch & 1) * 4;
    *(u32x2*)p = (u32x2){lo[i][0], lo[i][2]}; *(u32x2*)(p + 8) = (u32x2){lo[i][1], lo[i][3]};
  }
  if (w < 8) {
    const bool two = lane < 16;
    float v[16], sm = 0.f, q = 0.f;
#pragma unroll
    for (int i = 0; i < 8; ++i) { const unsigned u = (i < 4 || two) ? lx[i >> 2][i & 3] : 0u; v[2 * i] = bf2f(u & 0xffff); v[2 * i + 1] = bf2f(u >> 16); sm += v[2 * i] + v[2 * i + 1]; }
#pragma unroll
    for (int o = 32; o >= 1; o >>= 1) sm += __shfl_xor(sm, o);
    const float mean = sm * (1.0f / DM);
#pragma unroll
    for (int i = 0; i < 16; ++i) { const float d = (i < 8 || two) ? v[i] - mean : 0.f; q += d * d; }
#pragma unroll
    for (int o = 32; o >= 1; o >>= 1) q += __shfl_xor(q, o);
    const float mb = rbf(mean), den = rbf(sqrtf(rbf(rbf(q * (1.0f / DM)) + 1e-5f)));
#pragma unroll
    for (int hf = 0; hf < 2; ++hf)
      if (hf == 0 || two) {
        u32x4 o;
#pragma unroll
        for (int i = 0; i < 4; ++i) o[i] = pack2(rbf(v[hf * 8 + 2 * i] - mb) / den, rbf(v[hf * 8 + 2 * i + 1] - mb) / den);
        *(u32x4*)(a.xf + (size_t)(row >> 4) * 10240 + (hf * 64 + lane) * 128 + (row & 15) * 8) = o;
      }
  }
  bf16x4 vt[2][3];
  bf16x8 af[2][6];
#pragma unroll
  for (int hf = 0; hf < 2; ++hf) {
    u16* pa = st + 32 * w * PT_LD;
    asm volatile("s_waitcnt lgkmcnt(0)" ::: "memory");
#pragma unroll
    for (int i = 0; i < 6; ++i) { const int q = i * 64 + lane, r = q / 24; *(u32x4*)(pa + r * PA_LD + (q - r * 24) * 8) = la[6 * hf + i]; }
    asm volatile("s_waitcnt lgkmcnt(0)" ::: "memory");
#pragma unroll
    for (int k = 0; k < 6; ++k) af[hf][k] = *(const bf16x8*)(pa + c * PA_LD + k * 32 + g * 8);
#pragma unroll
    for (int k = 0; k < 3; ++k) vt[hf][k] = lds_tr(pa + (4 * g + (c >> 2)) * PA_LD + 16 * (3 * cg + k) + 4 * (c & 3));
  }
#pragma unroll
  for (int k = 0; k < 3; ++k) *(bf16x8*)(a.wf + ((size_t)(768 + h * 12 + 3 * cg + k) * 20 + 10 * dh + wk) * 512 + lane * 8) = cat8(vt[0][k], vt[1][k]);
  WG_BARRIER();
  PT_(2);
#pragma unroll
  for (int k = 0; k < 3; ++k) {
    const u16* p = smem + (32 * (3 * dh + k) + 4 * g + (c >> 2)) * PO_LD + 16 * w + 4 * (c & 3);
    *(bf16x8*)(a.wof + ((size_t)(h * 40 + 10 * cg + w) * 6 + 3 * dh + k) * 512 + lane * 8) = cat8(lds_tr(p), lds_tr(p + 16 * PO_LD));
  }
  u16* dst = a.uf + ((size_t)((20 * cg + (c >> 3)) * 16 + (h >> 1)) * 20 + 10 * dh + wk) * 512 + (g * 16 + (h & 1) * 8 + (c & 7)) * 8;
  const u16* bp = smem + (8 * g + (c >> 2)) * PO_LD + 4 * (c & 3);
#pragma unroll 2
  for (int t = 0; t < 10; ++t) {
    f32x4 d0 = {0.f, 0.f, 0.f, 0.f}, d1 = {0.f, 0.f, 0.f, 0.f};
#pragma unroll
    for (int kp = 0; kp < 6; ++kp) {
      const bf16x8 bw = cat8(lds_tr(bp + 32 * kp * PO_LD + 16 * t), lds_tr(bp + (32 * kp + 4) * PO_LD + 16 * t));
      d0 = mfma32(af[0][kp], bw, d0); d1 = mfma32(af[1][kp], bw, d1);
    }
    pro_st<(PRO_WT >> 2) & 3>(dst + (size_t)t * 2 * 16 * 20 * 512, (u32x4){pack2(d0[0], d0[1]), pack2(d0[2], d0[3]), pack2(d1[0], d1[1]), pack2(d1[2], d1[3])});
  }
  PT_(3);
  PT_(4);
}

struct RecArgs {
  const u16* wf; const u16* wof; const u16* uf; const u16* xf; char* qk; char* y; char* z; char* pt; char* sync; const char* cal;
  const u16* xold; u16* xr; const u16* wb; u16* qkvb; unsigned long long* dbg;
  int layer, tail, d0, g2c, g2t, dur0, tol, slow;
};
template <typename F, int... I> __device__ __forceinline__ void static_for_(F&& f, std::integer_sequence<int, I...>) { (f(std::integral_constant<int, I>()), ...); }
template <int N, typename F> __device__ __forceinline__ void static_for(F&& f) { static_for_(f, std::make_integer_sequence<int, N>()); }
#define RLX_AGENT __ATOMIC_RELAXED, __HIP_MEMORY_SCOPE_AGENT
#define SPIN_LOG 20
constexpr unsigned SPIN_CAP = 1u << SPIN_LOG;
#define PIN_A(x) asm volatile("" : "+a"(x))
#define PIN_V(x) asm volatile("" : "+v"(x))
#define PIN_S(x) do { (x) = __builtin_amdgcn_readfirstlane(x); } while (0)
#define SB() __builtin_amdgcn_sched_barrier(0)
#define NOPS(n) do { SB(); asm volatile("s_nop %0" :: "n"(n)); SB(); } while (0)
#define RFL(x) ((unsigned)__builtin_amdgcn_readfirstlane(x))
template <int CTRL> __device__ __forceinline__ unsigned dppu(unsigned v) { return (unsigned)__builtin_amdgcn_update_dpp(0, (int)v, CTRL, 0xf, 0xf, true); }
template <int CTRL> __device__ __forceinline__ float dppf(float v) { return u2f(dppu<CTRL>(f2u(v))); }
template <int BANKS> __device__ __forceinline__ unsigned ror8_banks(unsigned v) { return (unsigned)__builtin_amdgcn_update_dpp((int)v, (int)v, 0x128, 0xf, BANKS, false); }
constexpr int ROR1 = 0x121, ROR2 = 0x122, ROR4 = 0x124, ROR8 = 0x128, SHR8 = 0x118;
__device__ __forceinline__ float fmx(float a, float b) { return __builtin_elementwise_maximum(a, b); }
__device__ __forceinline__ unsigned mx3(unsigned a, unsigned b, unsigned c) { return max(max(a, b), c); }
__device__ __forceinline__ unsigned mx4(u32x4 v) { return max(mx3(v[0], v[1], v[2]), v[3]); }
__device__ __forceinline__ u32x8 cat2(u32x4 lo, u32x4 hi) { return __builtin_shufflevector(lo, hi, 0, 1, 2, 3, 4, 5, 6, 7); }
__device__ __forceinline__ u32x4 lo4(u32x8 v) { return __builtin_shufflevector(v, v, 0, 1, 2, 3); }
__device__ __forceinline__ u32x4 hi4(u32x8 v) { return __builtin_shufflevector(v, v, 4, 5, 6, 7); }
__device__ __forceinline__ u32x2 lo2(u32x4 v) { return __builtin_shufflevector(v, v, 0, 1); }
__device__ __forceinline__ u32x2 hi2(u32x4 v) { return __builtin_shufflevector(v, v, 2, 3); }
__device__ __forceinline__ u32x4 pack8(f32x4 a, f32x4 b) { return (u32x4){pack2(a[0], a[1]), pack2(a[2], a[3]), pack2(b[0], b[1]), pack2(b[2], b[3])}; }
#define DEV __device__ __forceinline__
DEV void smf_v(f32x4& acc, u32x4 a, u32x8 b, int idx) { asm volatile("v_smfmac_f32_16x16x64_bf16 %0, %1, %2, %3" : "+v"(acc) : "v"(a), "v"(b), "v"(idx)); }
template <int R> DEV void smf_a(f32x4& acc, u32x4 a, int idx) { asm volatile("v_smfmac_f32_16x16x64_bf16 %0, %1, a[%c3:%c4], %2" : "+v"(acc) : "v"(a), "v"(idx), "n"(R), "n"(R + 7)); }
template <int R> DEV void mf32_ba(f32x4& acc, u32x4 a) { asm volatile("v_mfma_f32_16x16x32_bf16 %0, %1, a[%c2:%c3], %0" : "+v"(acc) : "v"(a), "n"(R), "n"(R + 3)); }
template <int R> DEV void mf32z_ba(f32x4& d, u32x4 a) { asm volatile("v_mfma_f32_16x16x32_bf16 %0, %1, a[%c2:%c3], 0" : "=&v"(d) : "v"(a), "n"(R), "n"(R + 3)); }
template <int R> DEV void mf32_aa(f32x4& acc, u32x4 b) { asm volatile("v_mfma_f32_16x16x32_bf16 %0, a[%c2:%c3], %1, %0" : "+v"(acc) : "v"(b), "n"(R), "n"(R + 3)); }
template <int R> DEV void mf32z_aa(f32x4& d, u32x4 b) { asm volatile("v_mfma_f32_16x16x32_bf16 %0, a[%c2:%c3], %1, 0" : "=&v"(d) : "v"(b), "n"(R), "n"(R + 3)); }
template <int R> DEV void ld_a(__amdgpu_buffer_rsrc_t r, unsigned vo, unsigned so) { asm volatile("buffer_load_dwordx4 a[%c3:%c4], %0, %1, %2 offen" :: "v"(vo), "s"(r), "s"(so), "n"(R), "n"(R + 3) : "memory"); }
template <int R> DEV void ld_a1(__amdgpu_buffer_rsrc_t r, unsigned vo, unsigned so) { asm volatile("buffer_load_dwordx4 a[%c3:%c4], %0, %1, %2 offen sc1" :: "v"(vo), "s"(r), "s"(so), "n"(R), "n"(R + 3) : "memory"); }
DEV void mf32(f32x4& acc, u32x4 a, u32x4 b) { asm volatile("v_mfma_f32_16x16x32_bf16 %0, %1, %2, %0" : "+v"(acc) : "v"(a), "v"(b)); }
DEV void mf32z(f32x4& d, u32x4 a, u32x4 b) { asm volatile("v_mfma_f32_16x16x32_bf16 %0, %1, %2, 0" : "=&v"(d) : "v"(a), "v"(b)); }
DEV void mf32_n(f32x4& acc, u32x4 a, u32x4 b) { asm volatile("s_nop 1\n\tv_mfma_f32_16x16x32_bf16 %0, %1, %2, %0" : "+v"(acc) : "v"(a), "v"(b)); }
DEV void mf32z_n(f32x4& d, u32x4 a, u32x4 b) { asm volatile("s_nop 1\n\tv_mfma_f32_16x16x32_bf16 %0, %1, %2, 0" : "=&v"(d) : "v"(a), "v"(b)); }
DEV void mf16(f32x4& acc, u32x2 a, u32x2 b) { asm volatile("v_mfma_f32_16x16x16_bf16 %0, %1, %2, %0" : "+v"(acc) : "v"(a), "v"(b)); }
DEV void mf16z(f32x4& d, u32x2 a, u32x2 b) { asm volatile("v_mfma_f32_16x16x16_bf16 %0, %1, %2, 0" : "=&v"(d) : "v"(a), "v"(b)); }
DEV void zero4(f32x4& d) { f32x2 x, y; asm volatile("v_mov_b64 %0, 0\n\tv_mov_b64 %1, 0" : "=v"(x), "=v"(y)); d = __builtin_shufflevector(x, y, 0, 1, 2, 3); asm volatile("" : "+v"(d)); }
DEV void clock_now(u64& t) { asm volatile("s_memtime %0" : "=s"(t)); }
DEV void zero2(f32x2& x) { asm volatile("v_mov_b64 %0, 0" : "=v"(x)); }
DEV int fresh_lane() { int l; asm volatile("v_mbcnt_lo_u32_b32 %0, -1, 0\n\tv_mbcnt_hi_u32_b32 %0, -1, %0" : "=v"(l)); __builtin_assume(l >= 0 && l < 64); return l; }
DEV void keep_v(unsigned x) { asm volatile("" :: "v"(x)); }
DEV void set_m0(unsigned v) { asm volatile("s_mov_b32 m0, %0" :: "s"(v) : "memory"); }
template <int IMM> DEV void dma1k_m0(__amdgpu_buffer_rsrc_t r, unsigned vo, unsigned so) { asm volatile("buffer_load_dwordx4 %0, %1, %2 offen offset:%c3 lds" :: "v"(vo), "s"(r), "s"(so), "n"(IMM) : "memory"); }
DEV void keep4(u32x4 x) { asm volatile("s_nop 1" :: "v"(x)); }
DEV void pin_s(unsigned& x) { asm volatile("" : "+s"(x)); }
template <typename T> DEV void pin_v(T& x) { asm volatile("" : "+v"(x)); }
#define AGPR_FENCE() asm volatile("" ::: \
    "a0", "a1", "a2", "a3", "a4", "a5", "a6", "a7", "a8", "a9", "a10", "a11", "a12", "a13", "a14", "a15", "a16", "a17", "a18", "a19", "a20", "a21", "a22", \
    "a23", "a24", "a25", "a26", "a27", "a28", "a29", "a30", "a31", "a32", "a33", "a34", "a35", "a36", "a37", "a38", "a39", "a40", "a41", "a42", "a43", \
    "a44", "a45", "a46", "a47", "a48", "a49", "a50", "a51", "a52", "a53", "a54", "a55", "a56", "a57", "a58", "a59", "a60", "a61", "a62", "a63", "a64", \
    "a65", "a66", "a67", "a68", "a69", "a70", "a71", "a72", "a73", "a74", "a75", "a76", "a77", "a78", "a79", "a80", "a81", "a82", "a83", "a84", "a85", \
    "a86", "a87", "a88", "a89", "a90", "a91", "a92", "a93", "a94", "a95", "a96", "a97", "a98", "a99", "a100", "a101", "a102", "a103", "a104", "a105", \
    "a106", "a107", "a108", "a109", "a110", "a111", "a112", "a113", "a114", "a115", "a116", "a117", "a118", "a119", "a120", "a121", "a122", "a123", \
    "a124", "a125", "a126", "a127", "a128", "a129", "a130", "a131", "a132", "a133", "a134", "a135", "a136", "a137", "a138", "a139", "a140", "a141", \
    "a142", "a143", "a144", "a145", "a146", "a147", "a148", "a149", "a150", "a151", "a152", "a153", "a154", "a155", "a156", "a157", "a158", "a159", \
    "a160", "a161", "a162", "a163", "a164", "a165", "a166", "a167", "a168", "a169", "a170", "a171", "a172", "a173", "a174", "a175", "a176", "a177", \
    "a178", "a179", "a180", "a181", "a182", "a183", "a184", "a185", "a186", "a187", "a188", "a189", "a190", "a191", "a192", "a193", "a194", "a195", \
    "a196", "a197", "a198", "a199", "a200", "a201", "a202", "a203", "a204", "a205", "a206", "a207", "a208", "a209", "a210", "a211", "a212", "a213", \
    "a214", "a215", "a216", "a217", "a218", "a219", "a220", "a221", "a222", "a223", "a224", "a225", "a226", "a227", "a228", "a229", "a230", "a231", \
    "a232", "a233", "a234", "a235", "a236", "a237", "a238", "a239", "a240", "a241", "a242", "a243", "a244", "a245", "a246", "a247", "a248", "a249", \
    "a250", "a251", "a252", "a253", "a254", "a255")
#define LDP16(r, off, so) __builtin_amdgcn_raw_buffer_load_b128((r), (off), (so), 0)
#define VM_WAIT(n) asm volatile("s_waitcnt vmcnt(%0)" :: "n"(n) : "memory")
#define LD16(r, off, so) __builtin_amdgcn_raw_buffer_load_b128((r), (off), (so), 16)
#define LD8(r, off, so) __builtin_amdgcn_raw_buffer_load_b64((r), (off), (so), 16)
#define LD4(r, off, so) __builtin_amdgcn_raw_buffer_load_b32((r), (off), (so), 16)
#define ST16(v, r, off, so) store16((v), (r), (off), (so), 16)
DEV __amdgpu_buffer_rsrc_t rsrc_(const void* p, int n) { __amdgpu_buffer_rsrc_t r = __builtin_amdgcn_make_buffer_rsrc((void*)p, 0, n, 0x00020000); asm volatile("" : "+s"(r)); return r; }
#define RSRC(p, n) rsrc_((p), (int)(n))
#define LDS16(off) (*(LAS u32x4*)(lds + (off)))
template <int IMM> DEV void dma1k(__amdgpu_buffer_rsrc_t r, unsigned vo, unsigned so, unsigned ldsoff) {
  asm volatile("s_mov_b32 m0, %0\n\ts_nop 0\n\tbuffer_load_dwordx4 %1, %2, %3 offen offset:%c4 lds" :: "s"(ldsoff), "v"(vo), "s"(r), "s"(so), "n"(IMM) : "memory");
}

DEV int s_clamp(int v, int lim) { int r; asm("s_min_i32 %0, %1, %2\n\ts_max_i32 %0, %0, %3" : "=&s"(r) : "s"(v), "n"(lim), "n"(-lim) : "scc"); return r; }
DEV unsigned s_mulshr16(unsigned x, unsigned y) { unsigned r; asm("s_mul_i32 %0, %1, %2\n\ts_lshr_b32 %0, %0, 16" : "=s"(r) : "s"(x), "s"(y) : "scc"); return r; }
struct Clock {
  unsigned me, re, dm, dt, rho, sig;
  float fm, ft, fx, fy;
  DEV unsigned edge() {
    u64 m, r0, r1, r2, r3, r4, r5, r6, r7; unsigned n;
#define CNT_(k) "s_cmp_eq_u64 %" #k ", %1\n\ts_addc_u32 %9, %9, 0\n\t"
    asm volatile("s_memtime %0\n\ts_memrealtime %1\n\ts_nop 2\n\ts_memrealtime %2\n\ts_nop 2\n\ts_memrealtime %3\n\ts_nop 2\n\ts_memrealtime %4\n\ts_nop 2\n\t"
                 "s_memrealtime %5\n\ts_nop 2\n\ts_memrealtime %6\n\ts_nop 2\n\ts_memrealtime %7\n\ts_nop 2\n\ts_memrealtime %8\n\t"
                 "s_mov_b32 %9, 0\n\ts_waitcnt lgkmcnt(0)\n\t" CNT_(2) CNT_(3) CNT_(4) CNT_(5) CNT_(6) CNT_(7) CNT_(8)
                 : "=&s"(m), "=&s"(r0), "=&s"(r1), "=&s"(r2), "=&s"(r3), "=&s"(r4), "=&s"(r5), "=&s"(r6), "=&s"(r7), "=&s"(n) :: "scc", "memory");
    take(m, r0, n);
    return (unsigned)m;
  }
  DEV void take(u64 m, u64 r0, unsigned n) {
    const unsigned m1 = (unsigned)m + 16u * n + 12u, t1 = ((unsigned)r0 + 4u) * 8u;
    dm = m1 - me; dt = t1 - re; me = m1; re = t1;
  }
  DEV void edge_z(f32x4 (&z)[4], u32x4 x0, u32x4 x1, u32x8 u0, u32x8 u1, int idx) {
    u64 m, r0, r1, r2, r3, r4, r5, r6, r7; unsigned n;
#define EZ_(acc, x, b) "v_smfmac_f32_16x16x64_bf16 %" #acc ", %" #x ", " b ", %18\n\t"
    asm volatile(EZ_(10, 14, "a[64:71]") "s_memtime %0\n\ts_memrealtime %1\n\t" EZ_(11, 14, "a[144:151]") "s_nop 0\n\ts_memrealtime %2\n\t" EZ_(12, 14, "a[224:231]") "s_nop 0\n\ts_memrealtime %3\n\t"
                 EZ_(13, 14, "%16") "s_nop 0\n\ts_memrealtime %4\n\t" EZ_(10, 15, "a[72:79]") "s_nop 0\n\ts_memrealtime %5\n\t" EZ_(11, 15, "a[152:159]") "s_nop 0\n\ts_memrealtime %6\n\t"
                 EZ_(12, 15, "a[232:239]") "s_nop 0\n\ts_memrealtime %7\n\t" EZ_(13, 15, "%17") "s_nop 0\n\ts_memrealtime %8\n\t"
                 "s_mov_b32 %9, 0\n\ts_waitcnt lgkmcnt(0)\n\t" CNT_(2) CNT_(3) CNT_(4) CNT_(5) CNT_(6) CNT_(7) CNT_(8)
                 : "=&s"(m), "=&s"(r0), "=&s"(r1), "=&s"(r2), "=&s"(r3), "=&s"(r4), "=&s"(r5), "=&s"(r6), "=&s"(r7), "=&s"(n), "+v"(z[0]), "+v"(z[1]), "+v"(z[2]), "+v"(z[3])
                 : "v"(x0), "v"(x1), "v"(u0), "v"(u1), "v"(idx) : "scc", "memory");
    take(m, r0, n);
  }
  template <int K> DEV void rate_step() {
    if (K == 0) { fm = (float)dm; pin_v(fm); }
    if (K == 1) { ft = (float)dt; pin_v(ft); }
    if (K == 2) { fy = __builtin_amdgcn_rcpf(fm); pin_v(fy); }
    if (K == 3) { fx = __builtin_amdgcn_rcpf(ft); pin_v(fx); }
    if (K == 4) { fm *= 2048.0f; pin_v(fm); }
    if (K == 5) { ft *= 65536.0f; pin_v(ft); }
    if (K == 6) { fm *= fx; pin_v(fm); }
    if (K == 7) { ft *= fy; pin_v(ft); }
    if (K == 8) { fm = u2f((unsigned)fm); pin_v(fm); }
    if (K == 9) { ft = u2f((unsigned)ft); pin_v(ft); }
    if (K == 10) { dm = RFL(f2u(fm)); asm volatile("" : "+s"(dm)); }
    if (K == 11) { dt = RFL(f2u(ft)); asm volatile("" : "+s"(dt)); }
    if (K == 12) { const bool ok = dm - 12u * 256u < 14u * 256u; rho = ok ? dm : rho; sig = ok ? dt : sig; asm volatile("" : "+s"(rho), "+s"(sig)); }
  }
  template <int K> DEV void rho_step() {
    if (K < 2) rate_step<K>(); else if (K < 4) rate_step<K + 1>(); else if (K < 7) rate_step<2 * K - 2>();
    else { rho = dm - 12u * 256u < 14u * 256u ? dm : rho; asm volatile("" : "+s"(rho)); }
  }
  DEV void rate() { static_for<13>([&](auto K) { rate_step<decltype(K)::value>(); }); }
  DEV void init() { rho = 22u * 256u; sig = 65536u * 8u / 22u; me = 0u; re = 0u; edge(); }
  DEV unsigned local(unsigned t) const { unsigned r = me + (unsigned)((s_clamp((int)(t - re), 100000) * (int)rho) >> 11); PIN_S(r); return r; }
  DEV unsigned global(unsigned m) const { int p = s_clamp((int)(m - me), 40000) * (int)sig; PIN_S(p); p >>= 16; PIN_S(p); return re + (unsigned)p; }
};
__device__ __forceinline__ void wait_until(unsigned tgt) {
  unsigned t;
  asm volatile("1:\n\ts_memtime vcc\n\ts_waitcnt lgkmcnt(0)\n\ts_sub_i32 %0, %1, vcc_lo\n\ts_sub_u32 vcc_hi, %0, 209\n\ts_cmp_lt_u32 vcc_hi, 60000\n\ts_cbranch_scc1 1b\n\t"
               "s_sub_i32 %0, %0, 112\n\ts_max_i32 %0, %0, 0\n\ts_min_i32 %0, %0, 96\n\ts_and_b32 %0, %0, 0x7c\n\t"
               "s_getpc_b64 vcc\n2:\n\ts_sub_u32 %0, 3f-2b, %0\n\ts_add_u32 vcc_lo, vcc_lo, %0\n\ts_addc_u32 vcc_hi, vcc_hi, 0\n\ts_setpc_b64 vcc\n\t"
               ".rept 24\n\ts_nop 0\n\t.endr\n3:" : "=&s"(t) : "s"(tgt) : "vcc", "scc", "memory");
}
__device__ __forceinline__ void coarse_until(unsigned tgt) {
  for (;;) { const unsigned rem = tgt - mtc(); if (rem - 1u >= 60000u) break; }
}
#define DUR_UP 16
#define DUR_DN 2
DEV int max32(int d) {
  d = max(d, (int)dppu<ROR8>(d)); d = max(d, (int)dppu<ROR4>(d)); d = max(d, (int)dppu<ROR2>(d)); d = max(d, (int)dppu<ROR1>(d));
  return max(__builtin_amdgcn_readlane(d, 0), __builtin_amdgcn_readlane(d, 16));
}
struct Sched {
  struct { unsigned d0, slow; int tol; } a;
  unsigned T, tP, dur, gap, more;
  DEV void init(unsigned t, const RecArgs& r) { T = t; tP = t; dur = r.dur0; gap = 0u; more = 0u; a.d0 = r.d0; a.slow = r.slow; a.tol = r.tol; asm volatile("" : "+s"(dur), "+s"(a.d0), "+s"(a.slow), "+s"(a.tol)); }
  DEV unsigned on_time() const { return tP + gap; }
  DEV unsigned limit() const { return T + dur + a.tol; }
  DEV void next(u32x2 an, bool start) {
    const int dm = max32((int)(an[0] - T)), gm = max32((int)an[1]), over = dm - (int)dur;
    const bool late = start || over > a.tol;
    const bool calm = more == 0u;
    more = late ? more + 150u : 0u;
    T = late ? T + (unsigned)dm + a.slow + more : tP + gap;
    int dd = late ? (start ? 0 : min(over >> 1, DUR_UP)) : max(over, calm ? over >> DUR_DN : 0);
    PIN_S(dd);
    dur += (unsigned)dd; gap = (unsigned)gm;
    tP = T + dur + a.d0;
    PIN_S(T); PIN_S(tP); PIN_S(dur); PIN_S(more); PIN_S(gap);
  }
};
template <typename R>
__device__ __forceinline__ u32x2 ann_valid(u32x2 an, R rs, unsigned vo, unsigned so, bool& dead) {
  if (__builtin_expect(__any(max(an[0], an[1]) == SENT), 0)) {
    for (unsigned spins = 0; !dead;) {
      __builtin_amdgcn_s_sleep(3);
      const bool need = max(an[0], an[1]) == SENT;
      const u32x2 n = LD8(rs, need ? vo : OOB, so);
      if (need) an = n;
      if (!__any(max(an[0], an[1]) == SENT)) break;
      if (++spins > SPIN_CAP) dead = true;
      asm volatile("" ::: "memory");
    }
  }
  return an;
}
template <typename R>
__device__ __forceinline__ void need_progress(R rs, unsigned vo, unsigned want, unsigned& have) {
  for (unsigned spins = 0; have < want && spins < SPIN_CAP; ++spins) {
    unsigned v = LD4(rs, vo, 0);
    v = min(v, dppu<0x4E>(v)); v = min(v, dppu<0xB1>(v));
    have = RFL(v);
    if (have < want) __builtin_amdgcn_s_sleep(8);
    asm volatile("" ::: "memory");
  }
}
struct Softmax {
  f32x4 s; float m, x;
  DEV void a() {
    float t;
    x = fmx(fmx(s[0], s[1]), s[2]); pin_v(x); x = fmx(fmx(x, s[3]), m);
    asm volatile("s_nop 1\n\tv_max_f32_dpp %0, %0, %0 row_ror:8 row_mask:0xf bank_mask:0xf\n\ts_nop 1\n\tv_max_f32_dpp %0, %0, %0 row_ror:4 row_mask:0xf bank_mask:0xf\n\t"
                 "v_mov_b32 %1, %0\n\ts_nop 1\n\tv_permlane32_swap_b32 %0, %1\n\tv_max_f32 %0, %0, %1\n\tv_mul_f32 %0, 0xbdd53b94, %0" : "+v"(x), "=&v"(t));
  }
  DEV void b() {
    const float C = 0.07216878364870322f * 1.4426950408889634f;
    s = __builtin_elementwise_fma(s, (f32x4){C, C, C, C}, (f32x4){x, x, x, x}); m = __builtin_fmaf(m, C, x);
#pragma unroll
    for (int i = 0; i < 4; ++i) s[i] = __builtin_amdgcn_exp2f(s[i]);
    m = __builtin_amdgcn_exp2f(m);
    pin_v(s); pin_v(m);
  }
  DEV void c() {
    float t;
    const f32x2 u = (f32x2){s[0], s[1]} + (f32x2){s[2], s[3]};
    x = (u[0] + m) + u[1];
    asm volatile("s_nop 1\n\tv_add_f32_dpp %0, %0, %0 row_ror:8 row_mask:0xf bank_mask:0xf\n\ts_nop 1\n\tv_add_f32_dpp %0, %0, %0 row_ror:4 row_mask:0xf bank_mask:0xf\n\t"
                 "v_mov_b32 %1, %0\n\ts_nop 1\n\tv_permlane32_swap_b32 %0, %1\n\tv_add_f32 %0, %0, %1\n\tv_rcp_f32 %0, %0" : "+v"(x), "=&v"(t));
  }
  DEV u32x4 all(unsigned& q) {
    unsigned o0, o1, o2, o3; float s0 = s[0], s1 = s[1], s2 = s[2], s3 = s[3], t, u;
#define DP_ " row_mask:0xf bank_mask:0x"
    asm volatile("v_max3_f32 %4, %6, %7, %8\n\tv_max3_f32 %4, %4, %9, %10\n\t"
                 "v_mul_f32 %6, 0x3dd53b94, %6\n\tv_mul_f32 %7, 0x3dd53b94, %7\n\tv_max_f32_dpp %4, %4, %4 row_ror:8" DP_ "f\n\t"
                 "v_mul_f32 %8, 0x3dd53b94, %8\n\tv_mul_f32 %9, 0x3dd53b94, %9\n\tv_max_f32_dpp %4, %4, %4 row_ror:4" DP_ "f\n\t"
                 "v_mov_b32 %5, %4\n\tv_mul_f32 %10, 0x3dd53b94, %10\n\ts_nop 0\n\tv_permlane32_swap_b32 %4, %5\n\tv_max_f32 %4, %4, %5\n\t"
                 "v_fmamk_f32 %6, %4, 0xbdd53b94, %6\n\tv_fmamk_f32 %7, %4, 0xbdd53b94, %7\n\tv_exp_f32 %6, %6\n\tv_fmamk_f32 %8, %4, 0xbdd53b94, %8\n\tv_exp_f32 %7, %7\n\t"
                 "v_fmamk_f32 %9, %4, 0xbdd53b94, %9\n\tv_exp_f32 %8, %8\n\tv_fmamk_f32 %10, %4, 0xbdd53b94, %10\n\tv_exp_f32 %9, %9\n\tv_add_f32 %4, %6, %7\n\tv_exp_f32 %10, %10\n\t"
                 "v_add_f32 %5, %8, %9\n\tv_add_f32 %4, %4, %10\n\tv_add_f32 %4, %4, %5\n\t"
                 "s_nop 1\n\tv_add_f32_dpp %4, %4, %4 row_ror:8" DP_ "f\n\ts_nop 1\n\tv_add_f32_dpp %4, %4, %4 row_ror:4" DP_ "f\n\t"
                 "v_mov_b32 %5, %4\n\ts_nop 1\n\tv_permlane32_swap_b32 %4, %5\n\tv_add_f32 %4, %4, %5\n\tv_rcp_f32 %4, %4\n\ts_nop 0\n\t"
                 "v_mul_f32 %6, %6, %4\n\tv_mul_f32 %7, %7, %4\n\tv_mul_f32 %8, %8, %4\n\tv_mul_f32 %9, %9, %4\n\t"
                 "v_cvt_pk_bf16_f32 %0, %6, |%7|\n\tv_cvt_pk_bf16_f32 %2, %6, |%7|\n\tv_cvt_pk_bf16_f32 %1, %8, |%9|\n\tv_cvt_pk_bf16_f32 %3, %8, |%9|\n\t"
                 "v_mov_b32_dpp %0, %0 row_ror:8" DP_ "c\n\tv_mov_b32_dpp %2, %2 row_ror:8" DP_ "3\n\tv_mov_b32_dpp %1, %1 row_ror:8" DP_ "c\n\tv_mov_b32_dpp %3, %3 row_ror:8" DP_ "3"
                 : "=&v"(o0), "=&v"(o1), "=&v"(o2), "=&v"(o3), "=&v"(x), "=&v"(t), "+v"(s0), "+v"(s1), "+v"(s2), "+v"(s3), "+v"(m));
    (void)u; q = 0u;
    return (u32x4){o0, o1, o2, o3};
  }
  DEV u32x4 mem() {
    unsigned o0, o1, o2, o3;
    asm volatile("v_mul_f32 %0, %4, %5\n\tv_mul_f32 %1, %4, %5\n\ts_nop 1\n\tv_permlane32_swap_b32 %0, %1\n\tv_cvt_pk_bf16_f32 %0, %0, |%1|\n\ts_nop 1\n\t"
                 "v_mov_b32_dpp %1, %0 row_shl:4" DP_ "f\n\tv_mov_b32_dpp %2, %0 row_shl:8" DP_ "f\n\tv_mov_b32_dpp %3, %0 row_shl:12" DP_ "f"
                 : "=&v"(o0), "=&v"(o1), "=&v"(o2), "=&v"(o3) : "v"(m), "v"(x));
    return (u32x4){o0, o1, o2, o3};
  }
  DEV u32x4 d() {
    unsigned o0, o1, o2, o3; float t, u;
    asm volatile("s_nop 0\n\tv_mul_f32 %4, %10, %11\n\tv_mul_f32 %5, %10, %11\n\tv_mul_f32 %0, %6, %11\n\tv_mul_f32 %2, %7, %11\n\tv_permlane32_swap_b32 %4, %5\n\t"
                 "v_mul_f32 %1, %8, %11\n\tv_mul_f32 %3, %9, %11\n\tv_cvt_pk_bf16_f32 %0, %0, |%2|\n\tv_cvt_pk_bf16_f32 %1, %1, |%3|\n\tv_cvt_pk_bf16_f32 %4, %4, |%5|\n\t"
                 "v_mov_b32_dpp %2, %0 row_ror:8 row_mask:0xf bank_mask:0xf\n\tv_mov_b32_dpp %3, %1 row_ror:8 row_mask:0xf bank_mask:0xf\n\t"
                 "v_mov_b32_dpp %0, %4 row_shr:8 row_mask:0xf bank_mask:0x4\n\tv_mov_b32_dpp %1, %4 row_shr:4 row_mask:0xf bank_mask:0x4\n\t"
                 "v_mov_b32_dpp %2, %4 quad_perm:[0,1,2,3] row_mask:0xf bank_mask:0x4\n\tv_mov_b32_dpp %3, %4 row_shl:4 row_mask:0xf bank_mask:0x4"
                 : "=&v"(o0), "=&v"(o1), "=&v"(o2), "=&v"(o3), "=&v"(t), "=&v"(u) : "v"(s[0]), "v"(s[1]), "v"(s[2]), "v"(s[3]), "v"(m), "v"(x));
    return (u32x4){o0, o1, o2, o3};
  }
};
#define TMARK(k)
#define STAMP(k)
#define STAMP_DECL(sh)
#define STAMP_FLUSH(s, ref, x0, x1)
#define STAMP_PUT(s)
#define STAMP_DUMP()
#define DBG_LDS 0

#define POS_OF(k) ((pos4 >> (3 * ((k) & 3))) & 7u)
#define AN_AT 880u
#define LATE_PAD 300u
#define HEAD 1u
#define S_FINE 0
#define PFQK 0
#define RETRY_SLP 0
#define EX_UP 16u
#define SM_W 2
constexpr int S_EX = 0, S_ET = S_EX + 4 * 2048, S_CT = S_ET + 4 * 5120, S_LW = S_CT + 48, S_NL = 13, S_END = S_LW + 4 * S_NL * 2048;
__host__ __device__ constexpr int s_q(int n) { return n < 30 ? n / 6 : n < 50 ? 5 + (n - 30) / 4 : 5 + (n - 50) / 2; }
__host__ __device__ constexpr int s_t(int n) { return n < 30 ? n % 6 : n < 50 ? (n - 30) % 4 : 4 + (n & 1); }
__host__ __device__ constexpr bool s_lds(int n) { return n % 4 == 3 && n / 4 < S_NL; }
__host__ __device__ constexpr int s_ri(int n) { return n - ((n + 1) / 4 < S_NL ? (n + 1) / 4 : S_NL); }
template <int kind>
__device__ __forceinline__ void score_role(const RecArgs& a, LAS char* lds, int rep, int il, unsigned posM, unsigned posP, unsigned pos4) {
  const int lane = fresh_lane(), w = __builtin_amdgcn_readfirstlane(threadIdx.x >> 6), c = lane & 15, g = lane >> 4, hf = c >> 3, i8 = c & 7;
  const int h = rep * 4 + il, iod = rep >> 2;
  const __amdgpu_buffer_rsrc_t rs = RSRC(a.sync, SYNC_LAYER), rq = RSRC(a.qk, 2 * QK_IMG), rp = RSRC(a.pt, PT_IMG);
  Clock ck;
  if (kind) ck.init();
  u32x8 WV[28 - S_NL];
  {
    const __amdgpu_buffer_rsrc_t rw = RSRC(a.wf, 1152 * 20480);
    const unsigned vo = lane * 16, wb = (unsigned)(h * 12 + 3 * w) * 20480u;
    static_for<60>([&](auto N) {
      constexpr int n = decltype(N)::value, q = s_q(n), t = s_t(n), ri = s_ri(n);
      const unsigned so = wb + (unsigned)((t & 1) * 384 + (t >> 1)) * 20480u + (unsigned)(q < 5 ? q + 5 * iod : q - 5 * iod) * 2048u;
      if (s_lds(n)) { LDS16(S_LW + (w * S_NL + n / 4) * 2048 + lane * 16) = LDP16(rw, vo, so); LDS16(S_LW + (w * S_NL + n / 4) * 2048 + 1024 + lane * 16) = LDP16(rw, vo + 1024, so); }
      else if (ri >= 32) WV[ri < 32 ? 0 : ri - 32] = cat2(LDP16(rw, vo, so), LDP16(rw, vo + 1024, so));
    });
    SB();
    static_for<60>([&](auto N) {
      constexpr int n = decltype(N)::value, q = s_q(n), t = s_t(n), ri = s_ri(n);
      const unsigned so = wb + (unsigned)((t & 1) * 384 + (t >> 1)) * 20480u + (unsigned)(q < 5 ? q + 5 * iod : q - 5 * iod) * 2048u;
      if (!s_lds(n) && ri < 32) { ld_a<8 * (ri & 31)>(rw, vo, so); ld_a<8 * (ri & 31) + 4>(rw, vo, so + 1024u); VM_WAIT(40); }
    });
    VM_WAIT(0);
#pragma unroll
    for (int i = 0; i < 28 - S_NL; ++i) PIN_V(WV[i]);
  }
  const int sidx = hf ? 0xEEEE : 0x4444;
  u32x4 idn;
#pragma unroll
  for (int jj = 0; jj < 4; ++jj) idn[jj] = (((c >> 2) & 1) == (g & 1) && jj == ((c & 3) >> 1) + 2 * hf) ? ((c & 1) ? 0x3f800000u : 0x3f80u) : 0u;
  const unsigned lomask = hf ? 0u : ~0u;
  unsigned l16 = lane * 16u;
  const unsigned an_ld = lane < 32 && lane != h ? ((lane >> 4) * 2 + 1) * ROW + posM * 4096u + 1024u + c * 128u : OOB;
  const unsigned an_st = lane < 32 ? ((h >> 4) * 2 + 1) * ROW + (POS_OF(lane >> 3) ^ 4u) * 4096u + 1024u + (h & 15) * 128u + i8 * 16u : OOB;
  const int sr = (lane & 3) | ((lane >> 2) & 4), sg = ((lane >> 2) & 1) | ((lane >> 4) & 2), sk = ((lane >> 1) & 6) | (lane >> 5), cq = 8 * (w & 1) + sr;
  const bool smem_lane = (lane & 44) == 8;
  const unsigned px_st = OFS_P + (h >> 3) * ROW + POS_OF(2 * w + hf) * 4096u + ((h >> 1) & 3) * 1024u + sg * 256u + (h & 1) * 128u + sr * 16u;
  const unsigned pm_st = (lane & 12) == 0 ? OFS_P + 4 * ROW + POS_OF(2 * w + (lane >> 5)) * 4096u + h * 128u + sr * 16u : OOB;
  const unsigned pt_st = !hf ? (unsigned)(h * 2048 + (w >> 1) * 1024 + (sg * 16 + cq) * 16) : smem_lane ? PT_M + ((w >> 1) * NH + h) * 256u + cq * 16u : OOB;
  const unsigned lw = S_LW + w * S_NL * 2048 + lane * 16;
  unsigned ex_w = S_EX + w * 2048 + lane * 16; PIN_V(ex_w);
  const unsigned et_w = S_ET + w * 5120 + lane * 16, et_r = S_ET + ((w >> 1) ? 1 + hf : 0) * 1024 + (sg * 16 + cq) * 16,
                 et_m = S_ET + (3 + (w >> 1)) * 1024 + ((2 + (sk >> 2)) * 16 + cq) * 16 + (sk & 3) * 4;
  const unsigned ex_r = S_EX + (sg * 16 + hf * 8 + sr) * 16, ex_m = S_EX + 1024 + ((2 + (sk >> 2)) * 16 + sr) * 16 + (sk & 3) * 4;
  const unsigned jp_vo = lane < 4 ? OFS_CW + CW_JP + (unsigned)((((lane & 1) * 384 + h * 12 + ((lane >> 1) & 1) * 11) >> 4) * 128) : OOB;
  const unsigned pf_vo = lane < 48 ? (lane >= 24 ? QK_IMG + (lane - 24) * 128u : lane * 128u) : OOB;
  unsigned jhave = 0, junk = 0;
  u32x4 kf[3], qf[3];
  auto load_qk = [&](unsigned so) {
#pragma unroll
    for (int p = 0; p < 3; ++p) { kf[p] = LD16(rq, l16 + p * 1024, so + QK_IMG); qf[p] = LD16(rq, l16 + p * 1024, so); }
  };
  TMARK(1);
  need_progress(rs, jp_vo, HEAD, jhave);
  TMARK(2);
  unsigned qk_so = (unsigned)(h * 12 + 3 * w) * 1024u;
  load_qk(qk_so);
  bool dead = false;
  unsigned extra = 0, p_tail = a.tail, p_g2t = a.g2t, p_g2c = a.g2c;
  if (kind == 1) { pin_s(p_tail); pin_s(p_g2t); pin_s(p_g2c); }
  Softmax tk; tk.s = (f32x4){0.f, 0.f, 0.f, 0.f}; tk.m = 0.f;
  Sched sd;
  if (kind) { ck.edge(); ck.rate(); }
  if (kind == 2) sd.init(ck.global(mtc()), a);
  if (kind == 2) LDS16(S_CT) = (u32x4){0u, 0u, 1u, 0u};
  unsigned mT = mtc();
  u32x2 an = {0u, 0u}; bool owed = false;
  const unsigned kA = posM * 4096u + iod * (2 * ROW), kB = posM * 4096u + (1 - iod) * (2 * ROW);
  unsigned sl = 0u, soA = kA, soB = kB, soA1 = kA + ROW, soB1 = kB + ROW, sp = 0u, jp_so = 0u, pt_so = FAR, pf_so = FAR;
  f32x4 acc[6];
  u32x4 tl[2], th[2];
  auto prime = [&]() {
    tl[0] = LDS16(lw); th[0] = LDS16(lw + 1024); tl[1] = LDS16(lw + 2048); th[1] = LDS16(lw + 3072);
#pragma unroll
    for (int t = 0; t < 6; ++t) zero4(acc[t]);
    SB();
  };
  prime();
  auto token_parts = [&](f32x4 (&e)[4], float (&em)[4]) {
#pragma unroll
    for (int k = 0; k < 4; ++k) e[k] = __builtin_bit_cast(f32x4, LDS16(et_r + k * 5120));
#pragma unroll
    for (int k = 0; k < 4; ++k) em[k] = *(LAS float*)(lds + et_m + k * 5120);
  };
  auto token_sums = [&](f32x4 (&e)[4], float (&em)[4]) {
    f32x4 es = (e[0] + e[1]) + (e[2] + e[3]);
    PIN_V(es);
#pragma unroll
    for (int r = 0; r < 4; ++r) tk.s[r] = 16 * hf + 4 * sg + r <= 8 * w + sr ? es[r] : -INFINITY;
    const f32x2 u = (f32x2){em[0], em[1]} + (f32x2){em[2], em[3]};
    tk.m = u[0] + u[1];
    pin_v(tk.s); pin_v(tk.m);
  };
  unsigned budget = SPIN_CAP;
  STAMP_DECL(0);
#pragma unroll 1
  for (int s = 0; s < NSEG; ++s) {
    PIN_V(l16);
    f32x4 dx, dm, tr, ts;
    u32x4 cm[10], ta, tc, bq; u32x2 X[3];
#define LDM(q) LD16(rs, l16 + ((q) % 5 < 4 ? ((q) % 5) * 1024 : 0), (q) < 4 ? soA : (q) == 4 ? soA1 : (q) < 9 ? soB : soB1)
#define POS(n) do { constexpr int q_ = s_q(n), t_ = s_t(n), ri_ = s_ri(n); \
      if (s_lds(n)) smf_v(acc[t_], cm[q_], cat2(tl[((n) / 4) & 1], th[((n) / 4) & 1]), sidx); \
      else if (ri_ < 32) smf_a<8 * (ri_ & 31)>(acc[t_], cm[q_], sidx); else smf_v(acc[t_], cm[q_], WV[ri_ < 32 ? 0 : ri_ - 32], sidx); \
      SB(); \
      if ((n) % 4 == 3 && (n) / 4 + 2 < S_NL) tl[((n) / 4) & 1] = LDS16(lw + ((n) / 4 + 2) * 2048); \
      if ((n) % 4 == 0 && (n) > 0 && (n) / 4 + 1 < S_NL) th[((n) / 4 + 1) & 1] = LDS16(lw + ((n) / 4 + 1) * 2048 + 1024); \
      SB(); } while (0)
#define CVT(dst, src, k) do { unsigned v_ = pack2((src)[2 * (k)], (src)[2 * (k) + 1]); PIN_V(v_); dst = v_; SB(); } while (0)
#define B_LO(p, k) do { unsigned v_ = X[p][k] & lomask; PIN_V(v_); bq[k] = v_; SB(); } while (0)
#define B_HI(p, k) do { unsigned v_ = dppu<SHR8>(X[p][k]); PIN_V(v_); bq[2 + (k)] = v_; SB(); } while (0)
    unsigned jpend; u64 tb1 = 0;
    SB();
    wait_until(mT);
    SB();
    STAMP(0);
    cm[0] = LDM(0); SB(); tk.a(); SB();
    cm[1] = LDM(1); SB(); tk.b(); SB();
    cm[2] = LDM(2); SB(); tk.c(); SB();
    cm[3] = LDM(3); SB();
    u32x4 ptk = tk.d(); PIN_V(ptk); SB();
    cm[4] = LDM(4); SB();
    unsigned chk = 0;
    static_for<48>([&](auto N) {
      constexpr int n = decltype(N)::value, q = s_q(n);
      POS(n);
      if (n == 0 && !S_FINE) STAMP(1);
      if constexpr (n % 4 == 2 && 5 + n / 4 < 10) cm[5 + n / 4] = LDM(5 + n / 4);
      SB();
      if (n == 1 || n == 9 || n == 13 || n == 21 || n == 25 || n == 32 || n == 36 || n == 40 || n == 44 || n == 47) { chk = mx3(chk, cm[q][0], cm[q][2]); pin_v(chk); }
      SB();
    });
    POS(48);
    u64 bad = __builtin_amdgcn_ballot_w64(chk == SENT); asm volatile("" : "+s"(bad)); SB();
    POS(49);
    if (__builtin_expect(bad != 0, 0)) {
      *(LAS unsigned*)(lds + S_CT + 8) = (unsigned)s + 1u;
      u64 miss;
      do {
        if (RETRY_SLP) __builtin_amdgcn_s_sleep(RETRY_SLP);
#pragma unroll
        for (int q = 0; q < 5; ++q) { cm[q] = LDM(q); NOPS(7); }
        prime();
        NOPS(1);
        unsigned m = 0;
        static_for<50>([&](auto N) {
          constexpr int n = decltype(N)::value, q = s_q(n);
          POS(n);
          if constexpr (n % 4 == 2 && 5 + n / 4 < 10) cm[5 + n / 4] = LDM(5 + n / 4);
          SB();
          if (n == 1 || n == 9 || n == 13 || n == 21 || n == 25 || n == 32 || n == 36 || n == 40 || n == 44 || n == 47) { m = mx3(m, cm[q][0], cm[q][2]); pin_v(m); }
          SB();
        });
        miss = __builtin_amdgcn_ballot_w64(m == SENT);
        asm volatile("s_sub_u32 %1, %1, 1\n\ts_cselect_b32 %1, 0, %1\n\ts_cselect_b64 %0, 0, %0" : "+s"(miss), "+s"(budget) :: "scc", "memory");
      } while (miss != 0);
    }
    if (!S_FINE) STAMP(2);
    CVT(ta[0], acc[0], 0);
    POS(50); asm volatile("" : "+v"(tl[0]), "+v"(th[0])); SB(); CVT(ta[1], acc[0], 1);
    POS(51); __builtin_amdgcn_raw_buffer_store_b128(ptk, rp, pt_st, pt_so, 16); SB(); CVT(ta[2], acc[1], 0);
    POS(52); jpend = LD4(rs, jp_vo, jp_so); SB(); CVT(ta[3], acc[1], 1);
    asm volatile("" :: "v"(ptk));
    POS(53); CVT(tc[0], acc[2], 0);
    mf32z(tr, ta, idn); SB(); CVT(tc[1], acc[2], 1);
    POS(54);
    if (PFQK) { asm volatile("buffer_load_dword %0, %1, %2, %3 offen sc1" : "+v"(junk) : "v"(pf_vo), "s"(rq), "s"(pf_so) : "memory"); SB(); }
    CVT(tc[2], acc[3], 0);
    POS(55); CVT(tc[3], acc[3], 1);
    POS(56); CVT(X[0][0], tr, 0);
    mf32z(ts, tc, idn); SB(); CVT(X[0][1], tr, 1);
    POS(57); B_LO(0, 0);
    POS(58); B_LO(0, 1);
    POS(59); B_HI(0, 0);
    mf16z(dm, X[0], X[0]); SB(); B_HI(0, 1); CVT(X[1][0], ts, 0); CVT(X[1][1], ts, 1);
    CVT(ta[0], acc[4], 0); CVT(ta[1], acc[4], 1);
    mf32z(dx, kf[0], bq); SB(); CVT(ta[2], acc[5], 0); CVT(ta[3], acc[5], 1);
    B_LO(1, 0); B_LO(1, 1);
    mf32z(tr, ta, idn); SB(); B_HI(1, 0); B_HI(1, 1);
    mf16(dm, X[1], X[1]); SB();
    mf32(dx, kf[1], bq); SB();
    CVT(X[2][0], tr, 0); CVT(X[2][1], tr, 1); B_LO(2, 0); B_LO(2, 1);
    B_HI(2, 0); B_HI(2, 1);
    mf16(dm, X[2], X[2]); SB();
    mf32(dx, kf[2], bq); SB();
    if (!S_FINE) STAMP(3);
    NOPS(3);
    LDS16(ex_w + 1024) = __builtin_bit_cast(u32x4, dm);
    NOPS(2);
    LDS16(ex_w) = __builtin_bit_cast(u32x4, dx);
    if constexpr (kind == 0) {
      asm volatile("s_waitcnt lgkmcnt(0)\n\ts_barrier ; softmax waves" ::: "memory");
      if (!S_FINE) STAMP(4);
      f32x4 e[4]; float em[4];
#pragma unroll
      for (int k = 0; k < 4; ++k) e[k] = __builtin_bit_cast(f32x4, LDS16(ex_r + k * 2048));
#pragma unroll
      for (int k = 0; k < 4; ++k) em[k] = *(LAS float*)(lds + ex_m + k * 2048);
      SB();
      Softmax sm;
      sm.s = (e[0] + e[1]) + (e[2] + e[3]);
      const f32x2 u = (f32x2){em[0], em[1]} + (f32x2){em[2], em[3]};
      sm.m = u[0] + u[1];
      unsigned q_;
      const u32x4 v = sm.all(q_);
      __builtin_amdgcn_raw_buffer_store_b128(v, rs, px_st, sp, 16);
      SB();
      STAMP(5);
      const u32x4 vm = sm.mem();
      asm volatile("" :: "v"(v));
      ST16(vm, rs, pm_st, sp);
    } else if constexpr (kind == 1) {
      unsigned fl;
      const unsigned a_gp = p_g2t + s_mulshr16(p_g2c, ck.sig) + extra;
      asm volatile("s_waitcnt lgkmcnt(0)\n\ts_barrier ; clock\n\tds_read_b32 %0, %1" : "=&v"(fl) : "v"((unsigned)(uintptr_t)lds + S_CT + 8u), "s"(a_gp) : "memory");
      const unsigned m0 = ck.edge();
      PIN_V(fl);
      const bool failed = RFL(fl) == (unsigned)s + 1u;
      bq[0] = (ck.re + (unsigned)(((int)(m0 + p_tail - ck.me) * (int)ck.sig) >> 16) + (failed ? LATE_PAD : 0u)) & ~1u;
      bq[1] = a_gp;
      ST16(bq, rs, an_st, sl);
      *(LAS u32x2*)(lds + S_CT + 32) = lo2(bq);
      extra += failed && s > 0 && extra < 240u ? EX_UP + 1u : 0u; PIN_S(extra);
    } else {
      asm volatile("s_waitcnt lgkmcnt(0)\n\ts_barrier\n\ts_memtime %0" : "=s"(tb1) :: "memory");
      __builtin_amdgcn_raw_ptr_buffer_atomic_add_i32(1, rs, l16 << 20, s > 1 ? OFS_CW + CW_SS + (unsigned)(s - 2) * 128u : FAR, 0);
      asm volatile("s_waitcnt lgkmcnt(0)" : "+s"(tb1));
      if (owed) sd.next(an, false);
    }
    SB();
    if (kind) STAMP(5);
    const unsigned sl0 = sl, mT0 = mT;
    {
      f32x4 d[5]; f32x2 zz[12];
      static_for<15>([&](auto N) {
        constexpr int n = decltype(N)::value, t = n / 3, p = n % 3;
        const u32x2 fa = t < 2 ? lo2(kf[p]) : t == 2 ? hi2(kf[p]) : X[p], fb = (t == 0 || t == 3) ? lo2(qf[p]) : hi2(qf[p]);
        if (p == 0) mf16z(d[t], fa, fb); else mf16(d[t], fa, fb);
        SB();
        if (n < 4) { const u32x4 v = LDS16(lw + n * 1024); if (n & 1) th[n >> 1] = v; else tl[n >> 1] = v; }
        if (n >= 5 && p == 2) LDS16(et_w + (t - 1) * 1024) = __builtin_bit_cast(u32x4, d[t > 0 ? t - 1 : 0]);
        if (n == 4) { sl = (sl + M_SLOT) & (4 * M_SLOT - 1); pin_s(sl); }
        if (n == 6) { soA = sl + kA; soB = sl + kB; pin_s(soA); pin_s(soB); }
        if (n == 7) { soA1 = soA + ROW; soB1 = soB + ROW; pin_s(soA1); pin_s(soB1); }
        if (n == 9) { sp = P_SLOT - sp; pt_so = (unsigned)s * PT_SEG; pin_s(sp); pin_s(pt_so); }
        if (n == 10) { qk_so += s + 1 < NSEG ? NH * 12 * 1024u : FAR; pin_s(qk_so); }
        SB();
        if (n < 12) zero2(zz[n < 12 ? n : 0]);
        if (n == 12) { jpend = min(jpend, dppu<0x4E>(jpend)); pin_v(jpend); }
        if (n == 13) { jpend = min(jpend, dppu<0xB1>(jpend)); pin_v(jpend); }
        SB();
      });
#pragma unroll
      for (int t = 0; t < 6; ++t) { acc[t] = __builtin_shufflevector(zz[2 * t], zz[2 * t + 1], 0, 1, 2, 3); pin_v(acc[t]); }
      jhave = max(jhave, RFL(jpend));
      jp_so = jhave < min((unsigned)NSEG, (unsigned)s + 6u) ? 0u : FAR; pin_s(jp_so);
      pf_so = jhave > (unsigned)s + 2u ? qk_so + NH * 12 * 1024u : FAR; pin_s(pf_so);
      NOPS(1);
      LDS16(et_w + 4096) = __builtin_bit_cast(u32x4, d[4]);
      SB();
    }
    if (S_FINE) STAMP(1);
    if constexpr (kind == 2) {
      coarse_until((unsigned)tb1 + (s + 1 < NSEG ? AN_AT : 0u));
      an = LD8(rs, an_ld, s + 1 < NSEG ? sl0 : FAR);
      SB();
    }
    if constexpr (kind == 1) {
      ck.rate(); extra -= extra != 0u; PIN_S(extra);
      LDS16(S_CT + 16) = (u32x4){ck.me, ck.re, ck.rho, 0u};
    }
    if (__builtin_expect(jhave < min((unsigned)s + 2u, (unsigned)NSEG), 0)) need_progress(rs, jp_vo, min((unsigned)s + 2u, (unsigned)NSEG), jhave);
    STAMP(6);
    WG_BARRIER();
    if (S_FINE) STAMP(2);
    {
      f32x4 e[4]; float em[4]; u32x4 cc; u32x2 own;
      if constexpr (kind == 2) { cc = LDS16(S_CT + 16); own = *(LAS u32x2*)(lds + S_CT + 32); }
      token_parts(e, em);
      SB();
      PIN_V(l16);
      if constexpr (kind < 2) load_qk(qk_so);
      else {
        ck.me = RFL(cc[0]); ck.re = RFL(cc[1]); ck.rho = RFL(cc[2]);
        mT = ck.local(sd.on_time());
        u32x2 pub = {(unsigned)s + 1u, mT}; PIN_V(pub);
        const unsigned lim = sd.limit();
        SB();
        if (S_FINE) STAMP(3);
        if (lane == h) an = own;
        const u64 mis = __builtin_amdgcn_ballot_w64(max(an[0], an[1]) == SENT) | (__builtin_amdgcn_ballot_w64((int)(an[0] - lim) > 0) & 0xffffffffull);
        owed = s + 1 == NSEG || (s > 0 && mis == 0);
        if (__builtin_expect(!owed, 0)) {
          an = ann_valid(an, rs, an_ld, sl0, dead);
          sd.next(an, s == 0);
          mT = ck.local(sd.T); pub[1] = mT;
        }
        *(LAS u32x2*)(lds + S_CT) = pub;
        SB();
        load_qk(qk_so);
      }
      SB();
      token_sums(e, em);
    }
    SB();
    if (S_FINE) STAMP(4);
    if constexpr (kind < 2) {
      u32x2 v;
      do { v = *(volatile LAS u32x2*)(lds + S_CT); } while (RFL(v[0]) != (unsigned)s + 1u);
      mT = RFL(v[1]);
    }
    STAMP(7);
    STAMP_FLUSH(s, mT0, kind < 2 ? 0u : owed ? sd.on_time() : sd.T, bad != 0);
  }
  tk.a(); tk.b(); tk.c();
  ST16(tk.d(), rp, pt_st, (unsigned)(NSEG - 1) * PT_SEG);
  asm volatile("s_waitcnt vmcnt(0)" :: "v"(junk) : "memory");
  WG_BARRIER();
  __builtin_amdgcn_raw_ptr_buffer_atomic_add_i32(1, rs, l16 << 20, w < 2 ? OFS_CW + CW_SS + (unsigned)(NSEG - 2 + w) * 128u : FAR, 0);
  TMARK(3);
  STAMP_DUMP();
  __builtin_amdgcn_endpgm();
}

constexpr int O_M = 0, O_RED = O_M + 10240, O_CT = O_RED + 3072;
#define P_GAP 3
#define B_SLP 0
#define O_WAIT 0
#define O_REP 1
#define O_WIN 12
#define OM_LAG 170u
template <int kind>
__device__ __forceinline__ void out_role(const RecArgs& a, LAS char* lds, int rep, int k, unsigned posM, unsigned posP, unsigned pos4) {
  const int lane = fresh_lane(), w = kind == 0 ? 0 : kind == 2 ? 3 : __builtin_amdgcn_readfirstlane(threadIdx.x >> 6), c = lane & 15, g = lane >> 4, hf = c >> 3, i8 = c & 7;
  const int i = rep * 10 + k, iod = rep >> 2, ct = i >> 1, o = i & 1;
  const __amdgpu_buffer_rsrc_t rs = RSRC(a.sync, SYNC_LAYER), ry = RSRC(a.y, Y_IMG), rz = RSRC(a.z, Z_IMG);
  const unsigned l16 = lane * 16, l20 = l16 << 20;
  Clock ck;
  if (kind == 2) ck.init();
  const int t0 = 8 * iod + 2 * w, t2 = 8 * (1 - iod) + 2 * w;
  if (O_WAIT) __builtin_amdgcn_s_sleep(O_WAIT);
  u32x8 UV[8];
  {
    const __amdgpu_buffer_rsrc_t rw = RSRC(a.uf, 80 * 16 * 20480);
    static_for<40>([&](auto F) {
      constexpr int f = decltype(F)::value, sl = f / 10, L = f % 10;
      const unsigned so = (unsigned)(i * 16 + (sl < 2 ? t0 : t2) + (sl & 1)) * 20480u + L * 2048u;
      if (f < 32) { ld_a<8 * (f & 31)>(rw, l16, so); ld_a<8 * (f & 31) + 4>(rw, l16, so + 1024u); VM_WAIT(O_WIN); }
      else UV[f & 7] = cat2(LDP16(rw, l16, so), LDP16(rw, l16 + 1024, so));
    });
    VM_WAIT(0);
#pragma unroll
    for (int f = 0; f < 8; ++f) PIN_V(UV[f]);
  }
  const int sidx = hf ? 0xEEEE : 0x4444;
  const int hq = 2 * ((g < 2 ? t0 : t2) + (g & 1)) + hf;
  const unsigned y_vo = hf * 40960u + g * 256u + o * 128u + i8 * 16u;
  const unsigned z_vo = hq * 256u + o * 128u + i8 * 16u;
  const unsigned p_vo = OFS_P + posP * 4096u + l16, pm_vo = OFS_P + 4 * ROW + posP * 4096u + hq * 128u + i8 * 16u;
  const unsigned pA = (unsigned)(t0 >> 2) * ROW + (t0 & 3) * 1024u, pB = (unsigned)(t2 >> 2) * ROW + (t2 & 3) * 1024u;
  const unsigned m_vo = posM * 4096u + l16;
  const unsigned an_ld = lane < 32 ? ((lane >> 4) * 2 + 1) * ROW + posM * 4096u + 1024u + c * 128u : OOB;
  const int Lm = i >> 3;
  const unsigned m_st = lane < 32 ? ((Lm / 5) * 2 + (Lm % 5 == 4)) * ROW + (POS_OF(lane >> 3) ^ 4u) * 4096u + (Lm % 5 & 3) * 1024u + (i & 7) * 128u + i8 * 16u : OOB;
  unsigned mo[4], ml[4], mc[4];
#pragma unroll
  for (int q = 0; q < 4; ++q) {
    const int L = w == 1 ? q : 3 * w - 2 + q;
    const bool skip = (w != 1 && q == 3) || (L == Lm && (lane >> 3) == (i & 7));
    mo[q] = skip ? OOB : m_vo; ml[q] = skip ? O_RED + 2304 + (lane & 31) * 16 : O_M + L * 1024 + lane * 16;
    mc[q] = ((L / 5) * 2 + (L % 5 == 4)) * ROW + (L % 5 & 3) * 1024u;
  }
  const unsigned own_l = lane < 8 ? O_M + i * 128 + lane * 16 : O_RED + 2304 + (lane & 31) * 16;
  if (w == 0) { LDS16(own_l) = LD16(rs, m_st, 0); LDS16(O_CT) = (u32x4){0u, 0u, 1u, 0u}; *(LAS unsigned*)(lds + O_CT + 16) = 0u; }
  const unsigned rz_vo = ((rep ^ w) & 1) ? OOB : w < 2 ? OFS_P + ((2 * k + w) >> 2) * ROW + posP * 4096u + ((2 * k + w) & 3) * 1024u + l16
                       : w == 2 ? ((k / 5) * 2 + (k % 5 == 4)) * ROW + (k % 5 & 3) * 1024u + m_vo
                       : k < 4 ? ((k >> 1) * 2 + 1) * ROW + 1024u + (k & 1) * 1024u + m_vo : OOB;
  int rz_b = w == 3 ? 2 : 1; unsigned rz_m = w < 2 ? 1u : 3u, rz_k = w < 2 ? P_SLOT : M_SLOT;
  asm volatile("" : "+s"(rz_b), "+s"(rz_m), "+s"(rz_k));
  const unsigned red_w = (g >> 1) == hf ? O_RED + i8 * 272 + (g & 1) * 128 + (w * 2 + hf) * 8 : O_RED + 2304 + lane * 8;
  const unsigned red_r = O_RED + i8 * 272 + g * 64 + hf * 32;
  const unsigned jp_vo = lane < 4 ? OFS_CW + CW_YC + lane * 128u : OOB;
  unsigned jhave = 0;
  bool dead = false;
  Sched sd;
  if (kind == 2) { ck.edge(); ck.rate(); sd.init(ck.global(mtc()), a); }
  unsigned mM = mtc();
  TMARK(1);
  STAMP_DECL(8);
  u32x4 zf = {0u, 0u, 0u, 0u};
  auto step = [&](int s, auto LAST) __attribute__((always_inline)) {
    constexpr bool last = decltype(LAST)::value, kp = kind == 2, w0 = kind == 0;
    constexpr int NC = w0 ? 0 : kp ? 3 : 4;
    const unsigned sl = (unsigned)(s & 3) * M_SLOT, mT = mM;
    u32x4 yf[4];
    const unsigned ys = (unsigned)(s * NH * 40 + ct) * 1024u, z_so = s < 1 ? FAR : (unsigned)((s - 1) * 40 + ct) * 8192u;
    asm volatile("" :: "s"(sl + mc[0]), "s"(sl + mc[1]), "s"(sl + mc[2]), "s"(sl + mc[3]));
    STAMP(0);
    wait_until(mM);
    SB();
    u32x4 cm[4];
#pragma unroll
    for (int q = 0; q < NC; ++q) cm[q] = LD16(rs, mo[q], sl + mc[q]);
    SB();
    auto bulk = [&](auto K) {
      constexpr int k = decltype(K)::value;
      if constexpr (k < 4) { if constexpr (!last) yf[k] = LD16(ry, y_vo, ys + (k < 2 ? t0 : t2) * 81920u + (k & 1) * 81920u); }
      else __builtin_amdgcn_raw_buffer_store_b128(zf, rz, z_vo, z_so, 16);
    };
    f32x4 za[4];
#pragma unroll
    for (int t = 0; t < 4; ++t) zero4(za[t]);
    unsigned sv = SENT; PIN_V(sv);
    u32x4 pz = {sv, sv, sv, sv}; PIN_V(pz);
    const unsigned rz_so = s < rz_b ? FAR : ((unsigned)(s - rz_b) & rz_m) * rz_k, fl_so = s > 1 ? OFS_CW + CW_SO + rep * 128u + (unsigned)(s - 2) * 1024u : FAR;
    const unsigned sp = (unsigned)(s & 1) * P_SLOT, sn = (unsigned)((s + 1) & 3) * M_SLOT;
    asm volatile("" :: "s"(rz_so), "s"(fl_so), "s"(z_so), "s"(ys + t0 * 81920u), "s"(ys + t2 * 81920u),
                 "s"(sp + pA), "s"(sp + pB), "s"(sp), "s"(sn));
    if (__builtin_expect(jhave <= (unsigned)s, 0)) {
      for (unsigned spins = 0; spins < SPIN_CAP; ++spins) {
        if (RFL(LD4(rs, jp_vo, (unsigned)s * 128u)) >= 32u) break;
        __builtin_amdgcn_s_sleep(8);
        asm volatile("" ::: "memory");
      }
    }
    if (w0) NOPS(15);
    if (B_SLP) __builtin_amdgcn_s_sleep(B_SLP);
    SB(); static_for<5>(bulk); SB();
    unsigned jv;
    if constexpr (kp) { jv = LD4(rs, jp_vo, jhave < (unsigned)s + 3u ? (unsigned)(s + 1) * 128u : FAR); SB(); }
    if constexpr (kp) {
      static_for<8>([&](auto K) { ck.rho_step<decltype(K)::value>(); });
      SB();
    }
    unsigned fails = 0;
    if constexpr (!w0) {
      unsigned chk = 0u;
#pragma unroll
      for (int q = 0; q < NC; ++q) chk = mx3(chk, cm[q][0], cm[q][2]);
      if (__builtin_expect(__any(chk == SENT), 0)) {
        fails = 1;
        if (O_REP) *(LAS unsigned*)(lds + O_CT + 8) = (unsigned)s + 1u;
        for (unsigned spins = 0; !dead;) {
          __builtin_amdgcn_s_sleep(4);
          bool nd[NC]; u32x4 n[NC]; unsigned m = 0;
#pragma unroll
          for (int q = 0; q < NC; ++q) { nd[q] = mx4(cm[q]) == SENT; n[q] = LD16(rs, nd[q] ? m_vo : OOB, sl + mc[q]); }
#pragma unroll
          for (int q = 0; q < NC; ++q) { if (nd[q]) cm[q] = n[q]; m = max(m, mx4(cm[q])); }
          if (!__any(m == SENT)) break;
          if (++spins > SPIN_CAP) dead = true;
          asm volatile("" ::: "memory");
        }
      }
#pragma unroll
      for (int q = 0; q < NC; ++q) LDS16(ml[q]) = cm[q];
    }
    WG_BARRIER();
    STAMP(1);
    unsigned lt = 0u;
    {
      u32x4 mf[4];
      mf[0] = LDS16(O_M + l16); mf[1] = LDS16(O_M + 1024 + l16); mf[2] = LDS16(O_M + 2048 + l16);
      if constexpr (kp) lt = *(LAS unsigned*)(lds + O_CT + 8);
      SB();
      static_for<kp ? 8 : 10>([&](auto LL) {
        constexpr int L = decltype(LL)::value;
        smf_a<8 * L>(za[0], mf[L % 4], sidx); SB();
        if (L + 3 < 10) mf[(L + 3) % 4] = LDS16(O_M + (L + 3) * 1024 + l16);
        SB();
        smf_a<80 + 8 * L>(za[1], mf[L % 4], sidx); SB();
        if (L == 0) __builtin_amdgcn_raw_buffer_store_b128(pz, rs, rz_vo, rz_so, 16);
        if (L == 1 && w0) __builtin_amdgcn_raw_ptr_buffer_atomic_add_i32(1, rs, l20, fl_so, 0);
        SB();
        smf_a<160 + 8 * L>(za[2], mf[L % 4], sidx);
        if (L < 2) smf_a<240 + 8 * (L & 1)>(za[3], mf[L % 4], sidx); else smf_v(za[3], mf[L % 4], UV[L < 2 ? 0 : L - 2], sidx);
        SB();
      });
      asm volatile("" :: "v"(pz), "v"(zf));
      if constexpr (kp) ck.edge_z(za, mf[0], mf[1], UV[6], UV[7], sidx); else NOPS(7);
    }
    STAMP(2);
    unsigned zp[4];
    auto zpost = [&](int q) {
      auto x = __builtin_amdgcn_permlane32_swap(f2u(za[q >> 1][2 * (q & 1)]), f2u(za[2 + (q >> 1)][2 * (q & 1)]), false, false);
      auto y = __builtin_amdgcn_permlane32_swap(f2u(za[q >> 1][2 * (q & 1) + 1]), f2u(za[2 + (q >> 1)][2 * (q & 1) + 1]), false, false);
      zp[q] = pack2(u2f(x[0]) + u2f(x[1]), u2f(y[0]) + u2f(y[1])); pin_v(zp[q]);
      if (q == 3) {
        auto u = __builtin_amdgcn_permlane16_swap(zp[0], zp[2], false, false);
        auto v = __builtin_amdgcn_permlane16_swap(zp[1], zp[3], false, false);
        zf = (u32x4){u[0], v[0], u[1], v[1]}; pin_v(zf);
      }
    };
    if constexpr (last) { zpost(0); zpost(1); zpost(2); zpost(3); ST16(zf, rz, z_vo, (unsigned)(s * 40 + ct) * 8192u); return; }
    SB();
    unsigned mP;
    if constexpr (kp) {
      mP = ck.local(sd.tP);
      if (__builtin_expect(RFL(lt) == (unsigned)s + 1u, 0)) {
        fails |= 4;
        const u32x2 r = ann_valid((u32x2){SENT, SENT}, rs, an_ld, sl, dead);
        ck.edge();
        mP = ck.local(sd.T + (unsigned)max32((int)(r[0] - sd.T)) + sd.a.d0);
      }
      *(LAS u32x2*)(lds + O_CT) = (u32x2){(unsigned)s + 1u, mP};
    } else {
      u32x2 v;
      do { v = *(volatile LAS u32x2*)(lds + O_CT); } while (RFL(v[0]) != (unsigned)s + 1u);
      mP = RFL(v[1]);
    }
    STAMP(3);
    SB();
    wait_until(mP);
    SB();
    u32x4 pf[5];
    pf[0] = LD16(rs, p_vo, sp + pA); SB(); zpost(0); NOPS(P_GAP);
    pf[1] = LD16(rs, p_vo, sp + pA + 1024u); SB(); zpost(1); NOPS(P_GAP);
    pf[2] = LD16(rs, p_vo, sp + pB); SB(); zpost(2); NOPS(P_GAP);
    pf[3] = LD16(rs, p_vo, sp + pB + 1024u); SB(); zpost(3); SB();
    pf[4] = LD16(rs, pm_vo, sp); SB();
    u32x4 pub; u32x2 an; unsigned lim;
    if constexpr (kp) {
      jhave = max(jhave, (unsigned)s + 1u + (unsigned)__builtin_ctzll(__builtin_amdgcn_ballot_w64(jv < 32u)));
      mM = ck.local(sd.on_time() + OM_LAG); lim = sd.limit();
      pub = (u32x4){(unsigned)s + 1u, mM, jhave, 0u}; PIN_V(pub); pin_s(lim);
      SB(); an = LD8(rs, an_ld, sl);
    }
    STAMP(4);
    SB();
    f32x4 d;
    unsigned chk;
    mf32z(d, yf[0], pf[0]); SB(); chk = max(pf[0][0], pf[0][2]); PIN_V(chk); SB();
    mf32(d, yf[1], pf[1]); SB(); chk = mx3(chk, pf[1][0], pf[1][2]); PIN_V(chk); SB();
    mf32(d, yf[2], pf[2]); SB(); chk = mx3(chk, pf[2][0], pf[2][2]); PIN_V(chk); SB();
    mf32(d, yf[3], pf[3]); SB(); chk = mx3(chk, pf[3][0], pf[3][2]); PIN_V(chk); SB();
    mf32(d, zf, pf[4]); SB(); chk = mx3(chk, pf[4][0], pf[4][2]);
    u64 bad = __builtin_amdgcn_ballot_w64(chk == SENT); asm volatile("" : "+s"(bad));
    NOPS(5);
    *(LAS f32x2*)(lds + red_w) = (f32x2){d[0], d[1]}; *(LAS f32x2*)(lds + red_w + 64) = (f32x2){d[2], d[3]};
    SB();
    if (__builtin_expect(bad != 0, 0)) {
      fails |= 2;
      for (unsigned spins = 0; !dead;) {
        __builtin_amdgcn_s_sleep(2);
        bool nd[5]; u32x4 n[5]; unsigned m = 0;
#pragma unroll
        for (int q = 0; q < 5; ++q) { nd[q] = mx4(pf[q]) == SENT; n[q] = LD16(rs, nd[q] ? (q < 4 ? p_vo : pm_vo) : OOB, sp + (q < 2 ? pA : q < 4 ? pB : 0u) + (q & 1) * 1024u); }
#pragma unroll
        for (int q = 0; q < 5; ++q) { if (nd[q]) pf[q] = n[q]; m = max(m, mx4(pf[q])); }
        if (!__any(m == SENT)) break;
        if (++spins > SPIN_CAP) dead = true;
        asm volatile("" ::: "memory");
      }
#pragma unroll
      for (int q = 0; q < 5; ++q) PIN_V(pf[q]);
      NOPS(1);
      mf32z(d, yf[0], pf[0]); mf32(d, yf[1], pf[1]); mf32(d, yf[2], pf[2]); mf32(d, yf[3], pf[3]); mf32(d, zf, pf[4]);
      NOPS(7);
      *(LAS f32x2*)(lds + red_w) = (f32x2){d[0], d[1]}; *(LAS f32x2*)(lds + red_w + 64) = (f32x2){d[2], d[3]};
    }
    STAMP(5);
    if constexpr (w0) {
      asm volatile("s_waitcnt lgkmcnt(0)\n\ts_barrier ; first wave" ::: "memory");
      const f32x4 e = __builtin_bit_cast(f32x4, LDS16(red_r)) + __builtin_bit_cast(f32x4, LDS16(red_r + 16));
      const f32x2 u = (f32x2){e[0], e[1]} + (f32x2){e[2], e[3]};
      unsigned o0, o1, o2, o3;
      asm volatile("s_nop 1\n\tv_add_f32_dpp %0, %4, %4 row_ror:8 row_mask:0xf bank_mask:0xf\n\tv_add_f32_dpp %1, %5, %5 row_ror:8 row_mask:0xf bank_mask:0xf\n\t"
                   "v_cvt_pk_bf16_f32 %2, %0, %1\n\tv_min_u32 %0, -2, %2\n\tv_min_u32 %1, -2, %2\n\ts_nop 1\n\tv_permlane16_swap_b32 %0, %1\n\t"
                   "v_mov_b32 %2, %0\n\tv_mov_b32 %3, %1\n\ts_nop 0\n\tv_permlane32_swap_b32 %0, %2\n\tv_permlane32_swap_b32 %1, %3"
                   : "=&v"(o0), "=&v"(o1), "=&v"(o2), "=&v"(o3) : "v"(u[0]), "v"(u[1]));
      ST16(((u32x4){o0, o1, o2, o3}), rs, m_st, sn);
      LDS16(own_l) = (u32x4){o0, o1, o2, o3};
    } else if (kp) asm volatile("s_waitcnt lgkmcnt(0)\n\ts_barrier" ::: "memory");
    else asm volatile("s_waitcnt lgkmcnt(0)\n\ts_barrier\n\ts_sleep 3" ::: "memory");
    SB();
    STAMP(6);
    if constexpr (kp) {
      const u64 mis = __builtin_amdgcn_ballot_w64(max(an[0], an[1]) == SENT) | (__builtin_amdgcn_ballot_w64((int)(an[0] - lim) > 0) & 0xffffffffull);
      const bool fine = s > 0 && mis == 0;
      if (fine) LDS16(O_CT + 16) = pub;
      SB();
      sd.next(ann_valid(an, rs, an_ld, sl, dead), s == 0);
      if (__builtin_expect(!fine, 0)) { mM = ck.local(sd.T + OM_LAG); pub[1] = mM; LDS16(O_CT + 16) = pub; }
    } else {
      u32x4 v;
      do { v = *(volatile LAS u32x4*)(lds + O_CT + 16); } while (RFL(v[0]) != (unsigned)s + 1u);
      mM = RFL(v[1]); jhave = max(jhave, RFL(v[2]));
    }
    STAMP(7);
    STAMP_FLUSH(s, mT - OM_LAG * 21u / 8u, mP, fails);
  };
#pragma unroll 1
  for (int s = 0; s < NSEG - 1; ++s) step(s, std::false_type());
  step(NSEG - 1, std::true_type());
  VM_WAIT(0);
  WG_BARRIER();
  __builtin_amdgcn_raw_ptr_buffer_atomic_add_i32(1, rs, l20, w < 2 ? OFS_CW + CW_SO + rep * 128u + (unsigned)(NSEG - 2 + w) * 1024u : FAR, 0);
  TMARK(3);
  STAMP_DUMP();
  __builtin_amdgcn_endpgm();
}

#define QK_WAIT 0
#define J_FLAG 11
__host__ __device__ constexpr int j_slot(int kb) { return (kb + 1) % 3; }
__device__ __forceinline__ void proj_role(const RecArgs& a, LAS char* lds, int jn) {
  const int lane = fresh_lane(), w = __builtin_amdgcn_readfirstlane(threadIdx.x >> 6);
  const __amdgpu_buffer_rsrc_t rs = RSRC(a.sync, SYNC_LAYER), rx = RSRC(a.xf, S_ * DM * 2), ro = RSRC(a.qk, 2 * QK_IMG);
  const unsigned l16 = lane * 16, T0 = 16 * jn + 4 * w;
#define DMA_P(so, lo, p) dma1k<((p) & 3) * 1024>(rx, l16, (so) + ((p) >> 2) * 4096u, (lo) + ((p) >> 2) * 4096u)
  const unsigned lb = (unsigned)(uintptr_t)lds + w * 10240u;
  if (QK_WAIT) __builtin_amdgcn_s_sleep(QK_WAIT);
  static_for<10>([&](auto P_) { DMA_P(w * 10240u, lb, decltype(P_)::value); });
  u32x4 WV[16];
  {
    const __amdgpu_buffer_rsrc_t rw = RSRC(a.wf, 1152 * 20480);
    static_for<80>([&](auto F) {
      constexpr int f = decltype(F)::value;
      if (f < 64) { ld_a<4 * (f & 63)>(rw, l16, T0 * 20480u + f * 1024u); VM_WAIT(40); } else WV[f & 15] = LDP16(rw, l16, T0 * 20480u + f * 1024u);
    });
#pragma unroll
    for (int f = 0; f < 16; ++f) PIN_V(WV[f]);
  }
  const unsigned l20 = l16 << 20, f_so = w == 0 ? OFS_CW + CW_JP + jn * 128u : FAR, o_so = (T0 / 384u) * QK_IMG + (T0 % 384u) * 1024u;
  TMARK(1);
  u32x4 pend[4], xr[3][2];
  f32x4 acc[8];
#pragma unroll
  for (int q = 0; q < 8; ++q) zero4(acc[q]);
#pragma unroll
  for (int t = 0; t < 4; ++t) pend[t] = (u32x4){0u, 0u, 0u, 0u};
  auto pack = [&](int q) {
    unsigned u = pack2(acc[q][0], acc[q][1]), v = pack2(acc[q][2], acc[q][3]); pin_v(u); pin_v(v);
    pend[q >> 1][2 * (q & 1)] = u; pend[q >> 1][2 * (q & 1) + 1] = v;
  };
  VM_WAIT(0);
  WG_BARRIER();
  static_for<10>([&](auto P_) { DMA_P(40960u + w * 10240u, lb + 40960u, decltype(P_)::value); });
  unsigned bn = 40960u, pso = FAR, dso = 81920u + w * 10240u, dlo = lb + 81920u;
  unsigned xb = l16, xn = l16 + 40960u, sv = 0u, ov = l16;
  xr[1][0] = LDS16(l16); xr[1][1] = LDS16(l16 + 20480);
#pragma unroll 1
  for (int s = 0; s < NSEG; ++s) {
    PIN_V(ov);
    SB();
    static_for<160>([&](auto N) {
      constexpr int n = decltype(N)::value, kb = n / 8, q = n % 8, f = (q >> 1) * 20 + kb, sl = j_slot(kb);
      if (kb == 0) mf32z_aa<4 * (f & 63)>(acc[q], xr[sl][q & 1]);
      else if (f < 64) mf32_aa<4 * (f & 63)>(acc[q], xr[sl][q & 1]);
      else mf32(acc[q], WV[f & 15], xr[sl][q & 1]);
      SB();
      if constexpr (kb < 18) { if (q == 3 || q == 4) xr[j_slot(kb + 2)][q - 3] = LDS16(xb + (q - 3) * 20480 + (kb + 2) * 1024); }
      else if constexpr (kb == 19) { if (q < 2) xr[1][q] = LDS16(xn + q * 20480); }
      if constexpr (kb == 0) { if (q < 2) xr[2][q] = LDS16(xb + q * 20480 + 1024); }
      if constexpr (q == 6 && kb < 4) __builtin_amdgcn_raw_buffer_store_b128(pend[kb], ro, ov + kb * 1024, pso, 16);
      if constexpr (q == 6 && kb == 5) { pso = o_so + (unsigned)s * (384u * 1024u); pin_s(pso); }
      if constexpr (q == 5 && kb == 4) {
        if (s == 1) { asm volatile("s_waitcnt vmcnt(0)\n\ts_barrier" ::: "memory"); __builtin_amdgcn_raw_buffer_store_b32(sv, rs, l20, f_so, 16); }
      }
      if constexpr (q == 5 && kb == J_FLAG) asm volatile("s_waitcnt vmcnt(0)\n\ts_barrier" ::: "memory");
      if constexpr (q == 6 && kb == J_FLAG) __builtin_amdgcn_raw_buffer_store_b32(sv, rs, l20, f_so, 16);
      constexpr int dn = n - (J_FLAG + 1) * 8, dp = dn / 4;
      if constexpr (dn >= 0 && dp < 10) {
        if constexpr (dn % 4 == 0 && dp % 4 == 0 && dp > 0) { dso += 4096u; dlo += 4096u; pin_s(dso); pin_s(dlo); }
        if constexpr (dn % 4 == 1 && dp % 4 == 0) set_m0(dlo);
        if constexpr (dn % 4 == 2) dma1k_m0<(dp & 3) * 1024>(rx, l16, dso);
      }
      if constexpr (kb == 18) {
        if (q == 0) { dso += s + 3 < NSEG ? 40960u - 8192u : 0u - 8192u; pin_s(dso); }
        if (q == 1) { bn = bn == 81920u ? 0u : bn + 40960u; pin_s(bn); }
        if (q == 2) { dlo = dlo >= lb + 81920u ? lb : dlo + 40960u - 8192u; pin_s(dlo); }
      }
      SB();
      if constexpr (kb == 18) { if (q == 3) { xb = xn; pin_v(xb); } if (q == 4) { sv += 1u; pin_v(sv); } }
      if constexpr (kb == 19 && q == 2) { xn = bn + l16; pin_v(xn); }
      if constexpr (kb == 19 && q >= 3) pack(q - 3);
      if constexpr (kb == 0 && q < 3) pack(5 + q);
      SB();
    });
  }
  NOPS(7);
  pack(5); pack(6); pack(7);
#pragma unroll
  for (int t = 0; t < 4; ++t) ST16(pend[t], ro, l16 + t * 1024, pso);
  VM_WAIT(0);
  WG_BARRIER();
  __builtin_amdgcn_raw_buffer_store_b32((unsigned)NSEG, rs, l20, f_so, 16);
  TMARK(2);
}

constexpr int Y_V = 40960, Y_W = Y_V + 12288, Y_END = Y_W + 4 * 20 * 1024;
__device__ __forceinline__ void vwo_role(const RecArgs& a, LAS char* lds, int h, int par) {
  const int lane = fresh_lane(), w = __builtin_amdgcn_readfirstlane(threadIdx.x >> 6);
  const __amdgpu_buffer_rsrc_t rs = RSRC(a.sync, SYNC_LAYER), rx = RSRC(a.xf, S_ * DM * 2), ry = RSRC(a.y, Y_IMG);
  unsigned l16 = lane * 16;
  const unsigned lb = (unsigned)(uintptr_t)lds + w * 10240u, lw = Y_W + w * 20480 + l16, l20 = l16 << 20;
  static_for<10>([&](auto P_) { DMA_P(par * 40960u + w * 10240u, lb, decltype(P_)::value); });
  u32x4 WO[36];
  {
    const __amdgpu_buffer_rsrc_t rw = RSRC(a.wf, 1152 * 20480), ro = RSRC(a.wof, NH * 240 * 1024);
    static_for<60>([&](auto F) { constexpr int f = decltype(F)::value; ld_a<4 * f>(rw, l16, (unsigned)((768 + h * 12 + 3 * w) * 20 + f) * 1024u); VM_WAIT(40); });
    static_for<60>([&](auto F) {
      constexpr int f = decltype(F)::value, r = f - f / 3;
      const unsigned so = (unsigned)(h * 240 + w * 60 + f) * 1024u;
      if constexpr (f % 3 == 2) dma1k<0>(ro, l16, so, (unsigned)(uintptr_t)lds + Y_W + w * 20480 + (f / 3) * 1024);
      else if constexpr (r < 4) ld_a<240 + 4 * r>(ro, l16, so);
      else { WO[r - 4] = LDP16(ro, l16, so); SB(); }
      VM_WAIT(40);
    });
#pragma unroll
    for (int f = 0; f < 36; ++f) PIN_V(WO[f]);
  }
  VM_WAIT(0);
  TMARK(1);
  unsigned vw[3];
#pragma unroll
  for (int t = 0; t < 3; ++t) vw[t] = Y_V + ((3 * w + t) >> 1) * 2048 + l16 + ((3 * w + t) & 1) * 8;
#pragma unroll 1
  for (int s = par; s < NSEG; s += 2) {
    const unsigned ys = (unsigned)((s * NH + h) * 40 + 10 * w) * 1024u, f_so = (w == 0 && s > 3) ? OFS_CW + CW_YC + (unsigned)(s - 2) * 128u : FAR;
    unsigned dso = (unsigned)min(s + 2, NSEG - 1) * 40960u + w * 10240u, dlo = lb;
    PIN_V(l16);
    asm volatile("s_waitcnt vmcnt(6)\n\ts_barrier" ::: "memory");
    f32x4 acc[6]; u32x4 xr[3][2];
#pragma unroll
    for (int q = 0; q < 4; ++q) xr[q >> 1][q & 1] = LDS16(l16 + (q & 1) * 20480 + (q >> 1) * 1024);
    SB();
    static_for<120>([&](auto N) {
      constexpr int n = decltype(N)::value, kb = n / 6, q = n % 6;
      if (kb == 0) mf32z_aa<80 * (q >> 1)>(acc[q], xr[0][q & 1]); else mf32_aa<80 * (q >> 1) + 4 * kb>(acc[q], xr[kb % 3][q & 1]);
      SB();
      if (kb < 18 && (q == 2 || q == 3)) xr[(kb + 2) % 3][q & 1] = LDS16(l16 + (q & 1) * 20480 + (kb + 2) * 1024);
      SB();
    });
    u32x4 wl[3];
    wl[0] = LDS16(lw); wl[1] = LDS16(lw + 1024);
    SB();
#pragma unroll
    for (int q = 0; q < 6; ++q) { *(LAS u32x2*)(lds + vw[q >> 1] + (q & 1) * 1024) = (u32x2){pack2(acc[q][0], acc[q][1]), pack2(acc[q][2], acc[q][3])}; SB(); }
    asm volatile("s_waitcnt vmcnt(0) lgkmcnt(0)\n\ts_barrier" ::: "memory");
    u32x4 vf[12];
#pragma unroll
    for (int k = 0; k < 12; ++k) vf[k] = LDS16(Y_V + k * 1024 + l16);
    __builtin_amdgcn_raw_ptr_buffer_atomic_add_i32(1, rs, l20, f_so, 0);
    SB();
    f32x4 d[2][2]; u32x4 o;
    static_for<132>([&](auto N) {
      constexpr int n = decltype(N)::value, t = n / 12, k = n % 12, f = 6 * t + k / 2, r = f - f / 3;
      if constexpr (t < 10) {
        if constexpr (f % 3 == 2) mf32(d[t & 1][k & 1], vf[k], wl[(f / 3) % 3]);
        else if constexpr (r < 4) { if (k < 2) mf32z_ba<240 + 4 * r>(d[t & 1][k], vf[k]); else mf32_ba<240 + 4 * r>(d[t & 1][k & 1], vf[k]); }
        else { if (k < 2) mf32z(d[t & 1][k & 1], vf[k], WO[r - 4]); else mf32(d[t & 1][k & 1], vf[k], WO[r - 4]); }
      } else if (k == 0) NOPS(7);
      SB();
      if constexpr (k % 6 == 0 && n / 6 + 2 < 20) wl[(n / 6 + 2) % 3] = LDS16(lw + (n / 6 + 2) * 1024);
      if constexpr (t > 0 && k == 8) __builtin_amdgcn_raw_buffer_store_b128(o, ry, l16 + ((t - 1) & 3) * 1024, ys + ((t - 1) >> 2) * 4096u, 16);
      if constexpr (t < 5) {
        constexpr int dp = 2 * t + (k > 2);
        if constexpr (k == 0 && dp % 4 == 0 && dp > 0) { dso += 4096u; dlo += 4096u; pin_s(dso); pin_s(dlo); }
        if constexpr (k == 1 && dp % 4 == 0) set_m0(dlo);
        if constexpr (k == 2 || k == 10) dma1k_m0<(dp & 3) * 1024>(rx, l16, dso);
      }
      SB();
      if constexpr (t > 0 && (k == 2 || k == 4 || k == 5 || k == 7)) {
        constexpr int c = k == 2 ? 0 : k == 4 ? 1 : k == 5 ? 2 : 3;
        unsigned u = pack2(d[(t - 1) & 1][c >> 1][2 * (c & 1)], d[(t - 1) & 1][c >> 1][2 * (c & 1) + 1]); pin_v(u); o[c] = u;
      }
      if constexpr (t > 0 && k == 11) keep4(o);
      SB();
    });
    if (s < 2 || s + 2 >= NSEG) {
      VM_WAIT(0);
      asm volatile("s_barrier" ::: "memory");
      __builtin_amdgcn_raw_ptr_buffer_atomic_add_i32(1, rs, l20, w == 0 ? OFS_CW + CW_YC + (unsigned)s * 128u : FAR, 0);
    }
  }
  TMARK(2);
}

constexpr int T_LD = 84;
#define T_SLP 60
#define T_GRP 5
#define T_FAST 3
#define T_FLY2 10
#define T_FLY 1
__device__ __forceinline__ void token_role(const RecArgs& a, LAS char* lds, bool gentle) {
  const int tid = threadIdx.x, w = __builtin_amdgcn_readfirstlane(tid >> 6);
  const __amdgpu_buffer_rsrc_t rs = RSRC(a.sync, SYNC_LAYER), ry = RSRC(a.y, Y_IMG), rz = RSRC(a.z, Z_IMG), rp = RSRC(a.pt, PT_IMG), rx = RSRC(a.xr, S_ * DM * 2);
  LAS float* part = (LAS float*)lds;
  LAS unsigned* tick = (LAS unsigned*)(lds + 4 * 32 * T_LD * 4);
  auto wait_count = [&](unsigned off, unsigned want, bool calm) {
    if (w == 0) {
      for (unsigned spins = 0; spins < SPIN_CAP; ++spins) {
        if (RFL(LD4(rs, off, 0)) >= want) break;
        if (calm) __builtin_amdgcn_s_sleep(T_SLP); else __builtin_amdgcn_s_sleep(6);
        asm volatile("" ::: "memory");
      }
    }
    WG_BARRIER();
  };
#pragma unroll 1
  for (;;) {
    WG_BARRIER();
    if (tid == 0) *tick = __hip_atomic_fetch_add((GAS unsigned*)(a.sync + OFS_CW + CW_TQ), 1u, RLX_AGENT);
    WG_BARRIER();
    const int n = __builtin_amdgcn_readfirstlane(*(volatile LAS unsigned*)tick);
    if (n >= 512) { TMARK(3); return; }
    const int s = n >> 3, cg = n & 7;
    const bool calm = s < NSEG - 8, slow = gentle && s < NSEG - T_FAST;
    int lane = tid & 63; PIN_V(lane);
    const int c = lane & 15, g = lane >> 4;
    u32x4 xo[2];
#pragma unroll
    for (int r = 0; r < 2; ++r) { const int p = r * 256 + tid; if (p < 320) xo[r] = *(const u32x4*)(a.xold + (a.layer ? (size_t)s * 20480 + ((p >> 4) & 1) * 10240 + (10 * cg + (p >> 5)) * 128 + (p & 15) * 8 : (size_t)(s * 32 + (p & 31)) * DM + 80 * cg + (p >> 5) * 8)); }
    const unsigned l16 = lane * 16;
    wait_count(OFS_CW + CW_YC + s * 128, 32u, true);
    static_for<40>([&](auto F) {
      constexpr int f = decltype(F)::value;
      ld_a1<4 * f>(ry, l16, (unsigned)((s * NH + 8 * w + f / 5) * 40 + 5 * cg + f % 5) * 1024u);
      if (f % T_GRP == T_GRP - 1) { if (gentle) VM_WAIT(T_FLY); else VM_WAIT(T_FLY2); }
    });
    if (!calm) wait_count(OFS_CW + CW_SS + (s - 3) * 128, 32u, true);
    wait_count(OFS_CW + CW_SO + (s * 8 + cg) * 128, 10u, calm);
    static_for<10>([&](auto F) {
      constexpr int f = decltype(F)::value;
      ld_a1<160 + 4 * f>(rz, l16, (unsigned)((s * 40 + 5 * cg + f % 5) * NH + 8 * w + 4 * (f / 5)) * 256u);
      if (f % T_GRP == T_GRP - 1 && slow) VM_WAIT(T_FLY);
    });
    wait_count(OFS_CW + CW_SS + s * 128, 32u, calm);
    u32x4 bf[10][2];
#pragma unroll
    for (int kk = 0; kk < 10; ++kk)
#pragma unroll
      for (int b = 0; b < 2; ++b) {
        bf[kk][b] = LD16(rp, l16, (unsigned)s * PT_SEG + (kk < 8 ? (unsigned)((8 * w + kk) * 2 + b) * 1024u : PT_M + (unsigned)(b * NH + 8 * w + 4 * (kk - 8)) * 256u));
        if (b == 1 && (kk & 1) && slow) VM_WAIT(T_FLY);
      }
    f32x4 acc[5][2];
    static_for<100>([&](auto N) {
      constexpr int n = decltype(N)::value, kk = n / 10, t = (n % 10) >> 1, b = n & 1;
      if (kk == 0) mf32z_aa<4 * t>(acc[t][b], bf[0][b]); else mf32_aa<4 * (5 * kk + t)>(acc[t][b], bf[kk][b]);
    });
    NOPS(7);
#pragma unroll
    for (int t = 0; t < 5; ++t)
#pragma unroll
      for (int b = 0; b < 2; ++b) {
        LAS float* p = part + (w * 32 + 16 * b + c) * T_LD + 16 * t + 8 * (g & 1) + 2 * (g >> 1);
        *(LAS f32x2*)p = (f32x2){acc[t][b][0], acc[t][b][1]}; *(LAS f32x2*)(p + 4) = (f32x2){acc[t][b][2], acc[t][b][3]};
      }
    WG_BARRIER();
#pragma unroll
    for (int r = 0; r < 2; ++r) {
      const int p = r * 256 + tid;
      if (p < 320) {
        const LAS float* q = part + (p & 31) * T_LD + (p >> 5) * 8;
        f32x4 s0 = *(const LAS f32x4*)q, s1 = *(const LAS f32x4*)(q + 4);
#pragma unroll
        for (int k = 1; k < 4; ++k) { s0 += *(const LAS f32x4*)(q + k * 32 * T_LD); s1 += *(const LAS f32x4*)(q + k * 32 * T_LD + 4); }
        u32x4 o;
#pragma unroll
        for (int k = 0; k < 2; ++k) {
          o[k] = pack2(bf2f(xo[r][k] & 0xffff) + rbf(s0[2 * k]), bf2f(xo[r][k] >> 16) + rbf(s0[2 * k + 1]));
          o[2 + k] = pack2(bf2f(xo[r][2 + k] & 0xffff) + rbf(s1[2 * k]), bf2f(xo[r][2 + k] >> 16) + rbf(s1[2 * k + 1]));
        }
        ST16(o, rx, (unsigned)(((p >> 4) & 1) * 20480 + (10 * cg + (p >> 5)) * 256 + (p & 15) * 16), (unsigned)s * 40960u);
      }
    }
    VM_WAIT(0);
    WG_BARRIER();
    __builtin_amdgcn_raw_ptr_buffer_atomic_add_i32(1, rs, l16 << 20, w == 0 ? OFS_CW + CW_TC + (unsigned)s * 128u : FAR, 0);
  }
}

#define B_WIN 40
#define B_SETS 4
#define B_CUT 32
constexpr int B_ONE = 2 * 40960, B_ST = B_ONE + 20480;
template <bool VO>
__device__ __forceinline__ void bproj_role(const RecArgs& a, LAS char* lds, int rep, int s0, int s1, int ds) {
  const int lane = fresh_lane(), w = __builtin_amdgcn_readfirstlane(threadIdx.x >> 6), c = lane & 15, g = lane >> 4;
  const __amdgpu_buffer_rsrc_t rs = RSRC(a.sync, SYNC_LAYER), rx = RSRC(a.xr, S_ * DM * 2), ro = RSRC(a.qkvb, 3 * BH * S_ * 64);
  const unsigned l16 = lane * 16;
  const int T0 = 16 * rep + 4 * w;
  if (s0 == 0) { for (int d = 0; d < 4; ++d) __builtin_amdgcn_s_sleep(100); }
  u32x4 WV[16];
  {
    const __amdgpu_buffer_rsrc_t rw = RSRC(a.wb, 120 * 20480);
    static_for<80>([&](auto F) {
      constexpr int f = decltype(F)::value;
      const unsigned so = (unsigned)T0 * 20480u + f * 1024u;
      if (f < 64) ld_a<4 * (f & 63)>(rw, l16, so); else { WV[f & 15] = LDP16(rw, l16, so); SB(); }
      VM_WAIT(B_WIN);
    });
    VM_WAIT(0);
#pragma unroll
    for (int f = 0; f < 16; ++f) PIN_V(WV[f]);
  }
  auto mm = [&](auto F, auto Z, f32x4& acc, u32x4 x) {
    constexpr int f = decltype(F)::value; constexpr bool z = decltype(Z)::value;
    if constexpr (f < 64) { if (z) { if (VO) mf32z_ba<4 * f>(acc, x); else mf32z_aa<4 * f>(acc, x); } else { if (VO) mf32_ba<4 * f>(acc, x); else mf32_aa<4 * f>(acc, x); } }
    else { if (VO) mf32(acc, x, WV[f - 64]); else mf32(acc, WV[f - 64], x); }
  };
  u32x4 one = {0x3f803f80u, 0x3f803f80u, 0x3f803f80u, 0x3f803f80u}; PIN_V(one);
#pragma unroll
  for (int k = 0; k < 5; ++k) LDS16(B_ONE + (w * 5 + k) * 1024 + l16) = one;
  f32x4 cs[4];
  NOPS(1);
  static_for<80>([&](auto N) { constexpr int n = decltype(N)::value; mm(std::integral_constant<int, (n % 4) * 20 + n / 4>(), std::bool_constant<(n < 4)>(), cs[n % 4], one); });
  unsigned o_vo[4];
#pragma unroll
  for (int t = 0; t < 4; ++t) {
    const int n = 16 * (T0 + t) + (VO ? c : 4 * g), im = n / DM, hd = (n - im * DM) / BD, e = n - im * DM - hd * BD;
    o_vo[t] = n >= 3 * DM ? OOB : (unsigned)(im * BH + hd) * (S_ * 64u) + (VO ? ((g >> 1) * 32 + e) * 16 + (g & 1) * 8 : (e >> 3) * 512 + c * 16 + (e & 4) * 2);
  }
  const float qs = T0 < 40 ? 0.22360679774997896f * 1.4426950408889634f : 1.0f;
  const unsigned sa_ = (w & 1) * 20480 + l16, st_w = (c >> 2) == g ? B_ST + (w * 16 + c) * 4 : B_ST + 256 + lane * 4;
  u32x4 xq[10];
  auto ready = [&](int s) {
    for (unsigned spins = 0; spins < SPIN_CAP; ++spins) {
      if (RFL(LD4(rs, l16 << 20, OFS_CW + CW_TC + (unsigned)s * 128u)) >= 8u) break;
      if (s < NSEG - 4) __builtin_amdgcn_s_sleep(40); else __builtin_amdgcn_s_sleep(6);
      asm volatile("" ::: "memory");
    }
  };
  bool have = false;
  unsigned xb = 40960;
#pragma unroll 1
  for (int s = s0; s < s1; s += ds) {
    xb = 40960 - xb;
    const unsigned xn = 40960 - xb + w * 10240 + l16;
    if (!have) {
      ready(s);
#pragma unroll
      for (int k = 0; k < 10; ++k) xq[k] = LD16(rx, l16, (unsigned)s * 40960u + w * 10240u + k * 1024u);
#pragma unroll
      for (int k = 0; k < 10; ++k) LDS16(xb + w * 10240 + k * 1024 + l16) = xq[k];
    }
    const unsigned nx = LD4(rs, l16 << 20, s + ds < s1 ? OFS_CW + CW_TC + (unsigned)(s + ds) * 128u : FAR);
    WG_BARRIER();
    f32x4 acc[8], as; u32x4 xr[3][4];
    const unsigned xa = xb + l16, sa = xb + sa_, sb = w < 2 ? sa : B_ONE + l16;
    unsigned nso = FAR;
#define B_GET(kb, q) (xr[(kb) % 3][q] = LDS16(((q) < 2 ? xa + (q) * 20480 : (q) == 2 ? sa : sb) + (kb) * 1024))
#pragma unroll
    for (int q = 0; q < 8; ++q) B_GET(q >> 2, q & 3);
    SB();
    static_for<180>([&](auto N) {
      constexpr int n = decltype(N)::value, kb = n / 9, q = n % 9;
      if constexpr (q < 8) mm(std::integral_constant<int, (q >> 1) * 20 + kb>(), std::bool_constant<kb == 0>(), acc[q], xr[kb % 3][q & 1]);
      else { if (kb == 0) mf32z(as, xr[0][2], xr[0][3]); else mf32(as, xr[kb % 3][2], xr[kb % 3][3]); }
      SB();
      if (q >= 1 && q < 5 && kb + 2 < 20) B_GET(kb + 2, q - 1);
      if (n == 5 * 9 + 5) { have = RFL(nx) >= 8u; nso = have ? (unsigned)(s + ds) * 40960u + w * 10240u : FAR; pin_s(nso); }
      if constexpr (kb >= 5 && kb < 10 && (q == 6 || q == 7)) xq[2 * (kb - 5) + q - 6] = LD16(rx, l16, nso + (2 * (kb - 5) + q - 6) * 1024u);
      if constexpr (kb >= 15 && (q == 6 || q == 7)) LDS16(xn + (2 * (kb - 15) + q - 6) * 1024) = xq[2 * (kb - 15) + q - 6];
      SB();
    });
    NOPS(7);
    *(LAS float*)(lds + st_w) = (c & 2) ? ((c & 1) ? as[3] : as[2]) : ((c & 1) ? as[1] : as[0]);
    WG_BARRIER();
#pragma unroll
    for (int tt = 0; tt < 2; ++tt) {
      f32x4 mu, rr;
      if (VO) {
        const f32x4 sq = *(LAS f32x4*)(lds + B_ST + tt * 64 + g * 16), sm = *(LAS f32x4*)(lds + B_ST + 128 + tt * 64 + g * 16);
        mu = sm * (1.0f / DM);
#pragma unroll
        for (int r = 0; r < 4; ++r) rr[r] = __builtin_amdgcn_rsqf(fmaxf(sq[r] * (1.0f / DM) - mu[r] * mu[r], 0.f) + 1e-5f);
      } else {
        const float sq = *(LAS float*)(lds + B_ST + tt * 64 + c * 4), m1 = *(LAS float*)(lds + B_ST + 128 + tt * 64 + c * 4) * (1.0f / DM);
        const float r1 = __builtin_amdgcn_rsqf(fmaxf(sq * (1.0f / DM) - m1 * m1, 0.f) + 1e-5f) * qs;
        mu = (f32x4){m1, m1, m1, m1}; rr = (f32x4){r1, r1, r1, r1};
      }
#pragma unroll
      for (int t = 0; t < 4; ++t) {
        const f32x4 y = (acc[2 * t + tt] - mu * cs[t]) * rr;
        __builtin_amdgcn_raw_buffer_store_b64((u32x2){pack2(y[0], y[1]), pack2(y[2], y[3])}, ro, o_vo[t], (unsigned)s * 2048u + tt * (VO ? 1024u : 256u), 0);
      }
    }
  }
  TMARK(3);
  __builtin_amdgcn_endpgm();
}

__global__ __launch_bounds__(256, 1) void rec_kernel(RecArgs a) {
  __shared__ __attribute__((aligned(16))) char lds_[S_END + DBG_LDS];
  static_assert(Y_END <= S_END, "LDS");
  LAS char* lds = (LAS char*)lds_;
  const int grp = blockIdx.x & 7, pl = blockIdx.x >> 3;
  AGPR_FENCE();
  TMARK(0);
  if (grp < 4 && pl < 28) {
    const int lane = threadIdx.x & 63, rep = 2 * grp + (pl & 1), il = pl >> 1;
    const unsigned* tab = (const unsigned*)a.cal;
    const unsigned x = tab[256 + (lane >> 3)] & 7u, bx = ((x & 1u) << 2) | (x >> 1);
    const unsigned* tp = tab + ((bx ^ lane) & 7u) * 8u + (lane >> 3);
    unsigned v = (min(tp[0], tp[128]) + min(tp[64], tp[192])) & 0x1ffffu;
    v += __shfl_xor(v, 8); v += __shfl_xor(v, 16); v += __shfl_xor(v, 32);
    v = v * 8u + (lane & 7);
    v = min(v, (unsigned)__shfl_xor(v, 1)); v = min(v, (unsigned)__shfl_xor(v, 2)); v = min(v, (unsigned)__shfl_xor(v, 4));
    const int pv = (bx ^ v ^ (unsigned)a.layer) & 7u;
    const unsigned pos4 = __builtin_amdgcn_readlane(pv, 0) | __builtin_amdgcn_readlane(pv, 8) << 3 | __builtin_amdgcn_readlane(pv, 16) << 6 | __builtin_amdgcn_readlane(pv, 24) << 9;
    const unsigned posP = POS_OF(grp), posM = posP ^ 4u;
    const int w = __builtin_amdgcn_readfirstlane(threadIdx.x >> 6);
    if (il < 4) {
      if (w < 2) score_role<0>(a, lds, rep, il, posM, posP, pos4); else if (w == 2) score_role<1>(a, lds, rep, il, posM, posP, pos4); else score_role<2>(a, lds, rep, il, posM, posP, pos4);
    } else {
      if (w == 0) out_role<0>(a, lds, rep, il - 4, posM, posP, pos4); else if (w == 3) out_role<2>(a, lds, rep, il - 4, posM, posP, pos4); else out_role<1>(a, lds, rep, il - 4, posM, posP, pos4);
    }
    return;
  }
  if (grp >= 4 && pl < 30) {
    const int hq = grp - 4, jq = pl * 4 + hq, bq = jq - 48;
    if (pl < 12) proj_role(a, lds, jq);
    else if (pl < 28) vwo_role(a, lds, hq * 8 + ((pl - 12) >> 1), pl & 1);
    AGPR_FENCE();
    if (pl >= 28 || (unsigned)bq < 8u * B_SETS) {
      const int s0 = pl >= 28 ? 0 : B_CUT + (bq >> 3), s1 = pl >= 28 ? B_CUT : NSEG, ds = pl >= 28 ? 1 : B_SETS;
      if ((jq & 7) < 5) bproj_role<false>(a, lds, jq & 7, s0, s1, ds); else bproj_role<true>(a, lds, jq & 7, s0, s1, ds);
    }
  }
  AGPR_FENCE();
  token_role(a, lds, grp < 4);
}

#define NKS 2
constexpr int KQ = S_ / NKS;
#define OUT_IDX(t, n) ((size_t)((t) >> 4) * 10240 + ((n) >> 3) * 128 + ((t) & 15) * 8 + ((n) & 7))
DEV f32x16 qk32(u32x4 k, u32x4 q, f32x16 c) { return __builtin_amdgcn_mfma_f32_32x32x16_bf16(__builtin_bit_cast(bf16x8, k), __builtin_bit_cast(bf16x8, q), c, 0, 0, 0); }
template <int H> DEV f32x16 pv32(u32x4 v, f32x16 p, f32x16 c) {
  typedef __attribute__((ext_vector_type(8))) float f32x8;
  const f32x8 h = __builtin_shufflevector(p, p, 8 * H, 8 * H + 1, 8 * H + 2, 8 * H + 3, 8 * H + 4, 8 * H + 5, 8 * H + 6, 8 * H + 7);
  return __builtin_amdgcn_smfmac_f32_32x32x32_bf16(__builtin_bit_cast(hbf16x8, v), __builtin_bit_cast(hbf16x16, h), c, 0xDDDD, 0, 0);
}
DEV float max16(f32x16 s) {
  float mx = fmx(fmx(s[0], s[1]), s[2]);
#pragma unroll
  for (int i = 3; i < 15; i += 2) mx = fmx(fmx(mx, s[i]), s[i + 1]);
  mx = fmx(mx, s[15]);
  auto rr = __builtin_amdgcn_permlane32_swap(f2u(mx), f2u(mx), false, false);
  return fmx(u2f(rr[0]), u2f(rr[1]));
}
__global__ __launch_bounds__(256 * NKS, NKS) void attb_kernel(const u16* __restrict__ Qp, const u16* __restrict__ Kp, const u16* __restrict__ VbT,
                                                       u16* __restrict__ O) {
  __shared__ float mrg_[4][NKS][2][13][64];
  const int lane = threadIdx.x & 63, wv = __builtin_amdgcn_readfirstlane(threadIdx.x >> 6), kh = wv >> 2, r = lane & 31, hh = lane >> 5;
  float (*mrg)[2][13][64] = mrg_[wv & 3];
  const int hd = (blockIdx.x & 7) * 4 + ((blockIdx.x >> 3) & 3), qb = blockIdx.x >> 5;
  const int q0 = qb * 256 + (wv & 3) * 64;
  const __amdgpu_buffer_rsrc_t rk = RSRC(Kp, BH * S_ * 64), rv = RSRC(VbT, BH * S_ * 64);
  unsigned l16 = lane * 16;
  const unsigned so0 = (unsigned)(hd * S_ + kh * KQ) * 64u;
  u32x4 kf[4][2], vf[4][2], qf[2][2];
  kf[0][0] = LDP16(rk, l16, so0); kf[0][1] = LDP16(rk, l16 + 1024, so0);
#pragma unroll
  for (int qt = 0; qt < 2; ++qt)
#pragma unroll
    for (int ks = 0; ks < 2; ++ks) qf[qt][ks] = *(const u32x4*)(Qp + ((size_t)hd * S_ + q0 + qt * 32) * 32 + ks * 512 + lane * 8);
#pragma unroll
  for (int u = 0; u < 3; ++u) {
    vf[u][0] = LDP16(rv, l16, so0 + u * 2048u); vf[u][1] = LDP16(rv, l16 + 1024, so0 + u * 2048u);
    kf[u + 1][0] = LDP16(rk, l16, so0 + (u + 1) * 2048u); kf[u + 1][1] = LDP16(rk, l16 + 1024, so0 + (u + 1) * 2048u);
  }
  f32x16 Z;
#pragma unroll
  for (int i = 0; i < 16; ++i) Z[i] = 0.f;
  f32x16 o[2] = {Z, Z}, sa[2];
  float m[2];
#pragma unroll
  for (int qt = 0; qt < 2; ++qt) {
    const float mn = max16(qk32(kf[0][1], qf[qt][1], qk32(kf[0][0], qf[qt][0], Z))), hi = rbf(mn), lo = rbf(mn - hi);
    m[qt] = hi + lo;
    if (hh == 0) qf[qt][1][2] = pack2(-hi, -lo);
  }
  sa[0] = qk32(kf[0][1], qf[0][1], qk32(kf[0][0], qf[0][0], Z));
  sa[1] = Z; vf[3][1] = (u32x4){0u, 0u, 0u, 0u};
  {
    unsigned so = so0 + 6144u;
    vf[3][0] = vf[3][1];
    asm volatile("s_nop 11\n1:\n\t"
      "s_nop 1\n\t" "v_smfmac_f32_32x32x32_bf16 v[24:39], v[148:151], v[64:71], %[ix]\n\t"
      "buffer_load_dwordx4 v[144:147], %[l16], %[rv], %[so] offen offset:0\n\t"
      "v_exp_f32 v40, v40\n\t" "v_exp_f32 v41, v41\n\t" "v_exp_f32 v42, v42\n\t" "v_exp_f32 v43, v43\n\t"
      "v_mfma_f32_32x32x16_bf16 v[56:71], v[88:91], v[80:83], 0\n\t"
      "buffer_load_dwordx4 v[148:151], %[l16], %[rv], %[so] offen offset:1024\n\t"
      "v_exp_f32 v44, v44\n\t" "v_exp_f32 v45, v45\n\t" "v_exp_f32 v46, v46\n\t" "v_exp_f32 v47, v47\n\t"
      "v_mfma_f32_32x32x16_bf16 v[56:71], v[92:95], v[84:87], v[56:71]\n\t"
      "buffer_load_dwordx4 v[88:91], %[l16], %[rk], %[so] offen offset:2048\n\t"
      "v_exp_f32 v48, v48\n\t" "v_exp_f32 v49, v49\n\t" "v_exp_f32 v50, v50\n\t" "v_exp_f32 v51, v51\n\t"
      "s_waitcnt vmcnt(13)\n\t" "v_smfmac_f32_32x32x32_bf16 v[8:23], v[96:99], v[40:47], %[ix]\n\t"
      "buffer_load_dwordx4 v[92:95], %[l16], %[rk], %[so] offen offset:3072\n\t"
      "v_exp_f32 v52, v52\n\t" "v_exp_f32 v53, v53\n\t" "v_exp_f32 v54, v54\n\t" "v_exp_f32 v55, v55\n\t"
      "s_add_u32 %[so], %[so], 0x800\n\t" "s_waitcnt vmcnt(12)\n\t" "v_smfmac_f32_32x32x32_bf16 v[8:23], v[100:103], v[48:55], %[ix]\n\t"
      "v_exp_f32 v56, v56\n\t" "v_exp_f32 v57, v57\n\t" "v_exp_f32 v58, v58\n\t" "v_exp_f32 v59, v59\n\t"
      "v_mfma_f32_32x32x16_bf16 v[40:55], v[104:107], v[72:75], 0\n\t"
      "v_exp_f32 v60, v60\n\t" "v_exp_f32 v61, v61\n\t" "v_exp_f32 v62, v62\n\t" "v_exp_f32 v63, v63\n\t"
      "v_mfma_f32_32x32x16_bf16 v[40:55], v[108:111], v[76:79], v[40:55]\n\t"
      "v_exp_f32 v64, v64\n\t" "v_exp_f32 v65, v65\n\t" "v_exp_f32 v66, v66\n\t" "v_exp_f32 v67, v67\n\t"
      "v_smfmac_f32_32x32x32_bf16 v[24:39], v[96:99], v[56:63], %[ix]\n\t"
      "v_exp_f32 v68, v68\n\t" "v_exp_f32 v69, v69\n\t" "v_exp_f32 v70, v70\n\t" "v_exp_f32 v71, v71\n\t"
      "s_nop 1\n\t" "v_smfmac_f32_32x32x32_bf16 v[24:39], v[100:103], v[64:71], %[ix]\n\t"
      "buffer_load_dwordx4 v[96:99], %[l16], %[rv], %[so] offen offset:0\n\t"
      "v_exp_f32 v40, v40\n\t" "v_exp_f32 v41, v41\n\t" "v_exp_f32 v42, v42\n\t" "v_exp_f32 v43, v43\n\t"
      "v_mfma_f32_32x32x16_bf16 v[56:71], v[104:107], v[80:83], 0\n\t"
      "buffer_load_dwordx4 v[100:103], %[l16], %[rv], %[so] offen offset:1024\n\t"
      "v_exp_f32 v44, v44\n\t" "v_exp_f32 v45, v45\n\t" "v_exp_f32 v46, v46\n\t" "v_exp_f32 v47, v47\n\t"
      "v_mfma_f32_32x32x16_bf16 v[56:71], v[108:111], v[84:87], v[56:71]\n\t"
      "buffer_load_dwordx4 v[104:107], %[l16], %[rk], %[so] offen offset:2048\n\t"
      "v_exp_f32 v48, v48\n\t" "v_exp_f32 v49, v49\n\t" "v_exp_f32 v50, v50\n\t" "v_exp_f32 v51, v51\n\t"
      "s_waitcnt vmcnt(13)\n\t" "v_smfmac_f32_32x32x32_bf16 v[8:23], v[112:115], v[40:47], %[ix]\n\t"
      "buffer_load_dwordx4 v[108:111], %[l16], %[rk], %[so] offen offset:3072\n\t"
      "v_exp_f32 v52, v52\n\t" "v_exp_f32 v53, v53\n\t" "v_exp_f32 v54, v54\n\t" "v_exp_f32 v55, v55\n\t"
      "s_add_u32 %[so], %[so], 0x800\n\t" "s_waitcnt vmcnt(12)\n\t" "v_smfmac_f32_32x32x32_bf16 v[8:23], v[116:119], v[48:55], %[ix]\n\t"
      "v_exp_f32 v56, v56\n\t" "v_exp_f32 v57, v57\n\t" "v_exp_f32 v58, v58\n\t" "v_exp_f32 v59, v59\n\t"
      "v_mfma_f32_32x32x16_bf16 v[40:55], v[120:123], v[72:75], 0\n\t"
      "v_exp_f32 v60, v60\n\t" "v_exp_f32 v61, v61\n\t" "v_exp_f32 v62, v62\n\t" "v_exp_f32 v63, v63\n\t"
      "v_mfma_f32_32x32x16_bf16 v[40:55], v[124:127], v[76:79], v[40:55]\n\t"
      "v_exp_f32 v64, v64\n\t" "v_exp_f32 v65, v65\n\t" "v_exp_f32 v66, v66\n\t" "v_exp_f32 v67, v67\n\t"
      "v_smfmac_f32_32x32x32_bf16 v[24:39], v[112:115], v[56:63], %[ix]\n\t"
      "v_exp_f32 v68, v68\n\t" "v_exp_f32 v69, v69\n\t" "v_exp_f32 v70, v70\n\t" "v_exp_f32 v71, v71\n\t"
      "s_nop 1\n\t" "v_smfmac_f32_32x32x32_bf16 v[24:39], v[116:119], v[64:71], %[ix]\n\t"
      "buffer_load_dwordx4 v[112:115], %[l16], %[rv], %[so] offen offset:0\n\t"
      "v_exp_f32 v40, v40\n\t" "v_exp_f32 v41, v41\n\t" "v_exp_f32 v42, v42\n\t" "v_exp_f32 v43, v43\n\t"
      "v_mfma_f32_32x32x16_bf16 v[56:71], v[120:123], v[80:83], 0\n\t"
      "buffer_load_dwordx4 v[116:119], %[l16], %[rv], %[so] offen offset:1024\n\t"
      "v_exp_f32 v44, v44\n\t" "v_exp_f32 v45, v45\n\t" "v_exp_f32 v46, v46\n\t" "v_exp_f32 v47, v47\n\t"
      "v_mfma_f32_32x32x16_bf16 v[56:71], v[124:127], v[84:87], v[56:71]\n\t"
      "buffer_load_dwordx4 v[120:123], %[l16], %[rk], %[so] offen offset:2048\n\t"
      "v_exp_f32 v48, v48\n\t" "v_exp_f32 v49, v49\n\t" "v_exp_f32 v50, v50\n\t" "v_exp_f32 v51, v51\n\t"
      "s_waitcnt vmcnt(13)\n\t" "v_smfmac_f32_32x32x32_bf16 v[8:23], v[128:131], v[40:47], %[ix]\n\t"
      "buffer_load_dwordx4 v[124:127], %[l16], %[rk], %[so] offen offset:3072\n\t"
      "v_exp_f32 v52, v52\n\t" "v_exp_f32 v53, v53\n\t" "v_exp_f32 v54, v54\n\t" "v_exp_f32 v55, v55\n\t"
      "s_add_u32 %[so], %[so], 0x800\n\t" "s_waitcnt vmcnt(12)\n\t" "v_smfmac_f32_32x32x32_bf16 v[8:23], v[132:135], v[48:55], %[ix]\n\t"
      "v_exp_f32 v56, v56\n\t" "v_exp_f32 v57, v57\n\t" "v_exp_f32 v58, v58\n\t" "v_exp_f32 v59, v59\n\t"
      "v_mfma_f32_32x32x16_bf16 v[40:55], v[136:139], v[72:75], 0\n\t"
      "v_exp_f32 v60, v60\n\t" "v_exp_f32 v61, v61\n\t" "v_exp_f32 v62, v62\n\t" "v_exp_f32 v63, v63\n\t"
      "v_mfma_f32_32x32x16_bf16 v[40:55], v[140:143], v[76:79], v[40:55]\n\t"
      "v_exp_f32 v64, v64\n\t" "v_exp_f32 v65, v65\n\t" "v_exp_f32 v66, v66\n\t" "v_exp_f32 v67, v67\n\t"
      "v_smfmac_f32_32x32x32_bf16 v[24:39], v[128:131], v[56:63], %[ix]\n\t"
      "v_exp_f32 v68, v68\n\t" "v_exp_f32 v69, v69\n\t" "v_exp_f32 v70, v70\n\t" "v_exp_f32 v71, v71\n\t"
      "s_nop 1\n\t" "v_smfmac_f32_32x32x32_bf16 v[24:39], v[132:135], v[64:71], %[ix]\n\t"
      "buffer_load_dwordx4 v[128:131], %[l16], %[rv], %[so] offen offset:0\n\t"
      "v_exp_f32 v40, v40\n\t" "v_exp_f32 v41, v41\n\t" "v_exp_f32 v42, v42\n\t" "v_exp_f32 v43, v43\n\t"
      "v_mfma_f32_32x32x16_bf16 v[56:71], v[136:139], v[80:83], 0\n\t"
      "buffer_load_dwordx4 v[132:135], %[l16], %[rv], %[so] offen offset:1024\n\t"
      "v_exp_f32 v44, v44\n\t" "v_exp_f32 v45, v45\n\t" "v_exp_f32 v46, v46\n\t" "v_exp_f32 v47, v47\n\t"
      "v_mfma_f32_32x32x16_bf16 v[56:71], v[140:143], v[84:87], v[56:71]\n\t"
      "buffer_load_dwordx4 v[136:139], %[l16], %[rk], %[so] offen offset:2048\n\t"
      "v_exp_f32 v48, v48\n\t" "v_exp_f32 v49, v49\n\t" "v_exp_f32 v50, v50\n\t" "v_exp_f32 v51, v51\n\t"
      "s_waitcnt vmcnt(13)\n\t" "v_smfmac_f32_32x32x32_bf16 v[8:23], v[144:147], v[40:47], %[ix]\n\t"
      "buffer_load_dwordx4 v[140:143], %[l16], %[rk], %[so] offen offset:3072\n\t"
      "v_exp_f32 v52, v52\n\t" "v_exp_f32 v53, v53\n\t" "v_exp_f32 v54, v54\n\t" "v_exp_f32 v55, v55\n\t"
      "s_add_u32 %[so], %[so], 0x800\n\t" "s_waitcnt vmcnt(12)\n\t" "v_smfmac_f32_32x32x32_bf16 v[8:23], v[148:151], v[48:55], %[ix]\n\t"
      "v_exp_f32 v56, v56\n\t" "v_exp_f32 v57, v57\n\t" "v_exp_f32 v58, v58\n\t" "v_exp_f32 v59, v59\n\t"
      "v_mfma_f32_32x32x16_bf16 v[40:55], v[88:91], v[72:75], 0\n\t"
      "v_exp_f32 v60, v60\n\t" "v_exp_f32 v61, v61\n\t" "v_exp_f32 v62, v62\n\t" "v_exp_f32 v63, v63\n\t"
      "v_mfma_f32_32x32x16_bf16 v[40:55], v[92:95], v[76:79], v[40:55]\n\t"
      "v_exp_f32 v64, v64\n\t" "v_exp_f32 v65, v65\n\t" "v_exp_f32 v66, v66\n\t" "v_exp_f32 v67, v67\n\t"
      "v_smfmac_f32_32x32x32_bf16 v[24:39], v[144:147], v[56:63], %[ix]\n\t"
      "v_exp_f32 v68, v68\n\t" "v_exp_f32 v69, v69\n\t" "v_exp_f32 v70, v70\n\t" "v_exp_f32 v71, v71\n\t"
      "s_cmp_lt_u32 %[so], %[se]\n\ts_cbranch_scc1 1b\n\ts_waitcnt vmcnt(0)\n\t" "v_smfmac_f32_32x32x32_bf16 v[24:39], v[148:151], v[64:71], %[ix]\n\t" "s_nop 11"
      : "+{v[8:23]}"(o[0]), "+{v[24:39]}"(o[1]), "+{v[40:55]}"(sa[0]), "+{v[56:71]}"(sa[1]),
        "+{v[88:91]}"(kf[0][0]), "+{v[96:99]}"(vf[0][0]), "+{v[92:95]}"(kf[0][1]), "+{v[100:103]}"(vf[0][1]),
        "+{v[104:107]}"(kf[1][0]), "+{v[112:115]}"(vf[1][0]), "+{v[108:111]}"(kf[1][1]), "+{v[116:119]}"(vf[1][1]),
        "+{v[120:123]}"(kf[2][0]), "+{v[128:131]}"(vf[2][0]), "+{v[124:127]}"(kf[2][1]), "+{v[132:135]}"(vf[2][1]),
        "+{v[136:139]}"(kf[3][0]), "+{v[144:147]}"(vf[3][0]), "+{v[140:143]}"(kf[3][1]), "+{v[148:151]}"(vf[3][1]), [so] "+s"(so)
      : "{v[72:75]}"(qf[0][0]), "{v[76:79]}"(qf[0][1]), "{v[80:83]}"(qf[1][0]), "{v[84:87]}"(qf[1][1]), [l16] "v"(l16), [rk] "s"(rk), [rv] "s"(rv), [se] "s"(so0 + KQ * 64u + 6144u), [ix] "v"(0xDDDD) : "scc", "memory");
  }
  float big = 0.f;
#pragma unroll
  for (int i = 0; i < 12; ++i) big = fmx(fmx(big, __builtin_fabsf(o[0][i])), __builtin_fabsf(o[1][i]));
  if (__builtin_expect(__any(!(big < 1.0e37f) || (hh == 1 && !(o[0][8] < 9.0e18f && o[1][8] < 9.0e18f))), 0)) {
#pragma unroll
    for (int qt = 0; qt < 2; ++qt) {
      o[qt] = Z;
#pragma unroll 1
      for (unsigned so = so0; so < so0 + KQ * 64u; so += 2048u) {
        f32x16 x = qk32(LDP16(rk, l16 + 1024, so), qf[qt][1], qk32(LDP16(rk, l16, so), qf[qt][0], Z));
        const float mn = m[qt] + fmx(max16(x), 0.f), hi = rbf(mn), lo = rbf(mn - hi), me = hi + lo;
        const float delta = me - m[qt], alpha = __builtin_amdgcn_exp2f(-delta);
        m[qt] = me;
        if (hh == 0) qf[qt][1][2] = pack2(-hi, -lo);
#pragma unroll
        for (int i = 0; i < 16; ++i) { x[i] = __builtin_amdgcn_exp2f(x[i] - delta); o[qt][i] *= alpha; }
        o[qt] = pv32<1>(LDP16(rv, l16 + 1024, so), x, pv32<0>(LDP16(rv, l16, so), x, o[qt]));
      }
    }
  }
#pragma unroll
  for (int qt = 0; qt < 2; ++qt)
    if (kh != qt) {
#pragma unroll
      for (int i = 0; i < 12; ++i) mrg[kh][qt][i][lane] = o[qt][i];
      mrg[kh][qt][12][lane] = m[qt];
    }
  __syncthreads();
  if (kh < 2) {
    float mo = kh ? m[1] : m[0], mk[NKS - 1], mn = mo, v[12];
#pragma unroll
    for (int k = 0; k < NKS - 1; ++k) { mk[k] = mrg[k + (k >= kh)][kh][12][lane]; mn = fmaxf(mn, mk[k]); }
    const float a0 = __builtin_amdgcn_exp2f(mo - mn);
#pragma unroll
    for (int i = 0; i < 12; ++i) v[i] = (kh ? o[1][i] : o[0][i]) * a0;
#pragma unroll
    for (int k = 0; k < NKS - 1; ++k) {
      const float ak = __builtin_amdgcn_exp2f(mk[k] - mn);
#pragma unroll
      for (int i = 0; i < 12; ++i) v[i] += mrg[k + (k >= kh)][kh][i][lane] * ak;
    }
    auto lr = __builtin_amdgcn_permlane32_swap(f2u(v[8]), f2u(v[8]), false, false);
    const float inv = __builtin_amdgcn_rcpf(u2f(lr[1]));
    const int t = q0 + kh * 32 + r, n = hd * BD + 4 * hh;
    *(u32x2*)(O + OUT_IDX(t, n)) = (u32x2){pack2(v[0] * inv, v[1] * inv), pack2(v[2] * inv, v[3] * inv)};
    *(u32x2*)(O + OUT_IDX(t, n + 8)) = (u32x2){pack2(v[4] * inv, v[5] * inv), pack2(v[6] * inv, v[7] * inv)};
    if (hh == 0) *(u32x2*)(O + OUT_IDX(t, n + 16)) = (u32x2){pack2(v[8] * inv, v[9] * inv), pack2(v[10] * inv, v[11] * inv)};
  }
}

#define ATTB_LAUNCH(q, k, v, o, st) attb_kernel<<<BH * 8, 256 * NKS, 0, st>>>(q, k, v, o)
#define OP_SLP 6
struct OutArgs { const u16* of; const u16* wo; const u16* xr; u16* xb; u16* xf; u16* out; char* ex; };
constexpr int P_LD = 164;
__global__ __launch_bounds__(256, 1) void oproj_kernel(OutArgs a) {
  __shared__ __attribute__((aligned(16))) float part[4 * 32 * P_LD];
  __shared__ f32x2 red[8][32];
  const int tid = threadIdx.x, lane = tid & 63, w = __builtin_amdgcn_readfirstlane(tid >> 6), c = lane & 15, g = lane >> 4;
  const int s = (blockIdx.x & 7) + 8 * (blockIdx.x >> 5), cg = (blockIdx.x >> 3) & 3;
  const __amdgpu_buffer_rsrc_t ra = RSRC(a.of, S_ * DM * 2), rw = RSRC(a.wo, DM * DM * 2);
  u32x4 af[5][2], wf[5][10], xo[3];
#pragma unroll
  for (int k = 0; k < 5; ++k) {
#pragma unroll
    for (int b = 0; b < 2; ++b) af[k][b] = LDP16(ra, lane * 16, (unsigned)((2 * s + b) * 20 + 5 * w + k) * 1024u);
#pragma unroll
    for (int t = 0; t < 10; ++t) wf[k][t] = LDP16(rw, lane * 16, (unsigned)((10 * cg + t) * 20 + 5 * w + k) * 1024u);
  }
  const int tok = tid & 31;
  const size_t fo = (size_t)(2 * s + (tok >> 4)) * 10240 + (20 * cg + (tid >> 5)) * 128 + (tok & 15) * 8;
#pragma unroll
  for (int r = 0; r < 3; ++r) if (r < 2 || tid < 128) xo[r] = *(const u32x4*)(a.xr + fo + r * 1024);
  SB();
  f32x4 acc[10][2];
#pragma unroll
  for (int k = 0; k < 5; ++k)
#pragma unroll
    for (int t = 0; t < 10; ++t)
#pragma unroll
      for (int b = 0; b < 2; ++b)
        acc[t][b] = mfma32(__builtin_bit_cast(bf16x8, wf[k][t]), __builtin_bit_cast(bf16x8, af[k][b]), k ? acc[t][b] : (f32x4){0.f, 0.f, 0.f, 0.f});
#pragma unroll
  for (int t = 0; t < 10; ++t)
#pragma unroll
    for (int b = 0; b < 2; ++b) *(f32x4*)&part[(w * 32 + 16 * b + c) * P_LD + 16 * t + 4 * g] = acc[t][b];
  __syncthreads();
  float v[3][8], sm = 0.f, sq = 0.f;
#pragma unroll
  for (int r = 0; r < 3; ++r)
    if (r < 2 || tid < 128) {
      const float* q = part + tok * P_LD + (r * 8 + (tid >> 5)) * 8;
      f32x4 s0 = *(const f32x4*)q, s1 = *(const f32x4*)(q + 4);
#pragma unroll
      for (int k = 1; k < 4; ++k) { s0 += *(const f32x4*)(q + k * 32 * P_LD); s1 += *(const f32x4*)(q + k * 32 * P_LD + 4); }
      u32x4 o;
#pragma unroll
      for (int k = 0; k < 2; ++k) {
        o[k] = pack2(bf2f(xo[r][k] & 0xffff) + rbf(s0[2 * k]), bf2f(xo[r][k] >> 16) + rbf(s0[2 * k + 1]));
        o[2 + k] = pack2(bf2f(xo[r][2 + k] & 0xffff) + rbf(s1[2 * k]), bf2f(xo[r][2 + k] >> 16) + rbf(s1[2 * k + 1]));
      }
      if (a.out) *(u32x4*)(a.out + (size_t)(32 * s + tok) * DM + 160 * cg + (r * 8 + (tid >> 5)) * 8) = o;
      else *(u32x4*)(a.xb + fo + r * 1024) = o;
#pragma unroll
      for (int k = 0; k < 4; ++k) { v[r][2 * k] = bf2f(o[k] & 0xffff); v[r][2 * k + 1] = u2f(o[k] & 0xffff0000u); }
#pragma unroll
      for (int k = 0; k < 8; ++k) { sm += v[r][k]; sq += v[r][k] * v[r][k]; }
    }
  if (a.out) return;
  red[tid >> 5][tok] = (f32x2){sm, sq};
  __syncthreads();
  if (w == 0) {
    const __amdgpu_buffer_rsrc_t re = RSRC(a.ex, 64 * 4 * 256);
    f32x2 p = red[4 * (lane >> 5)][tok];
#pragma unroll
    for (int k = 1; k < 4; ++k) p += red[4 * (lane >> 5) + k][tok];
    p[0] += __shfl_xor(p[0], 32); p[1] += __shfl_xor(p[1], 32);
    const u32x2 mine = {min(f2u(p[0]), SENT - 1), min(f2u(p[1]), SENT - 1)};
    __builtin_amdgcn_raw_buffer_store_b64(mine, re, lane < 32 ? (unsigned)(cg * 256 + tok * 8) : OOB, (unsigned)s * 1024u, 16);
    u32x2 x[2];
#pragma unroll
    for (int k = 0; k < 2; ++k) x[k] = 2 * k + (lane >> 5) == cg ? mine : (u32x2){SENT, SENT};
    __builtin_amdgcn_s_sleep(OP_SLP);
    for (unsigned spins = 0; spins < SPIN_CAP; ++spins) {
      u32x2 y[2]; bool nd[2];
#pragma unroll
      for (int k = 0; k < 2; ++k) { nd[k] = max(x[k][0], x[k][1]) == SENT; y[k] = LD8(re, nd[k] ? (unsigned)(k * 512 + lane * 8) : OOB, (unsigned)s * 1024u); }
#pragma unroll
      for (int k = 0; k < 2; ++k) if (nd[k]) x[k] = y[k];
      if (!__any(mx3(x[0][0], x[0][1], max(x[1][0], x[1][1])) == SENT)) break;
      __builtin_amdgcn_s_sleep(2);
      asm volatile("" ::: "memory");
    }
    float ts = u2f(x[0][0]) + u2f(x[1][0]), tq = u2f(x[0][1]) + u2f(x[1][1]);
    ts += __shfl_xor(ts, 32); tq += __shfl_xor(tq, 32);
    const float mean = ts * (1.0f / DM), var = fmaxf(tq * (1.0f / DM) - mean * mean, 0.f);
    if (lane < 32) red[0][tok] = (f32x2){rbf(mean), 1.0f / rbf(sqrtf(rbf(rbf(var) + 1e-5f)))};
  }
  __syncthreads();
  const f32x2 st = red[0][tok];
#pragma unroll
  for (int r = 0; r < 3; ++r)
    if (r < 2 || tid < 128) {
      u32x4 o;
#pragma unroll
      for (int k = 0; k < 4; ++k) o[k] = pack2(rbf(v[r][2 * k] - st[0]) * st[1], rbf(v[r][2 * k + 1] - st[0]) * st[1]);
      *(u32x4*)(a.xf + fo + r * 1024) = o;
    }
}

#define P_TAIL 410
#define P_D0 95
#define P_G2C 700
#define P_G2T 225
#define P_DUR0 900
#define P_TOL 50
#define P_SLOW 880
extern "C" void kernel_launch(void* const* d_in, const int* in_sizes, int n_in, void* d_out, int out_size, void* d_ws,
                              size_t ws_size, hipStream_t stream) {
  const u16* A_x = (const u16*)d_in[0];
  const u16* A_mem0 = (const u16*)d_in[1];
  const u16* A_Wq = (const u16*)d_in[2];
  const u16* A_Wk = (const u16*)d_in[3];
  const u16* A_Wv = (const u16*)d_in[4];
  const u16* A_Wo = (const u16*)d_in[5];
  const u16* B_wqkv = (const u16*)d_in[8];
  const u16* B_wo = (const u16*)d_in[9];
  char* ws = (char*)(((uintptr_t)d_ws + ((size_t)2 << 20) - 1) & ~(uintptr_t)(((size_t)2 << 20) - 1));
  u16* WF = (u16*)(ws + OFF_WF); u16* WOF = (u16*)(ws + OFF_WOF); u16* UF = (u16*)(ws + OFF_UF);
  u16* WqkvBT = (u16*)(ws + OFF_WQKVBT); u16* WoBT = (u16*)(ws + OFF_WOBT);
  u16* xf = (u16*)d_out; u16* xn = (u16*)(ws + OFF_XN); u16* xb = xn;
  u16* Qb = (u16*)(ws + OFF_QB); u16* Kb = (u16*)(ws + OFF_KB); u16* VbT = (u16*)(ws + OFF_VBT); u16* Ob = (u16*)(ws + OFF_OB);
  {
    ProArgs pa{A_x, A_mem0, A_Wq, A_Wk, A_Wv, A_Wo, B_wqkv, B_wo, WF, WOF, UF, WqkvBT, WoBT, xf, Qb, ws};
    prologue_kernel<<<256, 704, 0, stream>>>(pa);
  }
  const u16* xcur = A_x;
  for (int layer = 0; layer < 2; ++layer) {
    {
      RecArgs r{WF, WOF, UF, xf, ws + OFF_QK, ws + OFF_Y, ws + OFF_Z, ws + OFF_PT, ws + (size_t)layer * SYNC_LAYER, ws + OFS_CAL + ROW, xcur, xn, WqkvBT, Qb, nullptr,
                layer, P_TAIL, P_D0, P_G2C, P_G2T, P_DUR0, P_TOL, P_SLOW};
      rec_kernel<<<256, 256, 0, stream>>>(r);
    }
    ATTB_LAUNCH(Qb, Kb, VbT, Ob, stream);
    {
      OutArgs o{Ob, WoBT, xn, xb, xf, layer ? (u16*)d_out : nullptr, ws + SYNC_LAYER + OFS_CAL};
      oproj_kernel<<<256, 256, 0, stream>>>(o);
      xcur = xb;
    }
  }
}
```
